```python
import torch

def _softmax(x, axis=-1):
    return torch.softmax(x, dim=axis)
def _gelu(x):
    return torch.nn.functional.gelu(x, approximate="tanh")
def _silu(x):
    return torch.nn.functional.silu(x)
def _sigmoid(x):
    return torch.sigmoid(x)
def _layernorm(x, axis=-1, eps=1e-5):
    m = x.mean(dim=axis, keepdim=True)
    v = x.var(dim=axis, keepdim=True, unbiased=False)
    return (x - m) / torch.sqrt(v + eps)
def _scatter_add(tgt, idx, src, axis=0):
    return tgt.clone().index_add_(axis, idx, src)
def _segment_sum(src, seg, n):
    out = torch.zeros((n,) + src.shape[1:], dtype=src.dtype)
    return out.index_add_(0, seg, src)
def _pad(x, pw):
    flat = tuple(v for pair in reversed(pw) for v in pair)
    return torch.nn.functional.pad(x, flat)

kernel_name = 'interleave_layers__grouped_kv_mqa__glu_gated_swiglu__prompt'

def setup_inputs():

    _w = (lambda a: (lambda t: t.to(torch.bfloat16) if t.is_floating_point() else t)(torch.as_tensor(a)))
    def _A_setup():
        B, S, D, H = 1, 2048, 512, 32
        Dh = D // H
        return {
            "x": _w((lambda *s: torch.randn(*s, dtype=torch.bfloat16) * 0.05)(B, S, D)),
            "wq": _w((lambda *s: torch.randn(*s, dtype=torch.bfloat16) * 0.05)(D, H * Dh)),
            "wk": _w((lambda *s: torch.randn(*s, dtype=torch.bfloat16) * 0.05)(D, Dh)),
            "wv": _w((lambda *s: torch.randn(*s, dtype=torch.bfloat16) * 0.05)(D, Dh)),
            "wo": _w((lambda *s: torch.randn(*s, dtype=torch.bfloat16) * 0.05)(H * Dh, D)),
        }
    _A_in = _A_setup()

    def _B_setup():
        B, S, D, F = 1, 2048, 512, 1792
        return {
            "x": _w((lambda *s: torch.randn(*s, dtype=torch.bfloat16) * 0.05)(B, S, D)),
            "w1": _w((lambda *s: torch.randn(*s, dtype=torch.bfloat16) * 0.4)(D, F) * (2.0 / D) ** 0.5),
            "w3": _w((lambda *s: torch.randn(*s, dtype=torch.bfloat16) * 0.4)(D, F) * (2.0 / D) ** 0.5),
            "w2": _w((lambda *s: torch.randn(*s, dtype=torch.bfloat16) * 0.4)(F, D) * (2.0 / F) ** 0.5),
        }
    _B_in = _B_setup()

    return {
        "A_x": _A_in["x"],
        "A_wq": _A_in["wq"],
        "A_wk": _A_in["wk"],
        "A_wv": _A_in["wv"],
        "A_wo": _A_in["wo"],
        "B_x": _B_in["x"],
        "B_w1": _B_in["w1"],
        "B_w3": _B_in["w3"],
        "B_w2": _B_in["w2"],
    }

def reference(A_x, A_wq, A_wk, A_wv, A_wo, B_x, B_w1, B_w3, B_w2):
    def _A_ref(x, wq, wk, wv, wo):
        B, S, D = x.shape
        H = 32
        Dh = D // H
        q = (lambda x, *d: x.permute(*d))((x @ wq).reshape(B, S, H, Dh), 0, 2, 1, 3)
        k = x @ wk
        v = x @ wv
        scores = torch.einsum("bhqd,bkd->bhqk", q, k) * (Dh ** -0.5)
        p = _softmax(scores, axis=-1)
        o = torch.einsum("bhqk,bkd->bhqd", p, v)
        return (lambda x, *d: x.permute(*d))(o, 0, 2, 1, 3).reshape(B, S, H * Dh) @ wo

    def _B_ref(x, w1, w3, w2):
        gate = torch.einsum("bsd,df->bsf", x, w1)
        up = torch.einsum("bsd,df->bsf", x, w3)
        h = _silu(gate) * up
        out = torch.einsum("bsf,fd->bsd", h, w2)
        return out

    x = A_x
    x = x + _A_ref(_layernorm(x, axis=-1), A_wq, A_wk, A_wv, A_wo)
    x = x + _B_ref(_layernorm(x, axis=-1), B_w1, B_w3, B_w2)
    x = x + _A_ref(_layernorm(x, axis=-1), A_wq, A_wk, A_wv, A_wo)
    x = x + _B_ref(_layernorm(x, axis=-1), B_w1, B_w3, B_w2)
    return x
```

```cpp
#include <hip/hip_runtime.h>
#include <hip/hip_bf16.h>
#include <stdint.h>

typedef __attribute__((ext_vector_type(8))) short bf16x8;
typedef __attribute__((ext_vector_type(4))) short s16x4;
typedef __attribute__((ext_vector_type(16))) float f32x16;
typedef __attribute__((ext_vector_type(4))) float f32x4;
typedef __attribute__((ext_vector_type(2))) float f32x2;
typedef __attribute__((ext_vector_type(4))) unsigned u32x4;
typedef __attribute__((ext_vector_type(2))) unsigned u32x2;
typedef __attribute__((ext_vector_type(2))) __bf16 bf16x2_t;
#define LAS __attribute__((address_space(3)))
#define GAS __attribute__((address_space(1)))
typedef unsigned short u16;

constexpr int S = 2048, D = 512, F = 1792;
constexpr float QSCALE = 0.25f * 1.4426950408889634f;
constexpr float LN_EPS = 1e-5f;
constexpr float FAST_LIMIT = 40.0f;

constexpr size_t WS_WOT = 0;
constexpr size_t WS_W13 = WS_WOT + 512 * 1024;
constexpr size_t WS_W2T = WS_W13 + 112 * 32 * 1024;
constexpr size_t WS_Q   = WS_W2T + 16 * 112 * 1024;
constexpr size_t WS_KV  = WS_Q + 2048 * 1024;
constexpr size_t WS_KMX = WS_KV + 128 * 1024;
constexpr size_t WS_CS  = WS_KMX + 1024;
constexpr size_t WS_X1  = WS_CS + 4096;
constexpr size_t WS_XN1 = WS_X1 + 2048 * 1024;
constexpr size_t WS_H   = WS_XN1 + 2048 * 1024;
constexpr size_t WS_X2  = WS_H + 64 * 112 * 1024;
constexpr size_t WS_ST  = WS_X2 + 2048 * 1024;

#define MFMA32(a, b, c) __builtin_amdgcn_mfma_f32_32x32x16_bf16(a, b, c, 0, 0, 0)
#define MFMA16(a, b, c) __builtin_amdgcn_mfma_f32_16x16x32_bf16(a, b, c, 0, 0, 0)
#define SBAR() __builtin_amdgcn_sched_barrier(0)
#define PIN(x) asm volatile("" : "+v"(x))

__device__ __forceinline__ unsigned pk2(float lo, float hi) {
    bf16x2_t v = {(__bf16)lo, (__bf16)hi};
    return __builtin_bit_cast(unsigned, v);
}
__device__ __forceinline__ float bflo(unsigned u) { return __uint_as_float(u << 16); }
__device__ __forceinline__ float bfhi(unsigned u) { return __uint_as_float(u & 0xffff0000u); }
__device__ __forceinline__ float rbf(float x) { return bflo(pk2(x, x)); }
__device__ __forceinline__ void glds16(const void* g, unsigned lds_base) {
    unsigned sv;
    asm volatile("s_mov_b32 %0, m0\n\ts_mov_b32 m0, %2\n\ts_nop 0\n\tglobal_load_lds_dwordx4 %1, off\n\ts_mov_b32 m0, %0"
                 : "=&s"(sv) : "v"(g), "s"(lds_base) : "memory");
}
template <int CTRL> __device__ __forceinline__ float dppf(float x) {
    return __int_as_float(__builtin_amdgcn_update_dpp(0, __float_as_int(x), CTRL, 0xf, 0xf, true));
}
__device__ __forceinline__ float xor32(float x) {
    auto r = __builtin_amdgcn_permlane32_swap(__float_as_uint(x), __float_as_uint(x), false, false);
    return __uint_as_float((threadIdx.x & 32) ? r[0] : r[1]);
}
__device__ __forceinline__ float sum_x32(float x) {
    auto r = __builtin_amdgcn_permlane32_swap(__float_as_uint(x), __float_as_uint(x), false, false);
    return __uint_as_float(r[0]) + __uint_as_float(r[1]);
}
__device__ __forceinline__ float max_x32(float x) {
    auto r = __builtin_amdgcn_permlane32_swap(__float_as_uint(x), __float_as_uint(x), false, false);
    return fmaxf(__uint_as_float(r[0]), __uint_as_float(r[1]));
}
__device__ __forceinline__ float sum_x16(float x) {
    auto r = __builtin_amdgcn_permlane16_swap(__float_as_uint(x), __float_as_uint(x), false, false);
    return __uint_as_float(r[0]) + __uint_as_float(r[1]);
}
__device__ __forceinline__ float max_x16(float x) {
    auto r = __builtin_amdgcn_permlane16_swap(__float_as_uint(x), __float_as_uint(x), false, false);
    return fmaxf(__uint_as_float(r[0]), __uint_as_float(r[1]));
}
__device__ __forceinline__ float sum8(float x) { x += dppf<0xB1>(x); x += dppf<0x4E>(x); x += dppf<0x141>(x); return x; }
__device__ __forceinline__ float sum16(float x) { x = sum8(x); x += dppf<0x140>(x); return x; }
__device__ __forceinline__ float max16(float x) {
    x = fmaxf(x, dppf<0xB1>(x)); x = fmaxf(x, dppf<0x4E>(x)); x = fmaxf(x, dppf<0x141>(x)); x = fmaxf(x, dppf<0x140>(x)); return x; }
__device__ __forceinline__ float sum64(float x) { return sum_x32(sum_x16(sum16(x))); }
__device__ __forceinline__ float max64(float x) { return max_x32(max_x16(max16(x))); }
__device__ __forceinline__ int rho16(int f) { return (f & 3) + 4 * ((f >> 3) & 1) + 8 * ((f >> 2) & 1); }

__device__ __forceinline__ void transpose8(const u32x4 (&r)[8], u32x4 (&o)[8]) {
#pragma unroll
    for (int c = 0; c < 4; ++c) {
#pragma unroll
        for (int p = 0; p < 4; ++p) {
            o[2 * c][p]     = __builtin_amdgcn_perm(r[2 * p + 1][c], r[2 * p][c], 0x05040100u);
            o[2 * c + 1][p] = __builtin_amdgcn_perm(r[2 * p + 1][c], r[2 * p][c], 0x07060302u);
        }
    }
}
__device__ __forceinline__ void retile_task(int task, const u16* wo, const u16* w1, const u16* w3, const u16* w2, char* ws) {
    const u16* src; int ld, k0, n0, kind;
    if (task < 4096) { kind = 0; src = wo; ld = 512; k0 = (task >> 6) * 8; n0 = (task & 63) * 8; }
    else if (task < 4096 + 14336) { int t = task - 4096; kind = 1; src = w1; ld = F; k0 = (t / 224) * 8; n0 = (t % 224) * 8; }
    else if (task < 4096 + 2 * 14336) { int t = task - 4096 - 14336; kind = 2; src = w3; ld = F; k0 = (t / 224) * 8; n0 = (t % 224) * 8; }
    else { int t = task - 4096 - 2 * 14336; kind = 3; src = w2; ld = 512; k0 = (t >> 6) * 8; n0 = (t & 63) * 8; }
    u32x4 r[8], o[8];
#pragma unroll
    for (int i = 0; i < 8; ++i) r[i] = *(const u32x4*)(src + (size_t)(k0 + i) * ld + n0);
    transpose8(r, o);
#pragma unroll
    for (int i = 0; i < 8; ++i) {
        const int n = n0 + i; size_t off;
        if (kind == 0) {
            const int nb = n >> 6, nl = n & 63, sub = 2 * (nl >> 5) + ((nl >> 2) & 1), rho = 4 * ((nl >> 3) & 3) + (nl & 3);
            off = WS_WOT + (size_t)((nb * 16 + (k0 >> 5)) * 4 + sub) * 1024 + (rho + 16 * ((k0 >> 3) & 3)) * 16;
        } else if (kind == 3) {
            const int rho = 16 * ((n >> 4) & 1) + rho16(n & 15);
            off = WS_W2T + (size_t)((n >> 5) * 112 + (k0 >> 4)) * 1024 + (rho + 32 * ((k0 >> 3) & 1)) * 16;
        } else {
            const int rho = (kind == 2 ? 16 : 0) + rho16(n & 15);
            off = WS_W13 + (size_t)((n >> 4) * 32 + (k0 >> 4)) * 1024 + (rho + 32 * ((k0 >> 3) & 1)) * 16;
        }
        *(u32x4*)(ws + off) = o[i];
    }
}
constexpr int RETILE_TASKS = 4096 + 3 * 14336;
constexpr int RETILE_BLOCKS = RETILE_TASKS / 512;

constexpr int D_LDS_X = 0, D_LDS_W = 65536, D_LDS_WKV = 131072, D_LDS_ST = 147456, D_LDS_BYTES = 147456 + 512;

template <bool FIRST>
__global__ void __launch_bounds__(512, 2) k_qkv(const u16* __restrict__ xin, const u16* __restrict__ wq, const u16* __restrict__ wk,
                                                const u16* __restrict__ wv, const u16* __restrict__ wo, const u16* __restrict__ w1,
                                                const u16* __restrict__ w3, const u16* __restrict__ w2, char* __restrict__ ws) {
    extern __shared__ __attribute__((aligned(16))) char lds[];
    const int tid = threadIdx.x, lane = tid & 63, w = __builtin_amdgcn_readfirstlane(tid >> 6);
    if (FIRST && blockIdx.x >= 256) { retile_task((blockIdx.x - 256) * 512 + tid, wo, w1, w3, w2, ws); return; }
    const int b = blockIdx.x, tb = (b & 7) * 4 + ((b >> 3) & 3), cb = b >> 5;
    const unsigned lds0 = (unsigned)(uintptr_t)lds;
    const int t16 = lane & 15, g = lane >> 4;
#pragma unroll
    for (int i = 0; i < 8; ++i) {
        const int fr = w * 8 + i, ts = fr >> 4, ks = fr & 15;
        const void* src;
        if (FIRST) src = xin + (size_t)(tb * 64 + ts * 16 + t16) * D + ks * 32 + g * 8;
        else src = ws + WS_X2 + (size_t)(tb * 64 + fr) * 1024 + lane * 16;
        glds16(src, lds0 + D_LDS_X + fr * 1024);
        const int row = fr * 8 + (lane >> 3), ch = (lane & 7) ^ (2 * ((row >> 1) & 1) + 4 * ((row >> 3) & 1));
        glds16(wq + (size_t)row * D + cb * 64 + ch * 8, lds0 + D_LDS_W + fr * 1024);
    }
    const u16* wkv = (cb & 1) ? wv : wk;
#pragma unroll
    for (int i = 0; i < 2; ++i) glds16(wkv + (w * 2 + i) * 512 + lane * 8, lds0 + D_LDS_WKV + (w * 2 + i) * 1024);
    f32x2* st = (f32x2*)(lds + D_LDS_ST);
    if (FIRST) {
        u32x4 rw[8];
#pragma unroll
        for (int i = 0; i < 8; ++i) rw[i] = *(const u32x4*)(xin + (size_t)(tb * 64 + w * 8 + i) * D + lane * 8);
#pragma unroll
        for (int i = 0; i < 8; ++i) {
            float v[8];
#pragma unroll
            for (int j = 0; j < 4; ++j) { v[2 * j] = bflo(rw[i][j]); v[2 * j + 1] = bfhi(rw[i][j]); }
            float s = ((v[0] + v[1]) + (v[2] + v[3])) + ((v[4] + v[5]) + (v[6] + v[7]));
            const float mean = sum64(s) * (1.0f / 512.0f);
            float q = 0.f;
#pragma unroll
            for (int j = 0; j < 8; ++j) { const float d = v[j] - mean; q = fmaf(d, d, q); }
            const float var = sum64(q) * (1.0f / 512.0f);
            if (lane == 0) st[w * 8 + i] = f32x2{mean, __builtin_amdgcn_rsqf(var + LN_EPS)};
        }
    } else {
        const f32x4 p = *(const f32x4*)(ws + WS_ST + (size_t)(tb * 64 + w * 8 + (lane >> 3)) * 128 + (lane & 7) * 16);
        const float mean = sum8(p[0] + p[2]) * (1.0f / 16.0f);
        const float d0 = p[0] - mean, d1 = p[2] - mean;
        const float var = sum8(p[1] + p[3] + 32.0f * (d0 * d0 + d1 * d1)) * (1.0f / 512.0f);
        if ((lane & 7) == 0) st[w * 8 + (lane >> 3)] = f32x2{mean, __builtin_amdgcn_rsqf(var + LN_EPS)};
    }
    asm volatile("s_waitcnt vmcnt(0) lgkmcnt(0)\n\ts_barrier" ::: "memory");
    const int ts = w & 3, cp = w >> 2;
    const int q4 = t16 >> 2, p4 = lane & 3;
    const bf16x8 ones = {0x3F80, 0x3F80, 0x3F80, 0x3F80, 0x3F80, 0x3F80, 0x3F80, 0x3F80};
    f32x4 acc0 = {0, 0, 0, 0}, acc1 = {0, 0, 0, 0}, cs0 = {0, 0, 0, 0}, cs1 = {0, 0, 0, 0}, akv = {0, 0, 0, 0}, ckv = {0, 0, 0, 0};
    const LAS char* xb = (const LAS char*)lds + D_LDS_X + ts * 16384 + lane * 16;
    const int fsw = 2 * ((q4 >> 1) & 1) + 4 * (g & 1);
    const LAS char* wb0 = (const LAS char*)lds + D_LDS_W + (8 * g + q4) * 128 + (((4 * cp + (p4 >> 1)) ^ fsw) * 16) + 8 * (p4 & 1);
    const LAS char* wb1 = (const LAS char*)lds + D_LDS_W + (8 * g + q4) * 128 + (((4 * cp + 2 + (p4 >> 1)) ^ fsw) * 16) + 8 * (p4 & 1);
    const LAS char* wbk = (const LAS char*)lds + D_LDS_WKV + (8 * g + q4) * 32 + p4 * 8;
    const bool kvw = (w == (cb >> 1));
    typedef short v4i16_t __attribute__((ext_vector_type(4)));
#define TRR(p) __builtin_amdgcn_ds_read_tr16_b64_v4i16((LAS v4i16_t*)(p))
#define CAT(a, b) __builtin_shufflevector(a, b, 0, 1, 2, 3, 4, 5, 6, 7)
#pragma unroll
    for (int ks = 0; ks < 16; ++ks) {
        const bf16x8 xf = *(const LAS bf16x8*)(xb + ks * 1024);
        const bf16x8 a0 = CAT(TRR(wb0 + ks * 4096), TRR(wb0 + ks * 4096 + 512));
        const bf16x8 a1 = CAT(TRR(wb1 + ks * 4096), TRR(wb1 + ks * 4096 + 512));
        acc0 = MFMA16(a0, xf, acc0); acc1 = MFMA16(a1, xf, acc1);
        if (FIRST) { cs0 = MFMA16(a0, ones, cs0); cs1 = MFMA16(a1, ones, cs1); }
    }
    if (kvw) {
#pragma unroll
        for (int ks = 0; ks < 16; ++ks) {
            const bf16x8 xf = *(const LAS bf16x8*)(xb + ks * 1024);
            const bf16x8 a = CAT(TRR(wbk + ks * 1024), TRR(wbk + ks * 1024 + 128));
            if (cb & 1) { akv = MFMA16(xf, a, akv); if (FIRST) ckv = MFMA16(ones, a, ckv); }
            else        { akv = MFMA16(a, xf, akv); if (FIRST) ckv = MFMA16(a, ones, ckv); }
        }
    }
    float* csg = (float*)(ws + WS_CS);
    if (FIRST) {
        if (tb == 0 && ts == 0 && t16 == 0) {
            *(f32x4*)(csg + cb * 64 + cp * 32 + 4 * g) = cs0; *(f32x4*)(csg + cb * 64 + cp * 32 + 16 + 4 * g) = cs1;
        }
        if (tb == 0 && kvw && cb < 2) {
            if (cb == 0) { if (t16 == 0) *(f32x4*)(csg + 512 + 4 * g) = ckv; }
            else if (g == 0) csg[528 + t16] = ckv[0];
        }
    } else {
        cs0 = *(const f32x4*)(csg + cb * 64 + cp * 32 + 4 * g); cs1 = *(const f32x4*)(csg + cb * 64 + cp * 32 + 16 + 4 * g);
        if (kvw) { if (cb & 1) { const float c = csg[528 + t16]; ckv = f32x4{c, c, c, c}; } else ckv = *(const f32x4*)(csg + 512 + 4 * g); }
    }
    {
        const f32x2 ms = st[ts * 16 + t16];
        const float sc = ms[1] * QSCALE;
        const int tok = tb * 64 + ts * 16 + t16;
        char* qb = ws + WS_Q + (size_t)((tok >> 3) * 8 + cb) * 1024 + ((2 * cp) * 8 + (tok & 7) + 32 * (g >> 1)) * 16 + (g & 1) * 8;
        u32x2 o0 = {pk2(sc * (acc0[0] - ms[0] * cs0[0]), sc * (acc0[1] - ms[0] * cs0[1])), pk2(sc * (acc0[2] - ms[0] * cs0[2]), sc * (acc0[3] - ms[0] * cs0[3]))};
        u32x2 o1 = {pk2(sc * (acc1[0] - ms[0] * cs1[0]), sc * (acc1[1] - ms[0] * cs1[1])), pk2(sc * (acc1[2] - ms[0] * cs1[2]), sc * (acc1[3] - ms[0] * cs1[3]))};
        *(u32x2*)qb = o0; *(u32x2*)(qb + 128) = o1;
        if (kvw) {
            const int kt = (tb * 64 + ts * 16) >> 5;
            char* kvb = ws + WS_KV + kt * 2048;
            if (cb & 1) {
                float v[4];
#pragma unroll
                for (int r = 0; r < 4; ++r) { const f32x2 m2 = st[ts * 16 + 4 * g + r]; v[r] = m2[1] * (akv[r] - m2[0] * ckv[r]); }
                *(u32x2*)(kvb + 1024 + (ts & 1) * 512 + (g & 1) * 256 + rho16(t16) * 16 + (g >> 1) * 8) = u32x2{pk2(v[0], v[1]), pk2(v[2], v[3])};
            } else {
                const u32x2 kk = {pk2(ms[1] * (akv[0] - ms[0] * ckv[0]), ms[1] * (akv[1] - ms[0] * ckv[1])),
                                  pk2(ms[1] * (akv[2] - ms[0] * ckv[2]), ms[1] * (akv[3] - ms[0] * ckv[3]))};
                *(u32x2*)(kvb + ((g >> 1) * 32 + (tok & 31)) * 16 + (g & 1) * 8) = kk;
                float n2 = bflo(kk[0]) * bflo(kk[0]) + bfhi(kk[0]) * bfhi(kk[0]) + bflo(kk[1]) * bflo(kk[1]) + bfhi(kk[1]) * bfhi(kk[1]);
                n2 = max16(sum_x32(sum_x16(n2)));
                if (lane == 0) ((float*)(ws + WS_KMX))[tb * 4 + ts] = n2;
            }
        }
    }
}

constexpr int A_LDS_KV = 0, A_LDS_CONST = 131072, A_LDS_PART = A_LDS_CONST + 8192, A_LDS_O = A_LDS_PART + 4 * 2560,
              A_LDS_ST = A_LDS_O + 8192, A_LDS_BYTES = A_LDS_ST + 512;

template <bool FIRST>
__global__ void __launch_bounds__(512, 2) k_attn(const u16* __restrict__ xin, char* __restrict__ ws) {
    extern __shared__ __attribute__((aligned(16))) char lds[];
    const int tid = threadIdx.x, lane = tid & 63, w = __builtin_amdgcn_readfirstlane(tid >> 6);
    const int b = blockIdx.x, blk = (b & 7) * 32 + (b >> 3);
    const unsigned lds0 = (unsigned)(uintptr_t)lds;
    const int qw = w & 3, kh = w >> 2, c32 = lane & 31, hi = lane >> 5, t16 = lane & 15, g = lane >> 4;
    bf16x8 qf[2];
    qf[0] = *(const bf16x8*)(ws + WS_Q + (size_t)(blk * 8 + qw) * 1024 + lane * 16);
    qf[1] = *(const bf16x8*)(ws + WS_Q + (size_t)(blk * 8 + 4 + qw) * 1024 + lane * 16);
    const f32x2 kmx = *(const f32x2*)(ws + WS_KMX + lane * 8);
#pragma unroll
    for (int i = 0; i < 16; ++i) {
        const int q = i >> 2, j = i & 3;
        const int pc = (w * 4 + j);
        const int piece = (pc >> 4) * 64 + q * 16 + (pc & 15);
        glds16(ws + WS_KV + piece * 1024 + lane * 16, lds0 + A_LDS_KV + piece * 1024);
    }
    if (tid < 128) {
        const int slot = tid >> 4, r = tid & 15;
        const unsigned v = (r == 0 || r == 4) ? 0x3F803F80u : 0u;
        *(LAS u32x4*)((LAS char*)lds + A_LDS_CONST + (slot >> 1) * 2048 + (slot & 1) * 512 + r * 16) = u32x4{v, v, v, v};
    }
    const float kmax = __builtin_sqrtf(max64(fmaxf(kmx[0], kmx[1])));
    const LAS char* kptr0 = (const LAS char*)lds + A_LDS_KV + kh * 65536 + lane * 16;
    const bool vdat = (c32 < 16);
    const LAS char* vptr0 = vdat ? (const LAS char*)lds + A_LDS_KV + kh * 65536 + 1024 + hi * 256 + c32 * 16
                                 : (const LAS char*)lds + A_LDS_CONST + (c32 - 16) * 16;
    const int vstep = vdat ? 8192 : 0;
    f32x4 oacc[4] = {{0, 0, 0, 0}, {0, 0, 0, 0}, {0, 0, 0, 0}, {0, 0, 0, 0}};
    const char* wot = ws + WS_WOT + (size_t)w * 65536 + lane * 16;

#pragma unroll
    for (int rnd = 0; rnd < 2; ++rnd) {
        bf16x8 wof[32];
#pragma unroll
        for (int i = 0; i < 32; ++i) wof[i] = *(const bf16x8*)(wot + (size_t)(rnd * 32 + i) * 1024);
        const bf16x8 q = qf[rnd];
        float n2 = 0.f;
#pragma unroll
        for (int j = 0; j < 8; ++j) { const float v = __uint_as_float((unsigned)(u16)q[j] << 16); n2 = fmaf(v, v, n2); }
        const float bound = __builtin_sqrtf(sum_x32(n2)) * kmax * 1.01f + 1e-6f;
        const bool fast = __all(bound <= FAST_LIMIT);
        if (rnd == 0) asm volatile("s_waitcnt vmcnt(32) lgkmcnt(0)\n\ts_barrier" ::: "memory");
        f32x16 o;
#pragma unroll
        for (int r = 0; r < 16; ++r) o[r] = 0.f;
        float m;
        const LAS char* kp = kptr0; const LAS char* vp = vptr0;
        if (fast) {
            m = bound;
            f32x16 nm;
#pragma unroll
            for (int r = 0; r < 16; ++r) nm[r] = -bound;
            for (int it = 0; it < 8; ++it) {
#pragma unroll
                for (int u = 0; u < 4; ++u) {
                    const bf16x8 kf = *(const LAS bf16x8*)(kp + u * 2048);
                    const bf16x8 v0 = *(const LAS bf16x8*)(vp + u * 2048);
                    const bf16x8 v1 = *(const LAS bf16x8*)(vp + u * 2048 + 512);
                    f32x16 s = MFMA32(kf, q, nm);
                    u32x4 p0, p1;
#pragma unroll
                    for (int j = 0; j < 4; ++j) {
                        p0[j] = pk2(__builtin_amdgcn_exp2f(s[2 * j]), __builtin_amdgcn_exp2f(s[2 * j + 1]));
                        p1[j] = pk2(__builtin_amdgcn_exp2f(s[8 + 2 * j]), __builtin_amdgcn_exp2f(s[9 + 2 * j]));
                    }
                    o = MFMA32(v0, __builtin_bit_cast(bf16x8, p0), o);
                    o = MFMA32(v1, __builtin_bit_cast(bf16x8, p1), o);
                }
                kp += 8192; vp += vstep;
            }
        } else {
            m = -INFINITY;
            f32x16 z;
#pragma unroll
            for (int r = 0; r < 16; ++r) z[r] = 0.f;
            for (int it = 0; it < 8; ++it) {
#pragma unroll
                for (int u = 0; u < 4; ++u) {
                    const bf16x8 kf = *(const LAS bf16x8*)(kp + u * 2048);
                    const bf16x8 v0 = *(const LAS bf16x8*)(vp + u * 2048);
                    const bf16x8 v1 = *(const LAS bf16x8*)(vp + u * 2048 + 512);
                    f32x16 s = MFMA32(kf, q, z);
                    float mx = s[0];
#pragma unroll
                    for (int r = 1; r < 16; ++r) mx = fmaxf(mx, s[r]);
                    const float mn = fmaxf(m, max_x32(mx));
                    const float al = __builtin_amdgcn_exp2f(m - mn);
                    m = mn;
#pragma unroll
                    for (int r = 0; r < 9; ++r) o[r] *= al;
                    u32x4 p0, p1;
#pragma unroll
                    for (int j = 0; j < 4; ++j) {
                        p0[j] = pk2(__builtin_amdgcn_exp2f(s[2 * j] - mn), __builtin_amdgcn_exp2f(s[2 * j + 1] - mn));
                        p1[j] = pk2(__builtin_amdgcn_exp2f(s[8 + 2 * j] - mn), __builtin_amdgcn_exp2f(s[9 + 2 * j] - mn));
                    }
                    o = MFMA32(v0, __builtin_bit_cast(bf16x8, p0), o);
                    o = MFMA32(v1, __builtin_bit_cast(bf16x8, p1), o);
                }
                kp += 8192; vp += vstep;
            }
        }
        LAS char* part = (LAS char*)lds + A_LDS_PART + qw * 2560;
        if (kh == 1) {
            *(LAS f32x4*)(part + lane * 16) = f32x4{o[0], o[1], o[2], o[3]};
            *(LAS f32x4*)(part + 1024 + lane * 16) = f32x4{o[4], o[5], o[6], o[7]};
            *(LAS f32x2*)(part + 2048 + lane * 8) = f32x2{o[8], m};
        }
        __syncthreads();
        if (kh == 0) {
            const f32x4 a = *(const LAS f32x4*)(part + lane * 16), c = *(const LAS f32x4*)(part + 1024 + lane * 16);
            const f32x2 lm = *(const LAS f32x2*)(part + 2048 + lane * 8);
            const float mm = fmaxf(m, lm[1]);
            const float a1 = __builtin_amdgcn_exp2f(m - mm), a2 = __builtin_amdgcn_exp2f(lm[1] - mm);
            const float inv = __builtin_amdgcn_rcpf(o[8] * a1 + lm[0] * a2);
            const float s1 = a1 * inv, s2 = a2 * inv;
            const u32x4 ov = {pk2(o[0] * s1 + a[0] * s2, o[1] * s1 + a[1] * s2), pk2(o[2] * s1 + a[2] * s2, o[3] * s1 + a[3] * s2),
                              pk2(o[4] * s1 + c[0] * s2, o[5] * s1 + c[1] * s2), pk2(o[6] * s1 + c[2] * s2, o[7] * s1 + c[3] * s2)};
            const int head = 16 * rnd + 4 * qw + (c32 >> 3);
            *(LAS u32x4*)((LAS char*)lds + A_LDS_O + (head >> 1) * 512 + ((head & 1) * 2 + hi) * 128 + (c32 & 7) * 16) = ov;
        }
        __syncthreads();
        const LAS char* ob = (const LAS char*)lds + A_LDS_O + rnd * 4096 + g * 128 + (lane & 7) * 16;
#pragma unroll
        for (int ks = 0; ks < 8; ++ks) {
            const bf16x8 of = *(const LAS bf16x8*)(ob + ks * 512);
#pragma unroll
            for (int sub = 0; sub < 4; ++sub) oacc[sub] = MFMA16(wof[ks * 4 + sub], of, oacc[sub]);
        }
    }
    const int tok = blk * 8 + (lane & 7);
    float v[16];
#pragma unroll
    for (int P = 0; P < 2; ++P) {
        const int n0 = 64 * w + 32 * P + 8 * g;
        u32x4 xr;
        if (FIRST) xr = *(const u32x4*)(xin + (size_t)tok * D + n0);
        else xr = *(const u32x4*)(ws + WS_X2 + (size_t)(tok >> 4) * 16384 + (n0 >> 5) * 1024 + (g * 16 + (tok & 15)) * 16);
#pragma unroll
        for (int j = 0; j < 4; ++j) {
            const float a0 = oacc[2 * P + (j >> 1)][(2 * j) & 3], a1 = oacc[2 * P + (j >> 1)][(2 * j + 1) & 3];
            const unsigned ar = pk2(a0, a1);
            const unsigned xs = pk2(bflo(xr[j]) + bflo(ar), bfhi(xr[j]) + bfhi(ar));
            v[8 * P + 2 * j] = bflo(xs); v[8 * P + 2 * j + 1] = bfhi(xs);
            xr[j] = xs;
        }
        if (t16 < 8) *(u32x4*)(ws + WS_X1 + (size_t)(tok >> 5) * 32768 + (n0 >> 4) * 1024 + ((tok & 31) + 32 * (g & 1)) * 16) = xr;
    }
    float s = 0.f;
#pragma unroll
    for (int j = 0; j < 16; ++j) s += v[j];
    const float mw = sum_x32(sum_x16(s)) * (1.0f / 64.0f);
    float qq = 0.f;
#pragma unroll
    for (int j = 0; j < 16; ++j) { const float d = v[j] - mw; qq = fmaf(d, d, qq); }
    qq = sum_x32(sum_x16(qq));
    LAS f32x2* st = (LAS f32x2*)((LAS char*)lds + A_LDS_ST);
    if (lane < 8) st[lane * 8 + w] = f32x2{mw, qq};
    __syncthreads();
    float mean = 0.f, m2 = 0.f;
    f32x4 pp[4];
#pragma unroll
    for (int j = 0; j < 4; ++j) { pp[j] = *(const LAS f32x4*)(st + (lane & 7) * 8 + 2 * j); mean += pp[j][0] + pp[j][2]; }
    mean *= 0.125f;
#pragma unroll
    for (int j = 0; j < 4; ++j) { const float d0 = pp[j][0] - mean, d1 = pp[j][2] - mean; m2 += pp[j][1] + pp[j][3] + 64.0f * (d0 * d0 + d1 * d1); }
    const float rstd = __builtin_amdgcn_rsqf(m2 * (1.0f / 512.0f) + LN_EPS);
#pragma unroll
    for (int P = 0; P < 2; ++P) {
        const int n0 = 64 * w + 32 * P + 8 * g;
        u32x4 xn;
#pragma unroll
        for (int j = 0; j < 4; ++j) xn[j] = pk2((v[8 * P + 2 * j] - mean) * rstd, (v[8 * P + 2 * j + 1] - mean) * rstd);
        if (t16 < 8) *(u32x4*)(ws + WS_XN1 + (size_t)(tok >> 5) * 32768 + (n0 >> 4) * 1024 + ((tok & 31) + 32 * (g & 1)) * 16) = xn;
    }
}

constexpr int B_STAGE = 28 * 1024, B_SLOTS = 5, B_LDS_BYTES = B_STAGE * B_SLOTS;

__global__ void __launch_bounds__(256, 1) k_up(char* __restrict__ ws) {
    extern __shared__ __attribute__((aligned(16))) char lds[];
    const int tid = threadIdx.x, lane = tid & 63, w = __builtin_amdgcn_readfirstlane(tid >> 6);
    const int b = blockIdx.x, x8 = b & 7, i32 = b >> 3;
    const int tb = (x8 & 1) * 8 + (i32 & 7), wb = (x8 >> 1) * 4 + (i32 >> 3);
    const unsigned lds0 = (unsigned)(uintptr_t)lds;
    const char* xsrc = ws + WS_XN1 + (size_t)(tb * 4 + w) * 32768 + lane * 16;
    const char* wsrc = ws + WS_W13 + (size_t)(wb * 7) * 32768 + (size_t)w * 1024 + lane * 16;
    u32x4 xf[32];
#define B_ISSUE(s) do { \
        _Pragma("unroll") for (int j = 0; j < 4; ++j) \
            asm volatile("global_load_dwordx4 %0, %1, off" : "=v"(xf[4 * (s) + j]) : "v"(xsrc + (4 * (s) + j) * 1024) : "memory"); \
        _Pragma("unroll") for (int j = 0; j < 7; ++j) glds16(wsrc + (size_t)j * 32768 + (s) * 4096, lds0 + ((s) % B_SLOTS) * B_STAGE + (w * 7 + j) * 1024); \
    } while (0)
    B_ISSUE(0); B_ISSUE(1); B_ISSUE(2); B_ISSUE(3); B_ISSUE(4);
    f32x16 acc[7];
#pragma unroll
    for (int j = 0; j < 7; ++j)
#pragma unroll
        for (int r = 0; r < 16; ++r) acc[j][r] = 0.f;
    const LAS char* wl = (const LAS char*)lds + lane * 16;
#define B_WAIT(s, N) asm volatile("s_waitcnt vmcnt(" #N ") lgkmcnt(0)\n\ts_barrier" \
        : "+v"(xf[4 * (s)]), "+v"(xf[4 * (s) + 1]), "+v"(xf[4 * (s) + 2]), "+v"(xf[4 * (s) + 3]) :: "memory")
#define B_COMPUTE(s) do { \
        _Pragma("unroll") for (int kk = 0; kk < 4; ++kk) { \
            _Pragma("unroll") for (int j = 0; j < 7; ++j) { \
                const bf16x8 a = *(const LAS bf16x8*)(wl + ((s) % B_SLOTS) * B_STAGE + (kk * 7 + j) * 1024); \
                acc[j] = MFMA32(a, __builtin_bit_cast(bf16x8, xf[4 * (s) + kk]), acc[j]); } } \
    } while (0)
    B_WAIT(0, 44); B_COMPUTE(0);
    B_WAIT(1, 33); B_ISSUE(5); B_COMPUTE(1);
    B_WAIT(2, 33); B_ISSUE(6); B_COMPUTE(2);
    B_WAIT(3, 33); B_ISSUE(7); B_COMPUTE(3);
    B_WAIT(4, 33); B_COMPUTE(4);
    B_WAIT(5, 22); B_COMPUTE(5);
    B_WAIT(6, 11); B_COMPUTE(6);
    B_WAIT(7, 0);  B_COMPUTE(7);
    char* hdst = ws + WS_H + (size_t)((tb * 4 + w) * 112 + wb * 7) * 1024 + lane * 16;
#pragma unroll
    for (int j = 0; j < 7; ++j) {
        float hv[8];
#pragma unroll
        for (int r = 0; r < 8; ++r) {
            const float gt = acc[j][r], up = acc[j][r + 8];
            hv[r] = gt * up * __builtin_amdgcn_rcpf(1.0f + __builtin_amdgcn_exp2f(gt * -1.4426950408889634f));
        }
        *(u32x4*)(hdst + j * 1024) = u32x4{pk2(hv[0], hv[1]), pk2(hv[2], hv[3]), pk2(hv[4], hv[5]), pk2(hv[6], hv[7])};
    }
}

constexpr int C_LDS_BYTES = 65536;
template <bool LAST>
__global__ void __launch_bounds__(256, 1) k_down(char* __restrict__ ws, u16* __restrict__ out) {
    extern __shared__ __attribute__((aligned(16))) char lds[];
    const int tid = threadIdx.x, lane = tid & 63, w = __builtin_amdgcn_readfirstlane(tid >> 6);
    const int b = blockIdx.x, x8 = b & 7, i32 = b >> 3;
    const int tb = (x8 >> 1) * 8 + (i32 & 7), cb = (x8 & 1) * 4 + (i32 >> 3);
    const char* hs = ws + WS_H + (size_t)(tb * 2 * 112 + w * 28) * 1024 + lane * 16;
    const char* wsr = ws + WS_W2T + (size_t)(cb * 2 * 112 + w * 28) * 1024 + lane * 16;
    constexpr int WIN = 12;
    bf16x8 hf[28][2], wf[28][2];
#define C_LOAD(i) do { hf[i][0] = *(const bf16x8*)(hs + (i) * 1024); wf[i][0] = *(const bf16x8*)(wsr + (i) * 1024); \
                       hf[i][1] = *(const bf16x8*)(hs + (112 + (i)) * 1024); wf[i][1] = *(const bf16x8*)(wsr + (112 + (i)) * 1024); } while (0)
#pragma unroll
    for (int i = 0; i < WIN; ++i) C_LOAD(i);
    const int tt = tb * 2 + (w & 1), ct = cb * 2 + (w >> 1), c32 = lane & 31, hi = lane >> 5;
    const u32x4 xr0 = *(const u32x4*)(ws + WS_X1 + (size_t)(tt * 32 + ct * 2) * 1024 + lane * 16);
    const u32x4 xr1 = *(const u32x4*)(ws + WS_X1 + (size_t)(tt * 32 + ct * 2 + 1) * 1024 + lane * 16);
    f32x16 acc[4];
#pragma unroll
    for (int j = 0; j < 4; ++j)
#pragma unroll
        for (int r = 0; r < 16; ++r) acc[j][r] = 0.f;
    SBAR();
#pragma unroll
    for (int i = 0; i < 28; ++i) {
        acc[0] = MFMA32(wf[i][0], hf[i][0], acc[0]);
        acc[1] = MFMA32(wf[i][0], hf[i][1], acc[1]);
        acc[2] = MFMA32(wf[i][1], hf[i][0], acc[2]);
        acc[3] = MFMA32(wf[i][1], hf[i][1], acc[3]);
        if (i + WIN < 28) C_LOAD(i + WIN);
        SBAR();
    }
    LAS char* lb = (LAS char*)lds + lane * 16;
#pragma unroll
    for (int j = 0; j < 4; ++j) {
        if (j != w) {
#pragma unroll
            for (int q = 0; q < 4; ++q)
                *(LAS f32x4*)(lb + (j * 4 + w) * 4096 + q * 1024) = f32x4{acc[j][4 * q], acc[j][4 * q + 1], acc[j][4 * q + 2], acc[j][4 * q + 3]};
        }
    }
    f32x16 f = (w == 0) ? acc[0] : (w == 1) ? acc[1] : (w == 2) ? acc[2] : acc[3];
    __syncthreads();
#pragma unroll
    for (int j = 1; j < 4; ++j) {
        const int sw = (w + j) & 3;
#pragma unroll
        for (int q = 0; q < 4; ++q) {
            const f32x4 p = *(const LAS f32x4*)(lb + (w * 4 + sw) * 4096 + q * 1024);
#pragma unroll
            for (int r = 0; r < 4; ++r) f[4 * q + r] += p[r];
        }
    }
    float v[16]; u32x4 xo[2];
#pragma unroll
    for (int j = 0; j < 8; ++j) {
        const unsigned xr = (j < 4) ? xr0[j & 3] : xr1[j & 3];
        const unsigned ar = pk2(f[2 * j], f[2 * j + 1]);
        const unsigned xs = pk2(bflo(xr) + bflo(ar), bfhi(xr) + bfhi(ar));
        xo[j >> 2][j & 3] = xs; v[2 * j] = bflo(xs); v[2 * j + 1] = bfhi(xs);
    }
    const int tok = tt * 32 + c32;
    if (LAST) {
        *(u32x4*)(out + (size_t)tok * D + ct * 32 + 8 * hi) = xo[0];
        *(u32x4*)(out + (size_t)tok * D + ct * 32 + 16 + 8 * hi) = xo[1];
    } else {
        char* xd = ws + WS_X2 + (size_t)(tok >> 4) * 16384 + ct * 1024 + (hi * 16 + (tok & 15)) * 16;
        *(u32x4*)xd = xo[0]; *(u32x4*)(xd + 512) = xo[1];
        float s = 0.f;
#pragma unroll
        for (int j = 0; j < 16; ++j) s += v[j];
        const float mw = sum_x32(s) * (1.0f / 32.0f);
        float qq = 0.f;
#pragma unroll
        for (int j = 0; j < 16; ++j) { const float d = v[j] - mw; qq = fmaf(d, d, qq); }
        qq = sum_x32(qq);
        if (hi == 0) *(f32x2*)(ws + WS_ST + (size_t)tok * 128 + ct * 8) = f32x2{mw, qq};
    }
}

extern "C" void kernel_launch(void* const* d_in, const int* in_sizes, int n_in, void* d_out, int out_size, void* d_ws, size_t ws_size,
                              hipStream_t stream) {
    const u16* x = (const u16*)d_in[0];
    const u16* wq = (const u16*)d_in[1]; const u16* wk = (const u16*)d_in[2]; const u16* wv = (const u16*)d_in[3]; const u16* wo = (const u16*)d_in[4];
    const u16* w1 = (const u16*)d_in[6]; const u16* w3 = (const u16*)d_in[7]; const u16* w2 = (const u16*)d_in[8];
    char* ws = (char*)d_ws; u16* out = (u16*)d_out;
    (void)hipFuncSetAttribute((const void*)k_qkv<true>, hipFuncAttributeMaxDynamicSharedMemorySize, D_LDS_BYTES);
    (void)hipFuncSetAttribute((const void*)k_qkv<false>, hipFuncAttributeMaxDynamicSharedMemorySize, D_LDS_BYTES);
    (void)hipFuncSetAttribute((const void*)k_attn<true>, hipFuncAttributeMaxDynamicSharedMemorySize, A_LDS_BYTES);
    (void)hipFuncSetAttribute((const void*)k_attn<false>, hipFuncAttributeMaxDynamicSharedMemorySize, A_LDS_BYTES);
    (void)hipFuncSetAttribute((const void*)k_up, hipFuncAttributeMaxDynamicSharedMemorySize, B_LDS_BYTES);
    (void)hipFuncSetAttribute((const void*)k_down<true>, hipFuncAttributeMaxDynamicSharedMemorySize, C_LDS_BYTES);
    (void)hipFuncSetAttribute((const void*)k_down<false>, hipFuncAttributeMaxDynamicSharedMemorySize, C_LDS_BYTES);
    k_qkv<true><<<256 + RETILE_BLOCKS, 512, D_LDS_BYTES, stream>>>(x, wq, wk, wv, wo, w1, w3, w2, ws);
    k_attn<true><<<256, 512, A_LDS_BYTES, stream>>>(x, ws);
    k_up<<<256, 256, B_LDS_BYTES, stream>>>(ws);
    k_down<false><<<256, 256, C_LDS_BYTES, stream>>>(ws, out);
    k_qkv<false><<<256, 512, D_LDS_BYTES, stream>>>(x, wq, wk, wv, wo, w1, w3, w2, ws);
    k_attn<false><<<256, 512, A_LDS_BYTES, stream>>>(x, ws);
    k_up<<<256, 256, B_LDS_BYTES, stream>>>(ws);
    k_down<true><<<256, 256, C_LDS_BYTES, stream>>>(ws, out);
}
```

```cpp
#include <hip/hip_runtime.h>
#include <hip/hip_bf16.h>
#include <stdint.h>

typedef unsigned short u16;

constexpr size_t WS_SYNC = 0x0;
constexpr size_t WS_DBG = 0x10000;
constexpr size_t WS_CS = 0x40000;
constexpr size_t WS_KMX = 0x41000;
constexpr size_t WS_QMX = 0x42000;
constexpr size_t WS_WKVT = 0x4a000;
constexpr size_t WS_KV = 0x52000;
constexpr size_t WS_ST = 0x92000;
constexpr size_t WS_WOT = 0xd2000;
constexpr size_t WS_WQT = 0x152000;
constexpr size_t WS_W13 = 0x1d2000;
constexpr size_t WS_W2T = 0x552000;
constexpr size_t WS_Q = 0x712000;
constexpr size_t WS_X1 = 0xb12000;
constexpr size_t WS_XN1 = 0xd12000;
constexpr size_t WS_X2 = 0x1112000;
constexpr size_t WS_H = 0x1312000;
constexpr size_t WS_BYTES = 0x1a12000;


__global__ void __launch_bounds__(256, 1) k_first(char* ws, const u16* xin, const u16* wq, const u16* wk, const u16* wv, const u16* wo,
                                                  const u16* w1, const u16* w3, const u16* w2) {
    const int w = __builtin_amdgcn_readfirstlane(threadIdx.x >> 6);
    asm volatile(
        "s_load_dwordx16 s[64:79], %0, 0\n"
        "s_load_dwordx2 s[80:81], %0, 64\n"
        "v_mbcnt_lo_u32_b32 v0, -1, 0\n"
        "v_mbcnt_hi_u32_b32 v0, -1, v0\n"
        "v_lshlrev_b32 v1, 4, v0\n"
        "s_mov_b32 s28, %2\n"
        "s_and_b32 s29, %1, 7\n"
        "s_lshr_b32 s30, %1, 3\n"
        "s_lshl_b32 s50, s29, 2\n"
        "s_lshr_b32 s37, s30, 3\n"
        "s_add_u32 s50, s50, s37\n"
        "s_and_b32 s51, s30, 7\n"
        "v_and_b32 v16, 15, v0\n"
        "v_lshrrev_b32 v17, 4, v0\n"
        "s_lshl_b32 s33, s50, 6\n"
        "s_lshl_b32 s37, s28, 4\n"
        "s_add_u32 s33, s33, s37\n"
        "v_lshlrev_b32 v2, 8, v16\n"
        "v_xor_b32 v5, v17, v16\n"
        "v_lshl_add_u32 v2, v5, 4, v2\n"
        "s_lshl_b32 s37, s28, 14\n"
        "s_add_u32 s39, s37, 0x14000\n"
        "v_add_u32 v2, s39, v2\n"
        "v_add_u32 v16, s33, v16\n"
        "s_lshl_b32 s33, s33, 10\n"
        "v_add_u32 v5, 0, v17\n"
        "v_and_b32 v6, 15, v0\n"
        "v_xor_b32 v6, v6, v5\n"
        "v_lshlrev_b32 v6, 4, v6\n"
        "v_lshl_add_u32 v28, v5, 10, v6\n"
        "v_add_u32 v5, 4, v17\n"
        "v_and_b32 v6, 15, v0\n"
        "v_xor_b32 v6, v6, v5\n"
        "v_lshlrev_b32 v6, 4, v6\n"
        "v_lshl_add_u32 v29, v5, 10, v6\n"
        "v_add_u32 v5, 8, v17\n"
        "v_and_b32 v6, 15, v0\n"
        "v_xor_b32 v6, v6, v5\n"
        "v_lshlrev_b32 v6, 4, v6\n"
        "v_lshl_add_u32 v30, v5, 10, v6\n"
        "v_add_u32 v5, 12, v17\n"
        "v_and_b32 v6, 15, v0\n"
        "v_xor_b32 v6, v6, v5\n"
        "v_lshlrev_b32 v6, 4, v6\n"
        "v_lshl_add_u32 v31, v5, 10, v6\n"
        "v_bfe_u32 v5, v0, 4, 1\n"
        "v_lshlrev_b32 v5, 1, v5\n"
        "v_and_b32 v6, 7, v0\n"
        "v_xor_b32 v5, v5, v6\n"
        "v_lshlrev_b32 v5, 4, v5\n"
        "v_lshrrev_b32 v6, 3, v0\n"
        "v_lshl_add_u32 v3, v6, 10, v5\n"
        "v_xor_b32 v4, 64, v3\n"
        "s_lshl_b32 s34, s28, 15\n"
        "s_lshl_b32 s37, s51, 7\n"
        "s_add_u32 s34, s34, s37\n"
        "s_lshl_b32 s35, s28, 12\n"
        "s_lshl_b32 s36, s28, 10\n"
        "s_add_u32 s49, s50, s51\n"
        "s_waitcnt lgkmcnt(0)\n"
        "s_mov_b64 s[20:21], s[66:67]\n"
        "s_and_b32 s21, s21, 0xffff\n"
        "s_mov_b32 s22, 0x200000\n"
        "s_mov_b32 s23, 0x20000\n"
        "s_mov_b64 s[24:25], s[68:69]\n"
        "s_and_b32 s25, s25, 0xffff\n"
        "s_mov_b32 s26, 0x80000\n"
        "s_mov_b32 s27, 0x20000\n"
        "s_mov_b64 s[16:17], s[64:65]\n"
        "s_and_b32 s17, s17, 0xffff\n"
        "s_mov_b32 s18, 0x1a12000\n"
        "s_mov_b32 s19, 0x20000\n"
        "s_and_b32 s37, s51, 1\n"
        "s_cmp_eq_u32 s37, 0\n"
        "s_cselect_b64 s[52:53], s[70:71], s[72:73]\n"
        "s_and_b32 s53, s53, 0xffff\n"
        "s_mov_b32 s54, 0x4000\n"
        "s_mov_b32 s55, 0x20000\n"
        "s_add_u32 s48, s49, 0\n"
        "s_and_b32 s48, s48, 3\n"
        "s_lshl_b32 s60, s48, 17\n"
        "s_add_u32 s60, s60, s34\n"
        "s_lshl_b32 s61, s48, 8\n"
        "s_add_u32 s61, s61, s33\n"
        "s_lshl_b32 s62, s48, 12\n"
        "s_add_u32 s62, s62, s36\n"
        "s_add_u32 m0, s35, 0x0\n"
        "s_add_u32 s37, s60, 0x0\n"
        "buffer_load_dwordx4 v3, s[24:27], s37 offen lds\n"
        "s_add_u32 m0, s39, 0x0\n"
        "s_nop 0\n"
        "buffer_load_dwordx4 v28, s[20:23], s61 offen lds\n"
        "s_add_u32 m0, s35, 0x400\n"
        "s_add_u32 s37, s60, 0x2000\n"
        "buffer_load_dwordx4 v4, s[24:27], s37 offen lds\n"
        "s_add_u32 m0, s39, 0x400\n"
        "s_nop 0\n"
        "buffer_load_dwordx4 v29, s[20:23], s61 offen lds\n"
        "s_add_u32 m0, s35, 0x800\n"
        "s_add_u32 s37, s60, 0x4000\n"
        "buffer_load_dwordx4 v3, s[24:27], s37 offen lds\n"
        "s_add_u32 m0, s39, 0x800\n"
        "s_nop 0\n"
        "buffer_load_dwordx4 v30, s[20:23], s61 offen lds\n"
        "s_add_u32 m0, s35, 0xc00\n"
        "s_add_u32 s37, s60, 0x6000\n"
        "buffer_load_dwordx4 v4, s[24:27], s37 offen lds\n"
        "s_add_u32 m0, s39, 0xc00\n"
        "s_nop 0\n"
        "buffer_load_dwordx4 v31, s[20:23], s61 offen lds\n"
        "s_add_u32 m0, s36, 0x10000\n"
        "s_nop 0\n"
        "buffer_load_dwordx4 v1, s[52:55], s62 offen lds\n"
        "s_getpc_b64 s[46:47]\n"
        "L_fpc:\n"
        "s_lshl_b32 s38, s30, 2\n"
        "s_add_u32 s38, s38, s28\n"
        "s_lshl_b32 s38, s38, 7\n"
        "v_lshl_add_u32 v255, v0, 14, s38\n"
        "v_min_u32 v255, L_fend-L_fpc-4, v255\n"
        "s_mov_b64 exec, 1\n"
        "global_load_dword v255, v255, s[46:47]\n"
        "s_mov_b64 exec, -1\n"
        "s_lshl_b32 s40, %1, 2\n"
        "s_add_u32 s40, s40, s28\n"
        "v_and_b32 v18, 3, v0\n"
        "v_lshlrev_b32 v18, 1, v18\n"
        "v_bfe_u32 v19, v0, 2, 1\n"
        "v_or_b32 v18, v18, v19\n"
        "v_and_or_b32 v18, v0, 56, v18\n"
        "v_lshlrev_b32 v19, 4, v18\n"
        "v_and_b32 v8, 31, v18\n"
        "v_lshrrev_b32 v9, 5, v18\n"
        "s_cmp_lt_u32 s40, 448\n"
        "s_cbranch_scc1 L_rt13\n"
        "s_cmp_lt_u32 s40, 672\n"
        "s_cbranch_scc1 L_rt2\n"
        "s_cmp_lt_u32 s40, 800\n"
        "s_cbranch_scc1 L_rto\n"
        "s_cmp_lt_u32 s40, 804\n"
        "s_cbranch_scc1 L_rtk\n"
        "s_mov_b32 s42, 3\n"
        "s_branch L_rtx\n"
        "L_rt13:\n"
        "s_cmp_lt_u32 s40, 224\n"
        "s_cselect_b64 s[56:57], s[76:77], s[78:79]\n"
        "s_cselect_b32 s43, 0, 224\n"
        "s_cselect_b32 s44, 0, 256\n"
        "s_sub_u32 s40, s40, s43\n"
        "s_mul_i32 s43, s40, 0x2493\n"
        "s_lshr_b32 s43, s43, 16\n"
        "s_mul_i32 s45, s43, 7\n"
        "s_sub_u32 s45, s40, s45\n"
        "s_mov_b32 s58, 0x1c0000\n"
        "s_mov_b32 s41, 3584\n"
        "s_mov_b32 s42, 0\n"
        "s_mul_i32 s46, s43, 0xe000\n"
        "s_lshl_b32 s47, s45, 9\n"
        "s_add_u32 s46, s46, s47\n"
        "v_mul_u32_u24 v6, 0x7000, v9\n"
        "v_lshl_add_u32 v6, v8, 4, v6\n"
        "v_add_u32 v6, s46, v6\n"
        "s_lshl_b32 s46, s45, 19\n"
        "s_lshl_b32 s47, s43, 10\n"
        "s_add_u32 s46, s46, s47\n"
        "s_add_u32 s46, s46, s44\n"
        "s_add_u32 s46, s46, 0x1d2000\n"
        "v_lshrrev_b32 v7, 1, v8\n"
        "v_lshlrev_b32 v7, 15, v7\n"
        "v_and_b32 v10, 1, v8\n"
        "v_lshl_add_u32 v7, v10, 6, v7\n"
        "v_lshl_add_u32 v7, v9, 9, v7\n"
        "v_add_u32 v7, s46, v7\n"
        "s_branch L_rtl\n"
        "L_rt2:\n"
        "s_sub_u32 s40, s40, 448\n"
        "s_mov_b64 s[56:57], s[80:81]\n"
        "s_mov_b32 s58, 0x1c0000\n"
        "s_mov_b32 s41, 1024\n"
        "s_mov_b32 s42, 0\n"
        "s_lshl_b32 s46, s40, 13\n"
        "v_add_u32 v6, s46, v19\n"
        "s_lshr_b32 s46, s40, 1\n"
        "s_lshl_b32 s46, s46, 10\n"
        "s_and_b32 s47, s40, 1\n"
        "s_lshl_b32 s47, s47, 9\n"
        "s_add_u32 s46, s46, s47\n"
        "s_add_u32 s46, s46, 0x552000\n"
        "v_lshrrev_b32 v7, 2, v18\n"
        "v_mul_u32_u24 v7, 0x1c000, v7\n"
        "v_bfe_u32 v10, v18, 1, 1\n"
        "v_lshl_add_u32 v7, v10, 8, v7\n"
        "v_and_b32 v10, 1, v18\n"
        "v_lshl_add_u32 v7, v10, 6, v7\n"
        "v_add_u32 v7, s46, v7\n"
        "s_branch L_rtl\n"
        "L_rto:\n"
        "s_cmp_lt_u32 s40, 736\n"
        "s_cselect_b64 s[56:57], s[74:75], s[68:69]\n"
        "s_cselect_b32 s44, 1, 0\n"
        "s_sub_u32 s40, s40, 672\n"
        "s_and_b32 s40, s40, 63\n"
        "s_mov_b32 s58, 0x80000\n"
        "s_mov_b32 s41, 1024\n"
        "s_mov_b32 s42, 1\n"
        "s_lshl_b32 s46, s40, 13\n"
        "v_add_u32 v6, s46, v19\n"
        "s_lshr_b32 s46, s40, 2\n"
        "s_add_u32 s47, s44, 12\n"
        "s_lshl_b32 s46, s46, s47\n"
        "s_and_b32 s47, s40, 3\n"
        "s_lshl_b32 s47, s47, 8\n"
        "s_add_u32 s46, s46, s47\n"
        "s_cmp_eq_u32 s44, 1\n"
        "s_cselect_b32 s47, 0xfff80000, 0\n"
        "s_add_u32 s46, s46, s47\n"
        "s_add_u32 s46, s46, 0x152000\n"
        "s_add_u32 s47, s44, 3\n"
        "v_lshrrev_b32 v7, s47, v18\n"
        "s_add_u32 s47, s44, 16\n"
        "v_lshlrev_b32 v7, s47, v7\n"
        "s_add_u32 s47, s44, 1\n"
        "v_bfe_u32 v10, v18, 2, s47\n"
        "v_lshl_add_u32 v7, v10, 11, v7\n"
        "v_and_b32 v10, 3, v18\n"
        "v_lshl_add_u32 v7, v10, 6, v7\n"
        "v_add_u32 v7, s46, v7\n"
        "s_branch L_rtl\n"
        "L_rtk:\n"
        "s_cmp_lt_u32 s40, 802\n"
        "s_cselect_b64 s[56:57], s[70:71], s[72:73]\n"
        "s_cselect_b32 s44, 0, 0x4000\n"
        "s_and_b32 s40, s40, 1\n"
        "s_mov_b32 s58, 0x4000\n"
        "s_mov_b32 s41, 32\n"
        "s_mov_b32 s42, 2\n"
        "s_lshl_b32 s46, s40, 13\n"
        "v_lshrrev_b32 v10, 1, v18\n"
        "v_lshlrev_b32 v6, 8, v10\n"
        "v_and_b32 v11, 1, v18\n"
        "v_lshl_add_u32 v6, v11, 4, v6\n"
        "v_add_u32 v6, s46, v6\n"
        "s_add_u32 s46, s46, s44\n"
        "s_add_u32 s46, s46, 0x4a000\n"
        "v_lshrrev_b32 v7, 3, v18\n"
        "v_lshlrev_b32 v7, 10, v7\n"
        "v_and_b32 v10, 3, v10\n"
        "v_lshl_add_u32 v7, v10, 8, v7\n"
        "v_lshl_add_u32 v7, v11, 7, v7\n"
        "v_add_u32 v7, s46, v7\n"
        "L_rtl:\n"
        "L_rtx:\n"
        "s_cmp_eq_u32 s42, 3\n"
        "s_cselect_b32 s58, 0, s58\n"
        "s_cselect_b64 s[56:57], s[68:69], s[56:57]\n"
        "s_cselect_b32 s41, 0, s41\n"
        "s_and_b32 s57, s57, 0xffff\n"
        "s_mov_b32 s59, 0x20000\n"
        "s_mov_b32 s43, 0\n"
        "buffer_load_dwordx4 v[192:195], v6, s[56:59], s43 offen\n"
        "s_add_u32 s43, s43, s41\n"
        "buffer_load_dwordx4 v[196:199], v6, s[56:59], s43 offen\n"
        "s_add_u32 s43, s43, s41\n"
        "buffer_load_dwordx4 v[200:203], v6, s[56:59], s43 offen\n"
        "s_add_u32 s43, s43, s41\n"
        "buffer_load_dwordx4 v[204:207], v6, s[56:59], s43 offen\n"
        "s_add_u32 s43, s43, s41\n"
        "buffer_load_dwordx4 v[208:211], v6, s[56:59], s43 offen\n"
        "s_add_u32 s43, s43, s41\n"
        "buffer_load_dwordx4 v[212:215], v6, s[56:59], s43 offen\n"
        "s_add_u32 s43, s43, s41\n"
        "buffer_load_dwordx4 v[216:219], v6, s[56:59], s43 offen\n"
        "s_add_u32 s43, s43, s41\n"
        "buffer_load_dwordx4 v[220:223], v6, s[56:59], s43 offen\n"
        "s_add_u32 s48, s49, 1\n"
        "s_and_b32 s48, s48, 3\n"
        "s_lshl_b32 s60, s48, 17\n"
        "s_add_u32 s60, s60, s34\n"
        "s_lshl_b32 s61, s48, 8\n"
        "s_add_u32 s61, s61, s33\n"
        "s_lshl_b32 s62, s48, 12\n"
        "s_add_u32 s62, s62, s36\n"
        "s_add_u32 m0, s35, 0x4000\n"
        "s_add_u32 s37, s60, 0x0\n"
        "buffer_load_dwordx4 v3, s[24:27], s37 offen lds\n"
        "s_add_u32 m0, s39, 0x1000\n"
        "s_nop 0\n"
        "buffer_load_dwordx4 v28, s[20:23], s61 offen lds\n"
        "s_add_u32 m0, s35, 0x4400\n"
        "s_add_u32 s37, s60, 0x2000\n"
        "buffer_load_dwordx4 v4, s[24:27], s37 offen lds\n"
        "s_add_u32 m0, s39, 0x1400\n"
        "s_nop 0\n"
        "buffer_load_dwordx4 v29, s[20:23], s61 offen lds\n"
        "s_add_u32 m0, s35, 0x4800\n"
        "s_add_u32 s37, s60, 0x4000\n"
        "buffer_load_dwordx4 v3, s[24:27], s37 offen lds\n"
        "s_add_u32 m0, s39, 0x1800\n"
        "s_nop 0\n"
        "buffer_load_dwordx4 v30, s[20:23], s61 offen lds\n"
        "s_add_u32 m0, s35, 0x4c00\n"
        "s_add_u32 s37, s60, 0x6000\n"
        "buffer_load_dwordx4 v4, s[24:27], s37 offen lds\n"
        "s_add_u32 m0, s39, 0x1c00\n"
        "s_nop 0\n"
        "buffer_load_dwordx4 v31, s[20:23], s61 offen lds\n"
        "s_add_u32 m0, s36, 0x11000\n"
        "s_nop 0\n"
        "buffer_load_dwordx4 v1, s[52:55], s62 offen lds\n"
        "v_bfe_u32 v8, v0, 2, 2\n"
        "v_lshl_add_u32 v9, v17, 3, v8\n"
        "v_bfe_u32 v10, v0, 3, 1\n"
        "v_lshlrev_b32 v10, 1, v10\n"
        "v_and_b32 v11, 1, v17\n"
        "v_lshl_add_u32 v10, v11, 2, v10\n"
        "v_bfe_u32 v11, v0, 1, 1\n"
        "v_xor_b32 v10, v10, v11\n"
        "v_lshlrev_b32 v10, 4, v10\n"
        "v_and_b32 v11, 1, v0\n"
        "v_lshl_add_u32 v10, v11, 3, v10\n"
        "v_lshl_add_u32 v12, v9, 7, v10\n"
        "s_lshl_b32 s37, s28, 5\n"
        "v_xor_b32 v12, s37, v12\n"
        "v_xor_b32 v13, 32, v12\n"
        "v_xor_b32 v14, 64, v12\n"
        "v_xor_b32 v15, 0x60, v12\n"
        "v_and_b32 v11, 3, v0\n"
        "v_lshlrev_b32 v11, 3, v11\n"
        "v_lshl_add_u32 v11, v9, 5, v11\n"
        "v_add_u32 v11, 0x10000, v11\n"
        "v_mov_b32 v24, 0x3f803f80\n"
        "v_mov_b32 v25, 0x3f803f80\n"
        "v_mov_b32 v26, 0x3f803f80\n"
        "v_mov_b32 v27, 0x3f803f80\n"
        "v_mov_b32 v9, 0x05040100\n"
        "v_mov_b32 v10, 0x07060302\n"
        "s_mov_b32 s46, 0xaaaaaaaa\n"
        "s_mov_b32 s47, 0xaaaaaaaa\n"
        "s_mov_b64 exec, s[46:47]\n"
        "v_swap_b32 v9, v10\n"
        "s_mov_b64 exec, -1\n"
        "s_cmp_lg_u32 s28, 3\n"
        "s_cbranch_scc1 L_nz\n"
        "s_cmp_ge_u32 %1, 128\n"
        "s_cbranch_scc1 L_nz\n"
        "v_mov_b32 v124, 0\n"
        "v_mov_b32 v125, 0\n"
        "v_mov_b32 v126, 0\n"
        "v_mov_b32 v127, 0\n"
        "s_lshl_b32 s37, %1, 8\n"
        "s_add_u32 s37, s37, 0x0\n"
        "s_mov_b64 exec, 0xffff\n"
        "buffer_store_dwordx4 v[124:127], v1, s[16:19], s37 offen sc1\n"
        "s_mov_b64 exec, -1\n"
        "L_nz:\n"
        "v_mov_b32 v92, -1\n"
        "v_mov_b32 v93, -1\n"
        "v_mov_b32 v94, -1\n"
        "v_mov_b32 v95, -1\n"
        "s_lshl_b32 s63, %1, 2\n"
        "s_add_u32 s63, s63, s28\n"
        "s_lshl_b32 s63, s63, 11\n"
        "s_add_u32 s63, s63, 0xd12000\n"
        "s_lshl_b32 s37, s28, 10\n"
        "v_add_u32 v11, s37, v11\n"
        "s_lshr_b32 s37, s51, 1\n"
        "s_sub_u32 s37, s37, s28\n"
        "s_lshl_b32 s37, s37, 14\n"
        "v_add_u32 v8, s37, v2\n"
        "s_lshl_b32 s37, s28, 6\n"
        "v_xor_b32 v8, s37, v8\n"
        "v_lshrrev_b32 v22, 3, v16\n"
        "v_lshlrev_b32 v18, 6, v22\n"
        "v_lshlrev_b32 v22, 13, v22\n"
        "v_lshrrev_b32 v23, 1, v17\n"
        "v_lshl_add_u32 v22, v23, 9, v22\n"
        "v_and_b32 v23, 7, v16\n"
        "v_lshl_add_u32 v22, v23, 4, v22\n"
        "v_and_b32 v23, 1, v17\n"
        "v_lshl_add_u32 v22, v23, 7, v22\n"
        "s_and_b32 s37, s30, 7\n"
        "s_lshl_b32 s64, s37, 10\n"
        "s_add_u32 s64, s64, 0x712000\n"
        "s_lshl_b32 s38, s28, 7\n"
        "v_xor_b32 v22, s38, v22\n"
        "s_lshl_b32 s66, s37, 3\n"
        "s_add_u32 s66, s66, 0x42000\n"
        "s_lshl_b32 s67, s50, 2\n"
        "s_add_u32 s67, s67, s28\n"
        "s_lshl_b32 s67, s67, 3\n"
        "s_add_u32 s67, s67, 0x41000\n"
        "s_mov_b32 s65, 0x52000\n"
        "s_lshr_b32 s38, s37, 1\n"
        "s_and_b32 s68, s37, 1\n"
        "s_cmp_eq_u32 s38, s28\n"
        "s_cselect_b32 s68, s68, 2\n"
        "v_lshrrev_b32 v19, 5, v16\n"
        "v_lshlrev_b32 v19, 11, v19\n"
        "s_cmp_eq_u32 s68, 1\n"
        "s_cbranch_scc1 L_F_pv\n"
        "v_and_b32 v23, 31, v16\n"
        "v_lshl_add_u32 v19, v23, 4, v19\n"
        "v_lshrrev_b32 v23, 1, v17\n"
        "v_lshl_add_u32 v19, v23, 9, v19\n"
        "v_and_b32 v23, 1, v17\n"
        "v_lshl_add_u32 v19, v23, 3, v19\n"
        "s_branch L_F_pe\n"
        "L_F_pv:\n"
        "v_bfe_u32 v23, v16, 4, 1\n"
        "v_lshl_add_u32 v19, v23, 9, v19\n"
        "v_bfe_u32 v23, v0, 2, 1\n"
        "v_lshl_add_u32 v19, v23, 8, v19\n"
        "v_lshl_add_u32 v19, v17, 6, v19\n"
        "v_bfe_u32 v23, v0, 3, 1\n"
        "v_lshl_add_u32 v19, v23, 3, v19\n"
        "v_and_b32 v23, 3, v0\n"
        "v_lshl_add_u32 v19, v23, 1, v19\n"
        "L_F_pe:\n"
        "v_xor_b32 v23, 0x100, v22\n"
        "s_waitcnt lgkmcnt(0)\n"
        "s_waitcnt vmcnt(17)\n"
        "s_barrier\n"
        "v_xor_b32 v5, 0, v2\n"
        "ds_read_b128 v[128:131], v5 offset:0\n"
        "v_xor_b32 v5, 64, v2\n"
        "ds_read_b128 v[132:135], v5 offset:0\n"
        "v_xor_b32 v5, 128, v2\n"
        "ds_read_b128 v[136:139], v5 offset:0\n"
        "v_xor_b32 v5, 192, v2\n"
        "ds_read_b128 v[140:143], v5 offset:0\n"
        "ds_read_b64_tr_b16 v[32:33], v12 offset:0\n"
        "ds_read_b64_tr_b16 v[34:35], v12 offset:512\n"
        "ds_read_b64_tr_b16 v[36:37], v13 offset:0\n"
        "ds_read_b64_tr_b16 v[38:39], v13 offset:512\n"
        "ds_read_b64_tr_b16 v[40:41], v14 offset:0\n"
        "ds_read_b64_tr_b16 v[42:43], v14 offset:512\n"
        "ds_read_b64_tr_b16 v[44:45], v15 offset:0\n"
        "ds_read_b64_tr_b16 v[46:47], v15 offset:512\n"
        "ds_read_b64_tr_b16 v[48:49], v12 offset:4096\n"
        "ds_read_b64_tr_b16 v[50:51], v12 offset:4608\n"
        "ds_read_b64_tr_b16 v[52:53], v13 offset:4096\n"
        "ds_read_b64_tr_b16 v[54:55], v13 offset:4608\n"
        "s_waitcnt lgkmcnt(10)\n"
        "v_mfma_f32_16x16x32_bf16 v[64:67], v[32:35], v[128:131], 0\n"
        "v_mfma_f32_16x16x32_bf16 v[96:99], v[32:35], v[24:27], 0\n"
        "s_add_u32 s48, s49, 2\n"
        "s_and_b32 s48, s48, 3\n"
        "s_lshl_b32 s60, s48, 17\n"
        "s_add_u32 s60, s60, s34\n"
        "s_lshl_b32 s61, s48, 8\n"
        "s_add_u32 s61, s61, s33\n"
        "s_lshl_b32 s62, s48, 12\n"
        "s_add_u32 s62, s62, s36\n"
        "s_add_u32 m0, s35, 0x8000\n"
        "s_add_u32 s37, s60, 0x0\n"
        "buffer_load_dwordx4 v3, s[24:27], s37 offen lds\n"
        "ds_read_b64_tr_b16 v[56:57], v14 offset:4096\n"
        "ds_read_b64_tr_b16 v[58:59], v14 offset:4608\n"
        "s_waitcnt lgkmcnt(10)\n"
        "v_mfma_f32_16x16x32_bf16 v[68:71], v[36:39], v[128:131], 0\n"
        "v_mfma_f32_16x16x32_bf16 v[116:119], v[24:27], v[128:131], 0\n"
        "s_add_u32 m0, s39, 0x2000\n"
        "s_nop 0\n"
        "buffer_load_dwordx4 v28, s[20:23], s61 offen lds\n"
        "ds_read_b64_tr_b16 v[60:61], v15 offset:4096\n"
        "ds_read_b64_tr_b16 v[62:63], v15 offset:4608\n"
        "s_waitcnt lgkmcnt(10)\n"
        "v_mfma_f32_16x16x32_bf16 v[72:75], v[40:43], v[128:131], 0\n"
        "s_add_u32 m0, s35, 0x8400\n"
        "s_add_u32 s37, s60, 0x2000\n"
        "buffer_load_dwordx4 v4, s[24:27], s37 offen lds\n"
        "ds_read_b64_tr_b16 v[32:33], v12 offset:8192\n"
        "ds_read_b64_tr_b16 v[34:35], v12 offset:8704\n"
        "s_waitcnt lgkmcnt(10)\n"
        "v_mfma_f32_16x16x32_bf16 v[76:79], v[44:47], v[128:131], 0\n"
        "v_mfma_f32_16x16x32_bf16 v[120:123], v[128:131], v[128:131], 0\n"
        "buffer_store_dwordx4 v[92:95], v1, s[16:19], s63 offen offset:0 sc1\n"
        "ds_read_b64_tr_b16 v[36:37], v13 offset:8192\n"
        "ds_read_b64_tr_b16 v[38:39], v13 offset:8704\n"
        "s_waitcnt lgkmcnt(10)\n"
        "v_mfma_f32_16x16x32_bf16 v[64:67], v[48:51], v[132:135], v[64:67]\n"
        "v_mfma_f32_16x16x32_bf16 v[96:99], v[48:51], v[24:27], v[96:99]\n"
        "s_add_u32 m0, s39, 0x2400\n"
        "s_nop 0\n"
        "buffer_load_dwordx4 v29, s[20:23], s61 offen lds\n"
        "ds_read_b128 v[84:87], v8 offset:0\n"
        "ds_read_b64_tr_b16 v[40:41], v11 offset:0\n"
        "ds_read_b64_tr_b16 v[42:43], v11 offset:128\n"
        "s_waitcnt lgkmcnt(11)\n"
        "v_mfma_f32_16x16x32_bf16 v[68:71], v[52:55], v[132:135], v[68:71]\n"
        "v_mfma_f32_16x16x32_bf16 v[116:119], v[24:27], v[132:135], v[116:119]\n"
        "ds_read_b64_tr_b16 v[44:45], v14 offset:8192\n"
        "ds_read_b64_tr_b16 v[46:47], v14 offset:8704\n"
        "s_waitcnt lgkmcnt(11)\n"
        "v_mfma_f32_16x16x32_bf16 v[72:75], v[56:59], v[132:135], v[72:75]\n"
        "s_add_u32 m0, s35, 0x8800\n"
        "s_add_u32 s37, s60, 0x4000\n"
        "buffer_load_dwordx4 v3, s[24:27], s37 offen lds\n"
        "ds_read_b64_tr_b16 v[48:49], v15 offset:8192\n"
        "ds_read_b64_tr_b16 v[50:51], v15 offset:8704\n"
        "s_waitcnt lgkmcnt(11)\n"
        "v_mfma_f32_16x16x32_bf16 v[76:79], v[60:63], v[132:135], v[76:79]\n"
        "v_mfma_f32_16x16x32_bf16 v[120:123], v[132:135], v[132:135], v[120:123]\n"
        "buffer_store_dwordx4 v[92:95], v1, s[16:19], s63 offen offset:1024 sc1\n"
        "ds_read_b64_tr_b16 v[52:53], v12 offset:12288\n"
        "ds_read_b64_tr_b16 v[54:55], v12 offset:12800\n"
        "s_waitcnt lgkmcnt(11)\n"
        "v_mfma_f32_16x16x32_bf16 v[64:67], v[32:35], v[136:139], v[64:67]\n"
        "v_mfma_f32_16x16x32_bf16 v[96:99], v[32:35], v[24:27], v[96:99]\n"
        "s_add_u32 m0, s39, 0x2800\n"
        "s_nop 0\n"
        "buffer_load_dwordx4 v30, s[20:23], s61 offen lds\n"
        "ds_read_b64_tr_b16 v[56:57], v13 offset:12288\n"
        "ds_read_b64_tr_b16 v[58:59], v13 offset:12800\n"
        "s_waitcnt lgkmcnt(11)\n"
        "v_mfma_f32_16x16x32_bf16 v[68:71], v[36:39], v[136:139], v[68:71]\n"
        "v_mfma_f32_16x16x32_bf16 v[116:119], v[24:27], v[136:139], v[116:119]\n"
        "ds_read_b64_tr_b16 v[60:61], v14 offset:12288\n"
        "ds_read_b64_tr_b16 v[62:63], v14 offset:12800\n"
        "s_waitcnt lgkmcnt(10)\n"
        "v_mfma_f32_16x16x32_bf16 v[80:83], v[40:43], v[84:87], 0\n"
        "v_mfma_f32_16x16x32_bf16 v[112:115], v[40:43], v[24:27], 0\n"
        "s_add_u32 m0, s35, 0x8c00\n"
        "s_add_u32 s37, s60, 0x6000\n"
        "buffer_load_dwordx4 v4, s[24:27], s37 offen lds\n"
        "ds_read_b64_tr_b16 v[32:33], v15 offset:12288\n"
        "ds_read_b64_tr_b16 v[34:35], v15 offset:12800\n"
        "s_waitcnt lgkmcnt(10)\n"
        "v_mfma_f32_16x16x32_bf16 v[72:75], v[44:47], v[136:139], v[72:75]\n"
        "s_waitcnt vmcnt(9)\n"
        "s_barrier\n"
        "v_xor_b32 v5, 0, v2\n"
        "ds_read_b128 v[144:147], v5 offset:4096\n"
        "v_xor_b32 v5, 64, v2\n"
        "ds_read_b128 v[148:151], v5 offset:4096\n"
        "v_xor_b32 v5, 128, v2\n"
        "ds_read_b128 v[152:155], v5 offset:4096\n"
        "v_xor_b32 v5, 192, v2\n"
        "ds_read_b128 v[156:159], v5 offset:4096\n"
        "ds_read_b64_tr_b16 v[36:37], v12 offset:16384\n"
        "ds_read_b64_tr_b16 v[38:39], v12 offset:16896\n"
        "s_waitcnt lgkmcnt(14)\n"
        "v_mfma_f32_16x16x32_bf16 v[76:79], v[48:51], v[136:139], v[76:79]\n"
        "v_mfma_f32_16x16x32_bf16 v[120:123], v[136:139], v[136:139], v[120:123]\n"
        "s_add_u32 m0, s39, 0x2c00\n"
        "s_nop 0\n"
        "buffer_load_dwordx4 v31, s[20:23], s61 offen lds\n"
        "ds_read_b64_tr_b16 v[40:41], v13 offset:16384\n"
        "ds_read_b64_tr_b16 v[42:43], v13 offset:16896\n"
        "s_waitcnt lgkmcnt(14)\n"
        "v_mfma_f32_16x16x32_bf16 v[64:67], v[52:55], v[140:143], v[64:67]\n"
        "v_mfma_f32_16x16x32_bf16 v[96:99], v[52:55], v[24:27], v[96:99]\n"
        "ds_read_b64_tr_b16 v[44:45], v14 offset:16384\n"
        "ds_read_b64_tr_b16 v[46:47], v14 offset:16896\n"
        "s_waitcnt lgkmcnt(14)\n"
        "v_mfma_f32_16x16x32_bf16 v[68:71], v[56:59], v[140:143], v[68:71]\n"
        "v_mfma_f32_16x16x32_bf16 v[116:119], v[24:27], v[140:143], v[116:119]\n"
        "s_add_u32 m0, s36, 0x12000\n"
        "s_nop 0\n"
        "buffer_load_dwordx4 v1, s[52:55], s62 offen lds\n"
        "ds_read_b64_tr_b16 v[48:49], v15 offset:16384\n"
        "ds_read_b64_tr_b16 v[50:51], v15 offset:16896\n"
        "s_waitcnt lgkmcnt(14)\n"
        "v_mfma_f32_16x16x32_bf16 v[72:75], v[60:63], v[140:143], v[72:75]\n"
        "ds_read_b64_tr_b16 v[52:53], v12 offset:20480\n"
        "ds_read_b64_tr_b16 v[54:55], v12 offset:20992\n"
        "s_waitcnt lgkmcnt(14)\n"
        "v_mfma_f32_16x16x32_bf16 v[76:79], v[32:35], v[140:143], v[76:79]\n"
        "v_mfma_f32_16x16x32_bf16 v[120:123], v[140:143], v[140:143], v[120:123]\n"
        "ds_read_b64_tr_b16 v[56:57], v13 offset:20480\n"
        "ds_read_b64_tr_b16 v[58:59], v13 offset:20992\n"
        "s_waitcnt lgkmcnt(10)\n"
        "v_mfma_f32_16x16x32_bf16 v[64:67], v[36:39], v[144:147], v[64:67]\n"
        "v_mfma_f32_16x16x32_bf16 v[96:99], v[36:39], v[24:27], v[96:99]\n"
        "s_add_u32 s48, s49, 3\n"
        "s_and_b32 s48, s48, 3\n"
        "s_lshl_b32 s60, s48, 17\n"
        "s_add_u32 s60, s60, s34\n"
        "s_lshl_b32 s61, s48, 8\n"
        "s_add_u32 s61, s61, s33\n"
        "s_lshl_b32 s62, s48, 12\n"
        "s_add_u32 s62, s62, s36\n"
        "s_add_u32 m0, s35, 0xc000\n"
        "s_add_u32 s37, s60, 0x0\n"
        "buffer_load_dwordx4 v3, s[24:27], s37 offen lds\n"
        "ds_read_b64_tr_b16 v[60:61], v14 offset:20480\n"
        "ds_read_b64_tr_b16 v[62:63], v14 offset:20992\n"
        "s_waitcnt lgkmcnt(10)\n"
        "v_mfma_f32_16x16x32_bf16 v[68:71], v[40:43], v[144:147], v[68:71]\n"
        "v_mfma_f32_16x16x32_bf16 v[116:119], v[24:27], v[144:147], v[116:119]\n"
        "s_add_u32 m0, s39, 0x3000\n"
        "s_nop 0\n"
        "buffer_load_dwordx4 v28, s[20:23], s61 offen lds\n"
        "ds_read_b64_tr_b16 v[32:33], v15 offset:20480\n"
        "ds_read_b64_tr_b16 v[34:35], v15 offset:20992\n"
        "s_waitcnt lgkmcnt(10)\n"
        "v_mfma_f32_16x16x32_bf16 v[72:75], v[44:47], v[144:147], v[72:75]\n"
        "ds_read_b64_tr_b16 v[36:37], v12 offset:24576\n"
        "ds_read_b64_tr_b16 v[38:39], v12 offset:25088\n"
        "s_waitcnt lgkmcnt(10)\n"
        "v_mfma_f32_16x16x32_bf16 v[76:79], v[48:51], v[144:147], v[76:79]\n"
        "v_mfma_f32_16x16x32_bf16 v[120:123], v[144:147], v[144:147], v[120:123]\n"
        "s_add_u32 m0, s35, 0xc400\n"
        "s_add_u32 s37, s60, 0x2000\n"
        "buffer_load_dwordx4 v4, s[24:27], s37 offen lds\n"
        "ds_read_b64_tr_b16 v[40:41], v13 offset:24576\n"
        "ds_read_b64_tr_b16 v[42:43], v13 offset:25088\n"
        "s_waitcnt lgkmcnt(10)\n"
        "v_mfma_f32_16x16x32_bf16 v[64:67], v[52:55], v[148:151], v[64:67]\n"
        "v_mfma_f32_16x16x32_bf16 v[96:99], v[52:55], v[24:27], v[96:99]\n"
        "ds_read_b128 v[84:87], v8 offset:4096\n"
        "ds_read_b64_tr_b16 v[44:45], v11 offset:4096\n"
        "ds_read_b64_tr_b16 v[46:47], v11 offset:4224\n"
        "s_waitcnt lgkmcnt(11)\n"
        "v_mfma_f32_16x16x32_bf16 v[68:71], v[56:59], v[148:151], v[68:71]\n"
        "v_mfma_f32_16x16x32_bf16 v[116:119], v[24:27], v[148:151], v[116:119]\n"
        "s_add_u32 m0, s39, 0x3400\n"
        "s_nop 0\n"
        "buffer_load_dwordx4 v29, s[20:23], s61 offen lds\n"
        "ds_read_b64_tr_b16 v[48:49], v14 offset:24576\n"
        "ds_read_b64_tr_b16 v[50:51], v14 offset:25088\n"
        "s_waitcnt lgkmcnt(11)\n"
        "v_mfma_f32_16x16x32_bf16 v[72:75], v[60:63], v[148:151], v[72:75]\n"
        "ds_read_b64_tr_b16 v[52:53], v15 offset:24576\n"
        "ds_read_b64_tr_b16 v[54:55], v15 offset:25088\n"
        "s_waitcnt lgkmcnt(11)\n"
        "v_mfma_f32_16x16x32_bf16 v[76:79], v[32:35], v[148:151], v[76:79]\n"
        "v_mfma_f32_16x16x32_bf16 v[120:123], v[148:151], v[148:151], v[120:123]\n"
        "s_add_u32 m0, s35, 0xc800\n"
        "s_add_u32 s37, s60, 0x4000\n"
        "buffer_load_dwordx4 v3, s[24:27], s37 offen lds\n"
        "ds_read_b64_tr_b16 v[56:57], v12 offset:28672\n"
        "ds_read_b64_tr_b16 v[58:59], v12 offset:29184\n"
        "s_waitcnt lgkmcnt(11)\n"
        "v_mfma_f32_16x16x32_bf16 v[64:67], v[36:39], v[152:155], v[64:67]\n"
        "v_mfma_f32_16x16x32_bf16 v[96:99], v[36:39], v[24:27], v[96:99]\n"
        "ds_read_b64_tr_b16 v[60:61], v13 offset:28672\n"
        "ds_read_b64_tr_b16 v[62:63], v13 offset:29184\n"
        "s_waitcnt lgkmcnt(11)\n"
        "v_mfma_f32_16x16x32_bf16 v[68:71], v[40:43], v[152:155], v[68:71]\n"
        "v_mfma_f32_16x16x32_bf16 v[116:119], v[24:27], v[152:155], v[116:119]\n"
        "s_add_u32 m0, s39, 0x3800\n"
        "s_nop 0\n"
        "buffer_load_dwordx4 v30, s[20:23], s61 offen lds\n"
        "ds_read_b64_tr_b16 v[32:33], v14 offset:28672\n"
        "ds_read_b64_tr_b16 v[34:35], v14 offset:29184\n"
        "s_waitcnt lgkmcnt(10)\n"
        "v_mfma_f32_16x16x32_bf16 v[80:83], v[44:47], v[84:87], v[80:83]\n"
        "v_mfma_f32_16x16x32_bf16 v[112:115], v[44:47], v[24:27], v[112:115]\n"
        "ds_read_b64_tr_b16 v[36:37], v15 offset:28672\n"
        "ds_read_b64_tr_b16 v[38:39], v15 offset:29184\n"
        "s_waitcnt lgkmcnt(10)\n"
        "v_mfma_f32_16x16x32_bf16 v[72:75], v[48:51], v[152:155], v[72:75]\n"
        "s_add_u32 m0, s35, 0xcc00\n"
        "s_add_u32 s37, s60, 0x6000\n"
        "buffer_load_dwordx4 v4, s[24:27], s37 offen lds\n"
        "v_perm_b32 v224, v196, v192, v9\n"
        "v_perm_b32 v228, v196, v192, v10\n"
        "v_perm_b32 v225, v204, v200, v9\n"
        "v_perm_b32 v229, v204, v200, v10\n"
        "v_perm_b32 v226, v212, v208, v9\n"
        "v_perm_b32 v230, v212, v208, v10\n"
        "v_perm_b32 v227, v220, v216, v9\n"
        "v_perm_b32 v231, v220, v216, v10\n"
        "v_perm_b32 v232, v197, v193, v9\n"
        "v_perm_b32 v236, v197, v193, v10\n"
        "v_perm_b32 v233, v205, v201, v9\n"
        "v_perm_b32 v237, v205, v201, v10\n"
        "v_perm_b32 v234, v213, v209, v9\n"
        "v_perm_b32 v238, v213, v209, v10\n"
        "v_perm_b32 v235, v221, v217, v9\n"
        "v_perm_b32 v239, v221, v217, v10\n"
        "v_perm_b32 v240, v198, v194, v9\n"
        "v_perm_b32 v244, v198, v194, v10\n"
        "v_perm_b32 v241, v206, v202, v9\n"
        "v_perm_b32 v245, v206, v202, v10\n"
        "v_perm_b32 v242, v214, v210, v9\n"
        "v_perm_b32 v246, v214, v210, v10\n"
        "v_perm_b32 v243, v222, v218, v9\n"
        "v_perm_b32 v247, v222, v218, v10\n"
        "v_perm_b32 v248, v199, v195, v9\n"
        "v_perm_b32 v252, v199, v195, v10\n"
        "v_perm_b32 v249, v207, v203, v9\n"
        "v_perm_b32 v253, v207, v203, v10\n"
        "v_perm_b32 v250, v215, v211, v9\n"
        "v_perm_b32 v254, v215, v211, v10\n"
        "v_perm_b32 v251, v223, v219, v9\n"
        "v_perm_b32 v255, v223, v219, v10\n"
        "s_mov_b32 s46, 0xcccccccc\n"
        "s_mov_b32 s47, 0xcccccccc\n"
        "s_mov_b64 exec, s[46:47]\n"
        "v_swap_b32 v224, v232\n"
        "v_swap_b32 v225, v233\n"
        "v_swap_b32 v226, v234\n"
        "v_swap_b32 v227, v235\n"
        "v_swap_b32 v228, v236\n"
        "v_swap_b32 v229, v237\n"
        "v_swap_b32 v230, v238\n"
        "v_swap_b32 v231, v239\n"
        "v_swap_b32 v240, v248\n"
        "v_swap_b32 v241, v249\n"
        "v_swap_b32 v242, v250\n"
        "v_swap_b32 v243, v251\n"
        "v_swap_b32 v244, v252\n"
        "v_swap_b32 v245, v253\n"
        "v_swap_b32 v246, v254\n"
        "v_swap_b32 v247, v255\n"
        "s_mov_b64 exec, -1\n"
        "v_mov_b32_dpp v228, v228 quad_perm:[1,0,3,2] row_mask:0xf bank_mask:0xf\n"
        "v_mov_b32_dpp v229, v229 quad_perm:[1,0,3,2] row_mask:0xf bank_mask:0xf\n"
        "v_mov_b32_dpp v230, v230 quad_perm:[1,0,3,2] row_mask:0xf bank_mask:0xf\n"
        "v_mov_b32_dpp v231, v231 quad_perm:[1,0,3,2] row_mask:0xf bank_mask:0xf\n"
        "v_mov_b32_dpp v244, v244 quad_perm:[1,0,3,2] row_mask:0xf bank_mask:0xf\n"
        "v_mov_b32_dpp v245, v245 quad_perm:[1,0,3,2] row_mask:0xf bank_mask:0xf\n"
        "v_mov_b32_dpp v246, v246 quad_perm:[1,0,3,2] row_mask:0xf bank_mask:0xf\n"
        "v_mov_b32_dpp v247, v247 quad_perm:[1,0,3,2] row_mask:0xf bank_mask:0xf\n"
        "v_mov_b32_dpp v232, v232 quad_perm:[2,3,0,1] row_mask:0xf bank_mask:0xf\n"
        "v_mov_b32_dpp v233, v233 quad_perm:[2,3,0,1] row_mask:0xf bank_mask:0xf\n"
        "v_mov_b32_dpp v234, v234 quad_perm:[2,3,0,1] row_mask:0xf bank_mask:0xf\n"
        "v_mov_b32_dpp v235, v235 quad_perm:[2,3,0,1] row_mask:0xf bank_mask:0xf\n"
        "v_mov_b32_dpp v248, v248 quad_perm:[2,3,0,1] row_mask:0xf bank_mask:0xf\n"
        "v_mov_b32_dpp v249, v249 quad_perm:[2,3,0,1] row_mask:0xf bank_mask:0xf\n"
        "v_mov_b32_dpp v250, v250 quad_perm:[2,3,0,1] row_mask:0xf bank_mask:0xf\n"
        "v_mov_b32_dpp v251, v251 quad_perm:[2,3,0,1] row_mask:0xf bank_mask:0xf\n"
        "v_mov_b32_dpp v236, v236 quad_perm:[3,2,1,0] row_mask:0xf bank_mask:0xf\n"
        "v_mov_b32_dpp v237, v237 quad_perm:[3,2,1,0] row_mask:0xf bank_mask:0xf\n"
        "v_mov_b32_dpp v238, v238 quad_perm:[3,2,1,0] row_mask:0xf bank_mask:0xf\n"
        "v_mov_b32_dpp v239, v239 quad_perm:[3,2,1,0] row_mask:0xf bank_mask:0xf\n"
        "v_mov_b32_dpp v252, v252 quad_perm:[3,2,1,0] row_mask:0xf bank_mask:0xf\n"
        "v_mov_b32_dpp v253, v253 quad_perm:[3,2,1,0] row_mask:0xf bank_mask:0xf\n"
        "v_mov_b32_dpp v254, v254 quad_perm:[3,2,1,0] row_mask:0xf bank_mask:0xf\n"
        "v_mov_b32_dpp v255, v255 quad_perm:[3,2,1,0] row_mask:0xf bank_mask:0xf\n"
        "s_mov_b64 exec, s[46:47]\n"
        "v_swap_b32 v224, v232\n"
        "v_swap_b32 v225, v233\n"
        "v_swap_b32 v226, v234\n"
        "v_swap_b32 v227, v235\n"
        "v_swap_b32 v228, v236\n"
        "v_swap_b32 v229, v237\n"
        "v_swap_b32 v230, v238\n"
        "v_swap_b32 v231, v239\n"
        "v_swap_b32 v240, v248\n"
        "v_swap_b32 v241, v249\n"
        "v_swap_b32 v242, v250\n"
        "v_swap_b32 v243, v251\n"
        "v_swap_b32 v244, v252\n"
        "v_swap_b32 v245, v253\n"
        "v_swap_b32 v246, v254\n"
        "v_swap_b32 v247, v255\n"
        "s_mov_b32 s46, 0xaaaaaaaa\n"
        "s_mov_b32 s47, 0xaaaaaaaa\n"
        "s_mov_b64 exec, s[46:47]\n"
        "v_swap_b32 v224, v228\n"
        "v_swap_b32 v225, v229\n"
        "v_swap_b32 v226, v230\n"
        "v_swap_b32 v227, v231\n"
        "v_swap_b32 v232, v236\n"
        "v_swap_b32 v233, v237\n"
        "v_swap_b32 v234, v238\n"
        "v_swap_b32 v235, v239\n"
        "v_swap_b32 v240, v244\n"
        "v_swap_b32 v241, v245\n"
        "v_swap_b32 v242, v246\n"
        "v_swap_b32 v243, v247\n"
        "v_swap_b32 v248, v252\n"
        "v_swap_b32 v249, v253\n"
        "v_swap_b32 v250, v254\n"
        "v_swap_b32 v251, v255\n"
        "s_mov_b64 exec, -1\n"
        "v_and_b32 v5, 3, v0\n"
        "v_lshlrev_b32 v5, 4, v5\n"
        "s_nop 1\n"
        "v_add_u32_dpp v192, v7, v5 quad_perm:[0,0,0,0] row_mask:0xf bank_mask:0xf\n"
        "v_add_u32_dpp v193, v7, v5 quad_perm:[1,1,1,1] row_mask:0xf bank_mask:0xf\n"
        "v_add_u32_dpp v194, v7, v5 quad_perm:[2,2,2,2] row_mask:0xf bank_mask:0xf\n"
        "v_add_u32_dpp v195, v7, v5 quad_perm:[3,3,3,3] row_mask:0xf bank_mask:0xf\n"
        "s_cmp_eq_u32 s42, 3\n"
        "s_cselect_b32 s58, 0, s18\n"
        "s_mov_b64 s[56:57], s[16:17]\n"
        "s_movk_i32 s43, 128\n"
        "s_cmp_eq_u32 s42, 1\n"
        "s_cselect_b32 s43, 1024, s43\n"
        "s_cmp_eq_u32 s42, 2\n"
        "s_cselect_b32 s43, 64, s43\n"
        "s_waitcnt vmcnt(7)\n"
        "s_barrier\n"
        "v_xor_b32 v5, 0, v2\n"
        "ds_read_b128 v[160:163], v5 offset:8192\n"
        "v_xor_b32 v5, 64, v2\n"
        "ds_read_b128 v[164:167], v5 offset:8192\n"
        "v_xor_b32 v5, 128, v2\n"
        "ds_read_b128 v[168:171], v5 offset:8192\n"
        "v_xor_b32 v5, 192, v2\n"
        "ds_read_b128 v[172:175], v5 offset:8192\n"
        "ds_read_b64_tr_b16 v[40:41], v12 offset:32768\n"
        "ds_read_b64_tr_b16 v[42:43], v12 offset:33280\n"
        "s_waitcnt lgkmcnt(14)\n"
        "v_mfma_f32_16x16x32_bf16 v[76:79], v[52:55], v[152:155], v[76:79]\n"
        "v_mfma_f32_16x16x32_bf16 v[120:123], v[152:155], v[152:155], v[120:123]\n"
        "ds_read_b64_tr_b16 v[44:45], v13 offset:32768\n"
        "ds_read_b64_tr_b16 v[46:47], v13 offset:33280\n"
        "s_waitcnt lgkmcnt(14)\n"
        "v_mfma_f32_16x16x32_bf16 v[64:67], v[56:59], v[156:159], v[64:67]\n"
        "v_mfma_f32_16x16x32_bf16 v[96:99], v[56:59], v[24:27], v[96:99]\n"
        "s_add_u32 m0, s39, 0x3c00\n"
        "s_nop 0\n"
        "buffer_load_dwordx4 v31, s[20:23], s61 offen lds\n"
        "ds_read_b64_tr_b16 v[48:49], v14 offset:32768\n"
        "ds_read_b64_tr_b16 v[50:51], v14 offset:33280\n"
        "s_waitcnt lgkmcnt(14)\n"
        "v_mfma_f32_16x16x32_bf16 v[68:71], v[60:63], v[156:159], v[68:71]\n"
        "v_mfma_f32_16x16x32_bf16 v[116:119], v[24:27], v[156:159], v[116:119]\n"
        "buffer_store_dwordx4 v[224:227], v192, s[56:59], 0 offen sc1\n"
        "ds_read_b64_tr_b16 v[52:53], v15 offset:32768\n"
        "ds_read_b64_tr_b16 v[54:55], v15 offset:33280\n"
        "s_waitcnt lgkmcnt(14)\n"
        "v_mfma_f32_16x16x32_bf16 v[72:75], v[32:35], v[156:159], v[72:75]\n"
        "s_add_u32 m0, s36, 0x13000\n"
        "s_nop 0\n"
        "buffer_load_dwordx4 v1, s[52:55], s62 offen lds\n"
        "ds_read_b64_tr_b16 v[56:57], v12 offset:36864\n"
        "ds_read_b64_tr_b16 v[58:59], v12 offset:37376\n"
        "s_waitcnt lgkmcnt(14)\n"
        "v_mfma_f32_16x16x32_bf16 v[76:79], v[36:39], v[156:159], v[76:79]\n"
        "v_mfma_f32_16x16x32_bf16 v[120:123], v[156:159], v[156:159], v[120:123]\n"
        "ds_read_b64_tr_b16 v[60:61], v13 offset:36864\n"
        "ds_read_b64_tr_b16 v[62:63], v13 offset:37376\n"
        "s_waitcnt lgkmcnt(10)\n"
        "v_mfma_f32_16x16x32_bf16 v[64:67], v[40:43], v[160:163], v[64:67]\n"
        "v_mfma_f32_16x16x32_bf16 v[96:99], v[40:43], v[24:27], v[96:99]\n"
        "ds_read_b64_tr_b16 v[32:33], v14 offset:36864\n"
        "ds_read_b64_tr_b16 v[34:35], v14 offset:37376\n"
        "s_waitcnt lgkmcnt(10)\n"
        "v_mfma_f32_16x16x32_bf16 v[68:71], v[44:47], v[160:163], v[68:71]\n"
        "v_mfma_f32_16x16x32_bf16 v[116:119], v[24:27], v[160:163], v[116:119]\n"
        "buffer_store_dwordx4 v[228:231], v193, s[56:59], 0 offen sc1\n"
        "ds_read_b64_tr_b16 v[36:37], v15 offset:36864\n"
        "ds_read_b64_tr_b16 v[38:39], v15 offset:37376\n"
        "s_waitcnt lgkmcnt(10)\n"
        "v_mfma_f32_16x16x32_bf16 v[72:75], v[48:51], v[160:163], v[72:75]\n"
        "ds_read_b64_tr_b16 v[40:41], v12 offset:40960\n"
        "ds_read_b64_tr_b16 v[42:43], v12 offset:41472\n"
        "s_waitcnt lgkmcnt(10)\n"
        "v_mfma_f32_16x16x32_bf16 v[76:79], v[52:55], v[160:163], v[76:79]\n"
        "v_mfma_f32_16x16x32_bf16 v[120:123], v[160:163], v[160:163], v[120:123]\n"
        "ds_read_b64_tr_b16 v[44:45], v13 offset:40960\n"
        "ds_read_b64_tr_b16 v[46:47], v13 offset:41472\n"
        "s_waitcnt lgkmcnt(10)\n"
        "v_mfma_f32_16x16x32_bf16 v[64:67], v[56:59], v[164:167], v[64:67]\n"
        "v_mfma_f32_16x16x32_bf16 v[96:99], v[56:59], v[24:27], v[96:99]\n"
        "ds_read_b128 v[84:87], v8 offset:8192\n"
        "ds_read_b64_tr_b16 v[48:49], v11 offset:8192\n"
        "ds_read_b64_tr_b16 v[50:51], v11 offset:8320\n"
        "s_waitcnt lgkmcnt(11)\n"
        "v_mfma_f32_16x16x32_bf16 v[68:71], v[60:63], v[164:167], v[68:71]\n"
        "v_mfma_f32_16x16x32_bf16 v[116:119], v[24:27], v[164:167], v[116:119]\n"
        "buffer_store_dwordx4 v[232:235], v194, s[56:59], 0 offen sc1\n"
        "ds_read_b64_tr_b16 v[52:53], v14 offset:40960\n"
        "ds_read_b64_tr_b16 v[54:55], v14 offset:41472\n"
        "s_waitcnt lgkmcnt(11)\n"
        "v_mfma_f32_16x16x32_bf16 v[72:75], v[32:35], v[164:167], v[72:75]\n"
        "ds_read_b64_tr_b16 v[56:57], v15 offset:40960\n"
        "ds_read_b64_tr_b16 v[58:59], v15 offset:41472\n"
        "s_waitcnt lgkmcnt(11)\n"
        "v_mfma_f32_16x16x32_bf16 v[76:79], v[36:39], v[164:167], v[76:79]\n"
        "v_mfma_f32_16x16x32_bf16 v[120:123], v[164:167], v[164:167], v[120:123]\n"
        "ds_read_b64_tr_b16 v[60:61], v12 offset:45056\n"
        "ds_read_b64_tr_b16 v[62:63], v12 offset:45568\n"
        "s_waitcnt lgkmcnt(11)\n"
        "v_mfma_f32_16x16x32_bf16 v[64:67], v[40:43], v[168:171], v[64:67]\n"
        "v_mfma_f32_16x16x32_bf16 v[96:99], v[40:43], v[24:27], v[96:99]\n"
        "ds_read_b64_tr_b16 v[32:33], v13 offset:45056\n"
        "ds_read_b64_tr_b16 v[34:35], v13 offset:45568\n"
        "s_waitcnt lgkmcnt(11)\n"
        "v_mfma_f32_16x16x32_bf16 v[68:71], v[44:47], v[168:171], v[68:71]\n"
        "v_mfma_f32_16x16x32_bf16 v[116:119], v[24:27], v[168:171], v[116:119]\n"
        "buffer_store_dwordx4 v[236:239], v195, s[56:59], 0 offen sc1\n"
        "ds_read_b64_tr_b16 v[36:37], v14 offset:45056\n"
        "ds_read_b64_tr_b16 v[38:39], v14 offset:45568\n"
        "s_waitcnt lgkmcnt(10)\n"
        "v_mfma_f32_16x16x32_bf16 v[80:83], v[48:51], v[84:87], v[80:83]\n"
        "v_mfma_f32_16x16x32_bf16 v[112:115], v[48:51], v[24:27], v[112:115]\n"
        "ds_read_b64_tr_b16 v[40:41], v15 offset:45056\n"
        "ds_read_b64_tr_b16 v[42:43], v15 offset:45568\n"
        "s_waitcnt lgkmcnt(10)\n"
        "v_mfma_f32_16x16x32_bf16 v[72:75], v[52:55], v[168:171], v[72:75]\n"
        "s_waitcnt vmcnt(3)\n"
        "s_barrier\n"
        "v_xor_b32 v5, 0, v2\n"
        "ds_read_b128 v[176:179], v5 offset:12288\n"
        "v_xor_b32 v5, 64, v2\n"
        "ds_read_b128 v[180:183], v5 offset:12288\n"
        "v_xor_b32 v5, 128, v2\n"
        "ds_read_b128 v[184:187], v5 offset:12288\n"
        "v_xor_b32 v5, 192, v2\n"
        "ds_read_b128 v[188:191], v5 offset:12288\n"
        "ds_read_b64_tr_b16 v[44:45], v12 offset:49152\n"
        "ds_read_b64_tr_b16 v[46:47], v12 offset:49664\n"
        "s_waitcnt lgkmcnt(14)\n"
        "v_mfma_f32_16x16x32_bf16 v[76:79], v[56:59], v[168:171], v[76:79]\n"
        "v_mfma_f32_16x16x32_bf16 v[120:123], v[168:171], v[168:171], v[120:123]\n"
        "ds_read_b64_tr_b16 v[48:49], v13 offset:49152\n"
        "ds_read_b64_tr_b16 v[50:51], v13 offset:49664\n"
        "s_waitcnt lgkmcnt(14)\n"
        "v_mfma_f32_16x16x32_bf16 v[64:67], v[60:63], v[172:175], v[64:67]\n"
        "v_mfma_f32_16x16x32_bf16 v[96:99], v[60:63], v[24:27], v[96:99]\n"
        "buffer_store_dwordx4 v[240:243], v192, s[56:59], s43 offen sc1\n"
        "ds_read_b64_tr_b16 v[52:53], v14 offset:49152\n"
        "ds_read_b64_tr_b16 v[54:55], v14 offset:49664\n"
        "s_waitcnt lgkmcnt(14)\n"
        "v_mfma_f32_16x16x32_bf16 v[68:71], v[32:35], v[172:175], v[68:71]\n"
        "v_mfma_f32_16x16x32_bf16 v[116:119], v[24:27], v[172:175], v[116:119]\n"
        "ds_read_b64_tr_b16 v[56:57], v15 offset:49152\n"
        "ds_read_b64_tr_b16 v[58:59], v15 offset:49664\n"
        "s_waitcnt lgkmcnt(14)\n"
        "v_mfma_f32_16x16x32_bf16 v[72:75], v[36:39], v[172:175], v[72:75]\n"
        "ds_read_b64_tr_b16 v[60:61], v12 offset:53248\n"
        "ds_read_b64_tr_b16 v[62:63], v12 offset:53760\n"
        "s_waitcnt lgkmcnt(14)\n"
        "v_mfma_f32_16x16x32_bf16 v[76:79], v[40:43], v[172:175], v[76:79]\n"
        "v_mfma_f32_16x16x32_bf16 v[120:123], v[172:175], v[172:175], v[120:123]\n"
        "ds_read_b64_tr_b16 v[32:33], v13 offset:53248\n"
        "ds_read_b64_tr_b16 v[34:35], v13 offset:53760\n"
        "s_waitcnt lgkmcnt(10)\n"
        "v_mfma_f32_16x16x32_bf16 v[64:67], v[44:47], v[176:179], v[64:67]\n"
        "v_mfma_f32_16x16x32_bf16 v[96:99], v[44:47], v[24:27], v[96:99]\n"
        "buffer_store_dwordx4 v[244:247], v193, s[56:59], s43 offen sc1\n"
        "ds_read_b64_tr_b16 v[36:37], v14 offset:53248\n"
        "ds_read_b64_tr_b16 v[38:39], v14 offset:53760\n"
        "s_waitcnt lgkmcnt(10)\n"
        "v_mfma_f32_16x16x32_bf16 v[68:71], v[48:51], v[176:179], v[68:71]\n"
        "v_mfma_f32_16x16x32_bf16 v[116:119], v[24:27], v[176:179], v[116:119]\n"
        "ds_read_b64_tr_b16 v[40:41], v15 offset:53248\n"
        "ds_read_b64_tr_b16 v[42:43], v15 offset:53760\n"
        "s_waitcnt lgkmcnt(10)\n"
        "v_mfma_f32_16x16x32_bf16 v[72:75], v[52:55], v[176:179], v[72:75]\n"
        "ds_read_b64_tr_b16 v[44:45], v12 offset:57344\n"
        "ds_read_b64_tr_b16 v[46:47], v12 offset:57856\n"
        "s_waitcnt lgkmcnt(10)\n"
        "v_mfma_f32_16x16x32_bf16 v[76:79], v[56:59], v[176:179], v[76:79]\n"
        "v_mfma_f32_16x16x32_bf16 v[120:123], v[176:179], v[176:179], v[120:123]\n"
        "ds_read_b64_tr_b16 v[48:49], v13 offset:57344\n"
        "ds_read_b64_tr_b16 v[50:51], v13 offset:57856\n"
        "s_waitcnt lgkmcnt(10)\n"
        "v_mfma_f32_16x16x32_bf16 v[64:67], v[60:63], v[180:183], v[64:67]\n"
        "v_mfma_f32_16x16x32_bf16 v[96:99], v[60:63], v[24:27], v[96:99]\n"
        "buffer_store_dwordx4 v[248:251], v194, s[56:59], s43 offen sc1\n"
        "ds_read_b128 v[84:87], v8 offset:12288\n"
        "ds_read_b64_tr_b16 v[52:53], v11 offset:12288\n"
        "ds_read_b64_tr_b16 v[54:55], v11 offset:12416\n"
        "s_waitcnt lgkmcnt(11)\n"
        "v_mfma_f32_16x16x32_bf16 v[68:71], v[32:35], v[180:183], v[68:71]\n"
        "v_mfma_f32_16x16x32_bf16 v[116:119], v[24:27], v[180:183], v[116:119]\n"
        "ds_read_b64_tr_b16 v[56:57], v14 offset:57344\n"
        "ds_read_b64_tr_b16 v[58:59], v14 offset:57856\n"
        "s_waitcnt lgkmcnt(11)\n"
        "v_mfma_f32_16x16x32_bf16 v[72:75], v[36:39], v[180:183], v[72:75]\n"
        "ds_read_b64_tr_b16 v[60:61], v15 offset:57344\n"
        "ds_read_b64_tr_b16 v[62:63], v15 offset:57856\n"
        "s_waitcnt lgkmcnt(11)\n"
        "v_mfma_f32_16x16x32_bf16 v[76:79], v[40:43], v[180:183], v[76:79]\n"
        "v_mfma_f32_16x16x32_bf16 v[120:123], v[180:183], v[180:183], v[120:123]\n"
        "ds_read_b64_tr_b16 v[32:33], v12 offset:61440\n"
        "ds_read_b64_tr_b16 v[34:35], v12 offset:61952\n"
        "s_waitcnt lgkmcnt(11)\n"
        "v_mfma_f32_16x16x32_bf16 v[64:67], v[44:47], v[184:187], v[64:67]\n"
        "v_mfma_f32_16x16x32_bf16 v[96:99], v[44:47], v[24:27], v[96:99]\n"
        "buffer_store_dwordx4 v[252:255], v195, s[56:59], s43 offen sc1\n"
        "ds_read_b64_tr_b16 v[36:37], v13 offset:61440\n"
        "ds_read_b64_tr_b16 v[38:39], v13 offset:61952\n"
        "s_waitcnt lgkmcnt(11)\n"
        "v_mfma_f32_16x16x32_bf16 v[68:71], v[48:51], v[184:187], v[68:71]\n"
        "v_mfma_f32_16x16x32_bf16 v[116:119], v[24:27], v[184:187], v[116:119]\n"
        "ds_read_b64_tr_b16 v[40:41], v14 offset:61440\n"
        "ds_read_b64_tr_b16 v[42:43], v14 offset:61952\n"
        "s_waitcnt lgkmcnt(10)\n"
        "v_mfma_f32_16x16x32_bf16 v[80:83], v[52:55], v[84:87], v[80:83]\n"
        "v_mfma_f32_16x16x32_bf16 v[112:115], v[52:55], v[24:27], v[112:115]\n"
        "ds_read_b64_tr_b16 v[44:45], v15 offset:61440\n"
        "ds_read_b64_tr_b16 v[46:47], v15 offset:61952\n"
        "s_waitcnt lgkmcnt(10)\n"
        "v_mfma_f32_16x16x32_bf16 v[72:75], v[56:59], v[184:187], v[72:75]\n"
        "s_waitcnt lgkmcnt(8)\n"
        "v_mfma_f32_16x16x32_bf16 v[76:79], v[60:63], v[184:187], v[76:79]\n"
        "v_mfma_f32_16x16x32_bf16 v[120:123], v[184:187], v[184:187], v[120:123]\n"
        "s_waitcnt lgkmcnt(6)\n"
        "v_mfma_f32_16x16x32_bf16 v[64:67], v[32:35], v[188:191], v[64:67]\n"
        "v_mfma_f32_16x16x32_bf16 v[96:99], v[32:35], v[24:27], v[96:99]\n"
        "s_waitcnt lgkmcnt(4)\n"
        "v_mfma_f32_16x16x32_bf16 v[68:71], v[36:39], v[188:191], v[68:71]\n"
        "v_mfma_f32_16x16x32_bf16 v[116:119], v[24:27], v[188:191], v[116:119]\n"
        "s_waitcnt lgkmcnt(2)\n"
        "v_mfma_f32_16x16x32_bf16 v[72:75], v[40:43], v[188:191], v[72:75]\n"
        "s_waitcnt lgkmcnt(0)\n"
        "v_mfma_f32_16x16x32_bf16 v[76:79], v[44:47], v[188:191], v[76:79]\n"
        "v_mfma_f32_16x16x32_bf16 v[120:123], v[188:191], v[188:191], v[120:123]\n"
        "v_lshlrev_b32 v5, 4, v17\n"
        "s_lshl_b32 s37, s28, 6\n"
        "s_add_u32 s37, s37, 0x24000\n"
        "v_add_u32 v5, s37, v5\n"
        "s_mov_b32 s46, 0x00010001\n"
        "s_mov_b32 s47, 0x00010001\n"
        "s_mov_b64 exec, s[46:47]\n"
        "ds_write_b128 v5, v[96:99]\n"
        "s_cmp_lg_u32 s50, 0\n"
        "s_cbranch_scc1 L_F_cs\n"
        "v_lshlrev_b32 v28, 4, v17\n"
        "s_lshl_b32 s38, s51, 8\n"
        "s_lshl_b32 s37, s28, 6\n"
        "s_add_u32 s38, s38, s37\n"
        "s_add_u32 s38, s38, 0x40000\n"
        "buffer_store_dwordx4 v[96:99], v28, s[16:19], s38 offen\n"
        "L_F_cs:\n"
        "s_mov_b64 exec, -1\n"
        "s_lshl_b32 s37, s28, 11\n"
        "s_add_u32 s37, s37, 0x24400\n"
        "v_add_u32 v11, s37, v1\n"
        "v_add_u32 v12, 0x24400, v1\n"
        "ds_write_b128 v11, v[80:83]\n"
        "ds_write_b128 v11, v[112:115] offset:1024\n"
        "v_xor_b32 v6, 64, v5\n"
        "v_xor_b32 v7, 0x80, v5\n"
        "v_xor_b32 v8, 0xc0, v5\n"
        "s_waitcnt lgkmcnt(0)\n"
        "s_barrier\n"
        "ds_read_b128 v[100:103], v6\n"
        "ds_read_b128 v[104:107], v7\n"
        "ds_read_b128 v[108:111], v8\n"
        "ds_read_b128 v[32:35], v12 offset:0\n"
        "ds_read_b128 v[36:39], v12 offset:1024\n"
        "ds_read_b128 v[40:43], v12 offset:2048\n"
        "ds_read_b128 v[44:47], v12 offset:3072\n"
        "ds_read_b128 v[48:51], v12 offset:4096\n"
        "ds_read_b128 v[52:55], v12 offset:5120\n"
        "ds_read_b128 v[56:59], v12 offset:6144\n"
        "ds_read_b128 v[60:63], v12 offset:7168\n"
        "v_mov_b32 v21, 0\n"
        "s_mov_b32 s46, 0x100001\n"
        "s_mov_b32 s47, 0x10000100\n"
        "s_mov_b64 exec, s[46:47]\n"
        "v_mov_b32 v21, v120\n"
        "s_mov_b32 s46, 0x200002\n"
        "s_mov_b32 s47, 0x20000200\n"
        "s_mov_b64 exec, s[46:47]\n"
        "v_mov_b32 v21, v121\n"
        "s_mov_b32 s46, 0x400004\n"
        "s_mov_b32 s47, 0x40000400\n"
        "s_mov_b64 exec, s[46:47]\n"
        "v_mov_b32 v21, v122\n"
        "s_mov_b32 s46, 0x800008\n"
        "s_mov_b32 s47, 0x80000800\n"
        "s_mov_b64 exec, s[46:47]\n"
        "v_mov_b32 v21, v123\n"
        "s_mov_b64 exec, -1\n"
        "v_mov_b32 v29, v21\n"
        "s_nop 1\n"
        "v_permlane16_swap_b32 v21, v29\n"
        "v_add_f32 v21, v21, v29\n"
        "v_mov_b32 v29, v21\n"
        "s_nop 1\n"
        "v_permlane32_swap_b32 v21, v29\n"
        "v_add_f32 v21, v21, v29\n"
        "v_mov_b32 v20, v116\n"
        "v_mul_f32 v20, 0x3b000000, v20\n"
        "v_mul_f32 v21, 0x3b000000, v21\n"
        "v_mul_f32 v28, v20, v20\n"
        "v_sub_f32 v21, v21, v28\n"
        "v_mul_f32 v28, 0x3b800000, v28\n"
        "v_cmp_ge_f32 vcc, v21, v28\n"
        "s_nop 0\n"
        "s_cmp_eq_u64 vcc, -1\n"
        "s_cbranch_scc1 L_F_ok\n"
        "v_mov_b32 v21, 0\n"
        "v_mov_b32 v30, 0\n"
        "v_lshlrev_b32 v28, 16, v128\n"
        "v_and_b32 v29, 0xffff0000, v128\n"
        "v_sub_f32 v28, v28, v20\n"
        "v_sub_f32 v29, v29, v20\n"
        "v_fmac_f32 v21, v28, v28\n"
        "v_fmac_f32 v30, v29, v29\n"
        "v_lshlrev_b32 v28, 16, v129\n"
        "v_and_b32 v29, 0xffff0000, v129\n"
        "v_sub_f32 v28, v28, v20\n"
        "v_sub_f32 v29, v29, v20\n"
        "v_fmac_f32 v21, v28, v28\n"
        "v_fmac_f32 v30, v29, v29\n"
        "v_lshlrev_b32 v28, 16, v130\n"
        "v_and_b32 v29, 0xffff0000, v130\n"
        "v_sub_f32 v28, v28, v20\n"
        "v_sub_f32 v29, v29, v20\n"
        "v_fmac_f32 v21, v28, v28\n"
        "v_fmac_f32 v30, v29, v29\n"
        "v_lshlrev_b32 v28, 16, v131\n"
        "v_and_b32 v29, 0xffff0000, v131\n"
        "v_sub_f32 v28, v28, v20\n"
        "v_sub_f32 v29, v29, v20\n"
        "v_fmac_f32 v21, v28, v28\n"
        "v_fmac_f32 v30, v29, v29\n"
        "v_lshlrev_b32 v28, 16, v132\n"
        "v_and_b32 v29, 0xffff0000, v132\n"
        "v_sub_f32 v28, v28, v20\n"
        "v_sub_f32 v29, v29, v20\n"
        "v_fmac_f32 v21, v28, v28\n"
        "v_fmac_f32 v30, v29, v29\n"
        "v_lshlrev_b32 v28, 16, v133\n"
        "v_and_b32 v29, 0xffff0000, v133\n"
        "v_sub_f32 v28, v28, v20\n"
        "v_sub_f32 v29, v29, v20\n"
        "v_fmac_f32 v21, v28, v28\n"
        "v_fmac_f32 v30, v29, v29\n"
        "v_lshlrev_b32 v28, 16, v134\n"
        "v_and_b32 v29, 0xffff0000, v134\n"
        "v_sub_f32 v28, v28, v20\n"
        "v_sub_f32 v29, v29, v20\n"
        "v_fmac_f32 v21, v28, v28\n"
        "v_fmac_f32 v30, v29, v29\n"
        "v_lshlrev_b32 v28, 16, v135\n"
        "v_and_b32 v29, 0xffff0000, v135\n"
        "v_sub_f32 v28, v28, v20\n"
        "v_sub_f32 v29, v29, v20\n"
        "v_fmac_f32 v21, v28, v28\n"
        "v_fmac_f32 v30, v29, v29\n"
        "v_lshlrev_b32 v28, 16, v136\n"
        "v_and_b32 v29, 0xffff0000, v136\n"
        "v_sub_f32 v28, v28, v20\n"
        "v_sub_f32 v29, v29, v20\n"
        "v_fmac_f32 v21, v28, v28\n"
        "v_fmac_f32 v30, v29, v29\n"
        "v_lshlrev_b32 v28, 16, v137\n"
        "v_and_b32 v29, 0xffff0000, v137\n"
        "v_sub_f32 v28, v28, v20\n"
        "v_sub_f32 v29, v29, v20\n"
        "v_fmac_f32 v21, v28, v28\n"
        "v_fmac_f32 v30, v29, v29\n"
        "v_lshlrev_b32 v28, 16, v138\n"
        "v_and_b32 v29, 0xffff0000, v138\n"
        "v_sub_f32 v28, v28, v20\n"
        "v_sub_f32 v29, v29, v20\n"
        "v_fmac_f32 v21, v28, v28\n"
        "v_fmac_f32 v30, v29, v29\n"
        "v_lshlrev_b32 v28, 16, v139\n"
        "v_and_b32 v29, 0xffff0000, v139\n"
        "v_sub_f32 v28, v28, v20\n"
        "v_sub_f32 v29, v29, v20\n"
        "v_fmac_f32 v21, v28, v28\n"
        "v_fmac_f32 v30, v29, v29\n"
        "v_lshlrev_b32 v28, 16, v140\n"
        "v_and_b32 v29, 0xffff0000, v140\n"
        "v_sub_f32 v28, v28, v20\n"
        "v_sub_f32 v29, v29, v20\n"
        "v_fmac_f32 v21, v28, v28\n"
        "v_fmac_f32 v30, v29, v29\n"
        "v_lshlrev_b32 v28, 16, v141\n"
        "v_and_b32 v29, 0xffff0000, v141\n"
        "v_sub_f32 v28, v28, v20\n"
        "v_sub_f32 v29, v29, v20\n"
        "v_fmac_f32 v21, v28, v28\n"
        "v_fmac_f32 v30, v29, v29\n"
        "v_lshlrev_b32 v28, 16, v142\n"
        "v_and_b32 v29, 0xffff0000, v142\n"
        "v_sub_f32 v28, v28, v20\n"
        "v_sub_f32 v29, v29, v20\n"
        "v_fmac_f32 v21, v28, v28\n"
        "v_fmac_f32 v30, v29, v29\n"
        "v_lshlrev_b32 v28, 16, v143\n"
        "v_and_b32 v29, 0xffff0000, v143\n"
        "v_sub_f32 v28, v28, v20\n"
        "v_sub_f32 v29, v29, v20\n"
        "v_fmac_f32 v21, v28, v28\n"
        "v_fmac_f32 v30, v29, v29\n"
        "v_lshlrev_b32 v28, 16, v144\n"
        "v_and_b32 v29, 0xffff0000, v144\n"
        "v_sub_f32 v28, v28, v20\n"
        "v_sub_f32 v29, v29, v20\n"
        "v_fmac_f32 v21, v28, v28\n"
        "v_fmac_f32 v30, v29, v29\n"
        "v_lshlrev_b32 v28, 16, v145\n"
        "v_and_b32 v29, 0xffff0000, v145\n"
        "v_sub_f32 v28, v28, v20\n"
        "v_sub_f32 v29, v29, v20\n"
        "v_fmac_f32 v21, v28, v28\n"
        "v_fmac_f32 v30, v29, v29\n"
        "v_lshlrev_b32 v28, 16, v146\n"
        "v_and_b32 v29, 0xffff0000, v146\n"
        "v_sub_f32 v28, v28, v20\n"
        "v_sub_f32 v29, v29, v20\n"
        "v_fmac_f32 v21, v28, v28\n"
        "v_fmac_f32 v30, v29, v29\n"
        "v_lshlrev_b32 v28, 16, v147\n"
        "v_and_b32 v29, 0xffff0000, v147\n"
        "v_sub_f32 v28, v28, v20\n"
        "v_sub_f32 v29, v29, v20\n"
        "v_fmac_f32 v21, v28, v28\n"
        "v_fmac_f32 v30, v29, v29\n"
        "v_lshlrev_b32 v28, 16, v148\n"
        "v_and_b32 v29, 0xffff0000, v148\n"
        "v_sub_f32 v28, v28, v20\n"
        "v_sub_f32 v29, v29, v20\n"
        "v_fmac_f32 v21, v28, v28\n"
        "v_fmac_f32 v30, v29, v29\n"
        "v_lshlrev_b32 v28, 16, v149\n"
        "v_and_b32 v29, 0xffff0000, v149\n"
        "v_sub_f32 v28, v28, v20\n"
        "v_sub_f32 v29, v29, v20\n"
        "v_fmac_f32 v21, v28, v28\n"
        "v_fmac_f32 v30, v29, v29\n"
        "v_lshlrev_b32 v28, 16, v150\n"
        "v_and_b32 v29, 0xffff0000, v150\n"
        "v_sub_f32 v28, v28, v20\n"
        "v_sub_f32 v29, v29, v20\n"
        "v_fmac_f32 v21, v28, v28\n"
        "v_fmac_f32 v30, v29, v29\n"
        "v_lshlrev_b32 v28, 16, v151\n"
        "v_and_b32 v29, 0xffff0000, v151\n"
        "v_sub_f32 v28, v28, v20\n"
        "v_sub_f32 v29, v29, v20\n"
        "v_fmac_f32 v21, v28, v28\n"
        "v_fmac_f32 v30, v29, v29\n"
        "v_lshlrev_b32 v28, 16, v152\n"
        "v_and_b32 v29, 0xffff0000, v152\n"
        "v_sub_f32 v28, v28, v20\n"
        "v_sub_f32 v29, v29, v20\n"
        "v_fmac_f32 v21, v28, v28\n"
        "v_fmac_f32 v30, v29, v29\n"
        "v_lshlrev_b32 v28, 16, v153\n"
        "v_and_b32 v29, 0xffff0000, v153\n"
        "v_sub_f32 v28, v28, v20\n"
        "v_sub_f32 v29, v29, v20\n"
        "v_fmac_f32 v21, v28, v28\n"
        "v_fmac_f32 v30, v29, v29\n"
        "v_lshlrev_b32 v28, 16, v154\n"
        "v_and_b32 v29, 0xffff0000, v154\n"
        "v_sub_f32 v28, v28, v20\n"
        "v_sub_f32 v29, v29, v20\n"
        "v_fmac_f32 v21, v28, v28\n"
        "v_fmac_f32 v30, v29, v29\n"
        "v_lshlrev_b32 v28, 16, v155\n"
        "v_and_b32 v29, 0xffff0000, v155\n"
        "v_sub_f32 v28, v28, v20\n"
        "v_sub_f32 v29, v29, v20\n"
        "v_fmac_f32 v21, v28, v28\n"
        "v_fmac_f32 v30, v29, v29\n"
        "v_lshlrev_b32 v28, 16, v156\n"
        "v_and_b32 v29, 0xffff0000, v156\n"
        "v_sub_f32 v28, v28, v20\n"
        "v_sub_f32 v29, v29, v20\n"
        "v_fmac_f32 v21, v28, v28\n"
        "v_fmac_f32 v30, v29, v29\n"
        "v_lshlrev_b32 v28, 16, v157\n"
        "v_and_b32 v29, 0xffff0000, v157\n"
        "v_sub_f32 v28, v28, v20\n"
        "v_sub_f32 v29, v29, v20\n"
        "v_fmac_f32 v21, v28, v28\n"
        "v_fmac_f32 v30, v29, v29\n"
        "v_lshlrev_b32 v28, 16, v158\n"
        "v_and_b32 v29, 0xffff0000, v158\n"
        "v_sub_f32 v28, v28, v20\n"
        "v_sub_f32 v29, v29, v20\n"
        "v_fmac_f32 v21, v28, v28\n"
        "v_fmac_f32 v30, v29, v29\n"
        "v_lshlrev_b32 v28, 16, v159\n"
        "v_and_b32 v29, 0xffff0000, v159\n"
        "v_sub_f32 v28, v28, v20\n"
        "v_sub_f32 v29, v29, v20\n"
        "v_fmac_f32 v21, v28, v28\n"
        "v_fmac_f32 v30, v29, v29\n"
        "v_lshlrev_b32 v28, 16, v160\n"
        "v_and_b32 v29, 0xffff0000, v160\n"
        "v_sub_f32 v28, v28, v20\n"
        "v_sub_f32 v29, v29, v20\n"
        "v_fmac_f32 v21, v28, v28\n"
        "v_fmac_f32 v30, v29, v29\n"
        "v_lshlrev_b32 v28, 16, v161\n"
        "v_and_b32 v29, 0xffff0000, v161\n"
        "v_sub_f32 v28, v28, v20\n"
        "v_sub_f32 v29, v29, v20\n"
        "v_fmac_f32 v21, v28, v28\n"
        "v_fmac_f32 v30, v29, v29\n"
        "v_lshlrev_b32 v28, 16, v162\n"
        "v_and_b32 v29, 0xffff0000, v162\n"
        "v_sub_f32 v28, v28, v20\n"
        "v_sub_f32 v29, v29, v20\n"
        "v_fmac_f32 v21, v28, v28\n"
        "v_fmac_f32 v30, v29, v29\n"
        "v_lshlrev_b32 v28, 16, v163\n"
        "v_and_b32 v29, 0xffff0000, v163\n"
        "v_sub_f32 v28, v28, v20\n"
        "v_sub_f32 v29, v29, v20\n"
        "v_fmac_f32 v21, v28, v28\n"
        "v_fmac_f32 v30, v29, v29\n"
        "v_lshlrev_b32 v28, 16, v164\n"
        "v_and_b32 v29, 0xffff0000, v164\n"
        "v_sub_f32 v28, v28, v20\n"
        "v_sub_f32 v29, v29, v20\n"
        "v_fmac_f32 v21, v28, v28\n"
        "v_fmac_f32 v30, v29, v29\n"
        "v_lshlrev_b32 v28, 16, v165\n"
        "v_and_b32 v29, 0xffff0000, v165\n"
        "v_sub_f32 v28, v28, v20\n"
        "v_sub_f32 v29, v29, v20\n"
        "v_fmac_f32 v21, v28, v28\n"
        "v_fmac_f32 v30, v29, v29\n"
        "v_lshlrev_b32 v28, 16, v166\n"
        "v_and_b32 v29, 0xffff0000, v166\n"
        "v_sub_f32 v28, v28, v20\n"
        "v_sub_f32 v29, v29, v20\n"
        "v_fmac_f32 v21, v28, v28\n"
        "v_fmac_f32 v30, v29, v29\n"
        "v_lshlrev_b32 v28, 16, v167\n"
        "v_and_b32 v29, 0xffff0000, v167\n"
        "v_sub_f32 v28, v28, v20\n"
        "v_sub_f32 v29, v29, v20\n"
        "v_fmac_f32 v21, v28, v28\n"
        "v_fmac_f32 v30, v29, v29\n"
        "v_lshlrev_b32 v28, 16, v168\n"
        "v_and_b32 v29, 0xffff0000, v168\n"
        "v_sub_f32 v28, v28, v20\n"
        "v_sub_f32 v29, v29, v20\n"
        "v_fmac_f32 v21, v28, v28\n"
        "v_fmac_f32 v30, v29, v29\n"
        "v_lshlrev_b32 v28, 16, v169\n"
        "v_and_b32 v29, 0xffff0000, v169\n"
        "v_sub_f32 v28, v28, v20\n"
        "v_sub_f32 v29, v29, v20\n"
        "v_fmac_f32 v21, v28, v28\n"
        "v_fmac_f32 v30, v29, v29\n"
        "v_lshlrev_b32 v28, 16, v170\n"
        "v_and_b32 v29, 0xffff0000, v170\n"
        "v_sub_f32 v28, v28, v20\n"
        "v_sub_f32 v29, v29, v20\n"
        "v_fmac_f32 v21, v28, v28\n"
        "v_fmac_f32 v30, v29, v29\n"
        "v_lshlrev_b32 v28, 16, v171\n"
        "v_and_b32 v29, 0xffff0000, v171\n"
        "v_sub_f32 v28, v28, v20\n"
        "v_sub_f32 v29, v29, v20\n"
        "v_fmac_f32 v21, v28, v28\n"
        "v_fmac_f32 v30, v29, v29\n"
        "v_lshlrev_b32 v28, 16, v172\n"
        "v_and_b32 v29, 0xffff0000, v172\n"
        "v_sub_f32 v28, v28, v20\n"
        "v_sub_f32 v29, v29, v20\n"
        "v_fmac_f32 v21, v28, v28\n"
        "v_fmac_f32 v30, v29, v29\n"
        "v_lshlrev_b32 v28, 16, v173\n"
        "v_and_b32 v29, 0xffff0000, v173\n"
        "v_sub_f32 v28, v28, v20\n"
        "v_sub_f32 v29, v29, v20\n"
        "v_fmac_f32 v21, v28, v28\n"
        "v_fmac_f32 v30, v29, v29\n"
        "v_lshlrev_b32 v28, 16, v174\n"
        "v_and_b32 v29, 0xffff0000, v174\n"
        "v_sub_f32 v28, v28, v20\n"
        "v_sub_f32 v29, v29, v20\n"
        "v_fmac_f32 v21, v28, v28\n"
        "v_fmac_f32 v30, v29, v29\n"
        "v_lshlrev_b32 v28, 16, v175\n"
        "v_and_b32 v29, 0xffff0000, v175\n"
        "v_sub_f32 v28, v28, v20\n"
        "v_sub_f32 v29, v29, v20\n"
        "v_fmac_f32 v21, v28, v28\n"
        "v_fmac_f32 v30, v29, v29\n"
        "v_lshlrev_b32 v28, 16, v176\n"
        "v_and_b32 v29, 0xffff0000, v176\n"
        "v_sub_f32 v28, v28, v20\n"
        "v_sub_f32 v29, v29, v20\n"
        "v_fmac_f32 v21, v28, v28\n"
        "v_fmac_f32 v30, v29, v29\n"
        "v_lshlrev_b32 v28, 16, v177\n"
        "v_and_b32 v29, 0xffff0000, v177\n"
        "v_sub_f32 v28, v28, v20\n"
        "v_sub_f32 v29, v29, v20\n"
        "v_fmac_f32 v21, v28, v28\n"
        "v_fmac_f32 v30, v29, v29\n"
        "v_lshlrev_b32 v28, 16, v178\n"
        "v_and_b32 v29, 0xffff0000, v178\n"
        "v_sub_f32 v28, v28, v20\n"
        "v_sub_f32 v29, v29, v20\n"
        "v_fmac_f32 v21, v28, v28\n"
        "v_fmac_f32 v30, v29, v29\n"
        "v_lshlrev_b32 v28, 16, v179\n"
        "v_and_b32 v29, 0xffff0000, v179\n"
        "v_sub_f32 v28, v28, v20\n"
        "v_sub_f32 v29, v29, v20\n"
        "v_fmac_f32 v21, v28, v28\n"
        "v_fmac_f32 v30, v29, v29\n"
        "v_lshlrev_b32 v28, 16, v180\n"
        "v_and_b32 v29, 0xffff0000, v180\n"
        "v_sub_f32 v28, v28, v20\n"
        "v_sub_f32 v29, v29, v20\n"
        "v_fmac_f32 v21, v28, v28\n"
        "v_fmac_f32 v30, v29, v29\n"
        "v_lshlrev_b32 v28, 16, v181\n"
        "v_and_b32 v29, 0xffff0000, v181\n"
        "v_sub_f32 v28, v28, v20\n"
        "v_sub_f32 v29, v29, v20\n"
        "v_fmac_f32 v21, v28, v28\n"
        "v_fmac_f32 v30, v29, v29\n"
        "v_lshlrev_b32 v28, 16, v182\n"
        "v_and_b32 v29, 0xffff0000, v182\n"
        "v_sub_f32 v28, v28, v20\n"
        "v_sub_f32 v29, v29, v20\n"
        "v_fmac_f32 v21, v28, v28\n"
        "v_fmac_f32 v30, v29, v29\n"
        "v_lshlrev_b32 v28, 16, v183\n"
        "v_and_b32 v29, 0xffff0000, v183\n"
        "v_sub_f32 v28, v28, v20\n"
        "v_sub_f32 v29, v29, v20\n"
        "v_fmac_f32 v21, v28, v28\n"
        "v_fmac_f32 v30, v29, v29\n"
        "v_lshlrev_b32 v28, 16, v184\n"
        "v_and_b32 v29, 0xffff0000, v184\n"
        "v_sub_f32 v28, v28, v20\n"
        "v_sub_f32 v29, v29, v20\n"
        "v_fmac_f32 v21, v28, v28\n"
        "v_fmac_f32 v30, v29, v29\n"
        "v_lshlrev_b32 v28, 16, v185\n"
        "v_and_b32 v29, 0xffff0000, v185\n"
        "v_sub_f32 v28, v28, v20\n"
        "v_sub_f32 v29, v29, v20\n"
        "v_fmac_f32 v21, v28, v28\n"
        "v_fmac_f32 v30, v29, v29\n"
        "v_lshlrev_b32 v28, 16, v186\n"
        "v_and_b32 v29, 0xffff0000, v186\n"
        "v_sub_f32 v28, v28, v20\n"
        "v_sub_f32 v29, v29, v20\n"
        "v_fmac_f32 v21, v28, v28\n"
        "v_fmac_f32 v30, v29, v29\n"
        "v_lshlrev_b32 v28, 16, v187\n"
        "v_and_b32 v29, 0xffff0000, v187\n"
        "v_sub_f32 v28, v28, v20\n"
        "v_sub_f32 v29, v29, v20\n"
        "v_fmac_f32 v21, v28, v28\n"
        "v_fmac_f32 v30, v29, v29\n"
        "v_lshlrev_b32 v28, 16, v188\n"
        "v_and_b32 v29, 0xffff0000, v188\n"
        "v_sub_f32 v28, v28, v20\n"
        "v_sub_f32 v29, v29, v20\n"
        "v_fmac_f32 v21, v28, v28\n"
        "v_fmac_f32 v30, v29, v29\n"
        "v_lshlrev_b32 v28, 16, v189\n"
        "v_and_b32 v29, 0xffff0000, v189\n"
        "v_sub_f32 v28, v28, v20\n"
        "v_sub_f32 v29, v29, v20\n"
        "v_fmac_f32 v21, v28, v28\n"
        "v_fmac_f32 v30, v29, v29\n"
        "v_lshlrev_b32 v28, 16, v190\n"
        "v_and_b32 v29, 0xffff0000, v190\n"
        "v_sub_f32 v28, v28, v20\n"
        "v_sub_f32 v29, v29, v20\n"
        "v_fmac_f32 v21, v28, v28\n"
        "v_fmac_f32 v30, v29, v29\n"
        "v_lshlrev_b32 v28, 16, v191\n"
        "v_and_b32 v29, 0xffff0000, v191\n"
        "v_sub_f32 v28, v28, v20\n"
        "v_sub_f32 v29, v29, v20\n"
        "v_fmac_f32 v21, v28, v28\n"
        "v_fmac_f32 v30, v29, v29\n"
        "v_add_f32 v21, v21, v30\n"
        "v_mov_b32 v29, v21\n"
        "s_nop 1\n"
        "v_permlane16_swap_b32 v21, v29\n"
        "v_add_f32 v21, v21, v29\n"
        "v_mov_b32 v29, v21\n"
        "s_nop 1\n"
        "v_permlane32_swap_b32 v21, v29\n"
        "v_add_f32 v21, v21, v29\n"
        "v_mul_f32 v21, 0x3b000000, v21\n"
        "L_F_ok:\n"
        "v_cvt_pk_bf16_f32 v21, v21, v20\n"
        "v_and_b32 v20, 0xffff0000, v21\n"
        "v_lshlrev_b32 v21, 16, v21\n"
        "v_add_f32 v21, 0x3727c5ac, v21\n"
        "v_cvt_pk_bf16_f32 v21, v21, v21\n"
        "v_lshlrev_b32 v21, 16, v21\n"
        "v_sqrt_f32 v21, v21\n"
        "s_nop 0\n"
        "v_cvt_pk_bf16_f32 v21, v21, v21\n"
        "v_lshlrev_b32 v21, 16, v21\n"
        "v_rcp_f32 v21, v21\n"
        "s_waitcnt lgkmcnt(0)\n"
        "v_pk_add_f32 v[32:33], v[32:33], v[40:41]\n"
        "v_pk_add_f32 v[48:49], v[48:49], v[56:57]\n"
        "v_pk_add_f32 v[34:35], v[34:35], v[42:43]\n"
        "v_pk_add_f32 v[50:51], v[50:51], v[58:59]\n"
        "v_pk_add_f32 v[80:81], v[32:33], v[48:49]\n"
        "v_pk_add_f32 v[82:83], v[34:35], v[50:51]\n"
        "v_pk_add_f32 v[36:37], v[36:37], v[44:45]\n"
        "v_pk_add_f32 v[52:53], v[52:53], v[60:61]\n"
        "v_pk_add_f32 v[38:39], v[38:39], v[46:47]\n"
        "v_pk_add_f32 v[54:55], v[54:55], v[62:63]\n"
        "v_pk_add_f32 v[112:113], v[36:37], v[52:53]\n"
        "v_pk_add_f32 v[114:115], v[38:39], v[54:55]\n"
        "s_lshl_b32 s37, s50, 3\n"
        "s_add_u32 s37, s37, s51\n"
        "s_lshl_b32 s38, s28, 1\n"
        "s_or_b32 s37, s37, s38\n"
        "s_cmp_ge_u32 s37, 2\n"
        "s_cbranch_scc1 L_F_c2\n"
        "v_lshlrev_b32 v28, 4, v17\n"
        "s_lshl_b32 s38, s51, 6\n"
        "s_add_u32 s38, s38, 0x40800\n"
        "s_mov_b32 s46, 0x00010001\n"
        "s_mov_b32 s47, 0x00010001\n"
        "s_mov_b64 exec, s[46:47]\n"
        "buffer_store_dwordx4 v[112:115], v28, s[16:19], s38 offen\n"
        "s_mov_b64 exec, -1\n"
        "L_F_c2:\n"
        "v_mul_f32 v24, 0x3eb8aa3b, v21\n"
        "v_mul_f32 v28, v20, v21\n"
        "v_mul_f32 v26, v20, v24\n"
        "v_mov_b32 v30, v21\n"
        "v_pk_mul_f32 v[64:65], v[64:65], v[24:25] op_sel_hi:[1,0]\n"
        "v_pk_mul_f32 v[66:67], v[66:67], v[24:25] op_sel_hi:[1,0]\n"
        "v_pk_mul_f32 v[68:69], v[68:69], v[24:25] op_sel_hi:[1,0]\n"
        "v_pk_mul_f32 v[70:71], v[70:71], v[24:25] op_sel_hi:[1,0]\n"
        "v_pk_mul_f32 v[72:73], v[72:73], v[24:25] op_sel_hi:[1,0]\n"
        "v_pk_mul_f32 v[74:75], v[74:75], v[24:25] op_sel_hi:[1,0]\n"
        "v_pk_mul_f32 v[76:77], v[76:77], v[24:25] op_sel_hi:[1,0]\n"
        "v_pk_mul_f32 v[78:79], v[78:79], v[24:25] op_sel_hi:[1,0]\n"
        "v_pk_mul_f32 v[80:81], v[80:81], v[30:31] op_sel_hi:[1,0]\n"
        "v_pk_mul_f32 v[82:83], v[82:83], v[30:31] op_sel_hi:[1,0]\n"
        "v_pk_fma_f32 v[64:65], v[96:97], v[26:27], v[64:65] op_sel_hi:[1,0,1] neg_lo:[0,1,0] neg_hi:[0,1,0]\n"
        "v_pk_fma_f32 v[66:67], v[98:99], v[26:27], v[66:67] op_sel_hi:[1,0,1] neg_lo:[0,1,0] neg_hi:[0,1,0]\n"
        "v_pk_fma_f32 v[68:69], v[100:101], v[26:27], v[68:69] op_sel_hi:[1,0,1] neg_lo:[0,1,0] neg_hi:[0,1,0]\n"
        "v_pk_fma_f32 v[70:71], v[102:103], v[26:27], v[70:71] op_sel_hi:[1,0,1] neg_lo:[0,1,0] neg_hi:[0,1,0]\n"
        "v_pk_fma_f32 v[72:73], v[104:105], v[26:27], v[72:73] op_sel_hi:[1,0,1] neg_lo:[0,1,0] neg_hi:[0,1,0]\n"
        "v_pk_fma_f32 v[74:75], v[106:107], v[26:27], v[74:75] op_sel_hi:[1,0,1] neg_lo:[0,1,0] neg_hi:[0,1,0]\n"
        "v_pk_fma_f32 v[76:77], v[108:109], v[26:27], v[76:77] op_sel_hi:[1,0,1] neg_lo:[0,1,0] neg_hi:[0,1,0]\n"
        "v_pk_fma_f32 v[78:79], v[110:111], v[26:27], v[78:79] op_sel_hi:[1,0,1] neg_lo:[0,1,0] neg_hi:[0,1,0]\n"
        "v_pk_fma_f32 v[80:81], v[112:113], v[28:29], v[80:81] op_sel_hi:[1,0,1] neg_lo:[0,1,0] neg_hi:[0,1,0]\n"
        "v_pk_fma_f32 v[82:83], v[114:115], v[28:29], v[82:83] op_sel_hi:[1,0,1] neg_lo:[0,1,0] neg_hi:[0,1,0]\n"
        "v_cvt_pk_bf16_f32 v84, v64, v65\n"
        "v_cvt_pk_bf16_f32 v85, v66, v67\n"
        "v_cvt_pk_bf16_f32 v86, v68, v69\n"
        "v_cvt_pk_bf16_f32 v87, v70, v71\n"
        "v_cvt_pk_bf16_f32 v88, v72, v73\n"
        "v_cvt_pk_bf16_f32 v89, v74, v75\n"
        "v_cvt_pk_bf16_f32 v90, v76, v77\n"
        "v_cvt_pk_bf16_f32 v91, v78, v79\n"
        "v_cvt_pk_bf16_f32 v24, v80, v81\n"
        "v_cvt_pk_bf16_f32 v25, v82, v83\n"
        "v_permlane16_swap_b32 v84, v86\n"
        "v_permlane16_swap_b32 v85, v87\n"
        "v_permlane16_swap_b32 v88, v90\n"
        "v_permlane16_swap_b32 v89, v91\n"
        "buffer_store_dwordx4 v[84:87], v22, s[16:19], s64 offen sc1\n"
        "buffer_store_dwordx4 v[88:91], v23, s[16:19], s64 offen sc1\n"
        "s_cmp_lg_u32 s68, 0\n"
        "s_cbranch_scc1 L_F_nk\n"
        "buffer_store_dwordx2 v[24:25], v19, s[16:19], s65 offen\n"
        "L_F_nk:\n"
        "s_cmp_lg_u32 s68, 1\n"
        "s_cbranch_scc1 L_F_nv\n"
        "buffer_store_short v24, v19, s[16:19], s65 offen offset:1024\n"
        "buffer_store_short_d16_hi v24, v19, s[16:19], s65 offen offset:1040\n"
        "buffer_store_short v25, v19, s[16:19], s65 offen offset:1056\n"
        "buffer_store_short_d16_hi v25, v19, s[16:19], s65 offen offset:1072\n"
        "L_F_nv:\n"
        "v_dot2_f32_bf16 v64, v84, v84, 0\n"
        "v_dot2_f32_bf16 v68, v88, v88, 0\n"
        "v_dot2_f32_bf16 v66, v24, v24, 0\n"
        "v_dot2_f32_bf16 v64, v85, v85, v64\n"
        "v_dot2_f32_bf16 v68, v89, v89, v68\n"
        "v_dot2_f32_bf16 v66, v25, v25, v66\n"
        "v_dot2_f32_bf16 v64, v86, v86, v64\n"
        "v_dot2_f32_bf16 v68, v90, v90, v68\n"
        "v_dot2_f32_bf16 v64, v87, v87, v64\n"
        "v_dot2_f32_bf16 v68, v91, v91, v68\n"
        "v_mov_b32 v67, v66\n"
        "s_nop 0\n"
        "v_mov_b32 v65, v64\n"
        "v_mov_b32 v69, v68\n"
        "v_permlane16_swap_b32 v66, v67\n"
        "v_permlane32_swap_b32 v64, v65\n"
        "v_permlane32_swap_b32 v68, v69\n"
        "v_add_f32 v66, v66, v67\n"
        "v_add_f32 v64, v64, v65\n"
        "v_add_f32 v68, v68, v69\n"
        "v_mov_b32 v67, v66\n"
        "v_max_f32 v64, v64, v68\n"
        "v_mov_b32 v65, v64\n"
        "v_permlane32_swap_b32 v66, v67\n"
        "s_nop 0\n"
        "v_permlane16_swap_b32 v64, v65\n"
        "v_add_f32 v66, v66, v67\n"
        "v_max_f32 v64, v64, v65\n"
        "s_nop 0\n"
        "v_max_f32_dpp v66, v66, v66 quad_perm:[1,0,3,2] row_mask:0xf bank_mask:0xf\n"
        "v_max_f32_dpp v64, v64, v64 quad_perm:[1,0,3,2] row_mask:0xf bank_mask:0xf\n"
        "s_nop 0\n"
        "v_max_f32_dpp v66, v66, v66 quad_perm:[2,3,0,1] row_mask:0xf bank_mask:0xf\n"
        "v_max_f32_dpp v64, v64, v64 quad_perm:[2,3,0,1] row_mask:0xf bank_mask:0xf\n"
        "s_nop 0\n"
        "v_max_f32_dpp v66, v66, v66 row_half_mirror row_mask:0xf bank_mask:0xf\n"
        "v_max_f32_dpp v64, v64, v64 row_half_mirror row_mask:0xf bank_mask:0xf\n"
        "s_nop 0\n"
        "v_max_f32_dpp v66, v66, v66 row_mirror row_mask:0xf bank_mask:0xf\n"
        "v_mov_b32 v65, v64\n"
        "v_mov_b32 v67, v66\n"
        "s_mov_b64 exec, 0x101\n"
        "buffer_store_dwordx2 v[64:65], v18, s[16:19], s66 offen\n"
        "s_cmp_lg_u32 s68, 0\n"
        "s_cbranch_scc1 L_F_nm\n"
        "s_mov_b64 exec, 1\n"
        "buffer_store_dwordx2 v[66:67], off, s[16:19], s67\n"
        "L_F_nm:\n"
        "s_mov_b64 exec, -1\n"
        "s_endpgm\n"
        "L_fend:\n"
        :
        : "s"(__builtin_amdgcn_kernarg_segment_ptr()), "s"(blockIdx.x), "s"(w)
        : "memory", "vcc", "scc", "v0", "v1", "v2", "v3", "v4", "v5", "v6", "v7", "v8", "v9", "v10", "v11", "v12", "v13", "v14", "v15", "v16", "v17", "v18", "v19", "v20", "v21", "v22", "v23", "v24", "v25", "v26", "v27", "v28", "v29", "v30", "v31", "v32", "v33", "v34", "v35", "v36", "v37", "v38", "v39", "v40", "v41", "v42", "v43", "v44", "v45", "v46", "v47", "v48", "v49", "v50", "v51", "v52", "v53", "v54", "v55", "v56", "v57", "v58", "v59", "v60", "v61", "v62", "v63", "v64", "v65", "v66", "v67", "v68", "v69", "v70", "v71", "v72", "v73", "v74", "v75", "v76", "v77", "v78", "v79", "v80", "v81", "v82", "v83", "v84", "v85", "v86", "v87", "v88", "v89", "v90", "v91", "v92", "v93", "v94", "v95", "v96", "v97", "v98", "v99", "v100", "v101", "v102", "v103", "v104", "v105", "v106", "v107", "v108", "v109", "v110", "v111", "v112", "v113", "v114", "v115", "v116", "v117", "v118", "v119", "v120", "v121", "v122", "v123", "v124", "v125", "v126", "v127", "v128", "v129", "v130", "v131", "v132", "v133", "v134", "v135", "v136", "v137", "v138", "v139", "v140", "v141", "v142", "v143", "v144", "v145", "v146", "v147", "v148", "v149", "v150", "v151", "v152", "v153", "v154", "v155", "v156", "v157", "v158", "v159", "v160", "v161", "v162", "v163", "v164", "v165", "v166", "v167", "v168", "v169", "v170", "v171", "v172", "v173", "v174", "v175", "v176", "v177", "v178", "v179", "v180", "v181", "v182", "v183", "v184", "v185", "v186", "v187", "v188", "v189", "v190", "v191", "v192", "v193", "v194", "v195", "v196", "v197", "v198", "v199", "v200", "v201", "v202", "v203", "v204", "v205", "v206", "v207", "v208", "v209", "v210", "v211", "v212", "v213", "v214", "v215", "v216", "v217", "v218", "v219", "v220", "v221", "v222", "v223", "v224", "v225", "v226", "v227", "v228", "v229", "v230", "v231", "v232", "v233", "v234", "v235", "v236", "v237", "v238", "v239", "v240", "v241", "v242", "v243", "v244", "v245", "v246", "v247", "v248", "v249", "v250", "v251", "v252", "v253", "v254", "v255", "a0", "a1", "a2", "a3", "a4", "a5", "a6", "a7", "a8", "a9", "a10", "a11", "a12", "a13", "a14", "a15", "a16", "a17", "a18", "a19", "a20", "a21", "a22", "a23", "a24", "a25", "a26", "a27", "a28", "a29", "a30", "a31", "a32", "a33", "a34", "a35", "a36", "a37", "a38", "a39", "a40", "a41", "a42", "a43", "a44", "a45", "a46", "a47", "a48", "a49", "a50", "a51", "a52", "a53", "a54", "a55", "a56", "a57", "a58", "a59", "a60", "a61", "a62", "a63", "a64", "a65", "a66", "a67", "a68", "a69", "a70", "a71", "a72", "a73", "a74", "a75", "a76", "a77", "a78", "a79", "a80", "a81", "a82", "a83", "a84", "a85", "a86", "a87", "a88", "a89", "a90", "a91", "a92", "a93", "a94", "a95", "a96", "a97", "a98", "a99", "a100", "a101", "a102", "a103", "a104", "a105", "a106", "a107", "a108", "a109", "a110", "a111", "a112", "a113", "a114", "a115", "a116", "a117", "a118", "a119", "a120", "a121", "a122", "a123", "a124", "a125", "a126", "a127", "a128", "a129", "a130", "a131", "a132", "a133", "a134", "a135", "a136", "a137", "a138", "a139", "a140", "a141", "a142", "a143", "a144", "a145", "a146", "a147", "a148", "a149", "a150", "a151", "a152", "a153", "a154", "a155", "a156", "a157", "a158", "a159", "a160", "a161", "a162", "a163", "a164", "a165", "a166", "a167", "a168", "a169", "a170", "a171", "a172", "a173", "a174", "a175", "a176", "a177", "a178", "a179", "a180", "a181", "a182", "a183", "a184", "a185", "a186", "a187", "a188", "a189", "a190", "a191", "a192", "a193", "a194", "a195", "a196", "a197", "a198", "a199", "a200", "a201", "a202", "a203", "a204", "a205", "a206", "a207", "a208", "a209", "a210", "a211", "a212", "a213", "a214", "a215", "a216", "a217", "a218", "a219", "a220", "a221", "a222", "a223", "a224", "a225", "a226", "a227", "a228", "a229", "a230", "a231", "a232", "a233", "a234", "a235", "a236", "a237", "a238", "a239", "a240", "a241", "a242", "a243", "a244", "a245", "a246", "a247", "a248", "a249", "a250", "a251", "a252", "a253", "a254", "a255", "s16", "s17", "s18", "s19", "s20", "s21", "s22", "s23", "s24", "s25", "s26", "s27", "s28", "s29", "s30", "s31", "s33", "s34", "s35", "s36", "s37", "s38", "s39", "s40", "s41", "s42", "s43", "s44", "s45", "s46", "s47", "s48", "s49", "s50", "s51", "s52", "s53", "s54", "s55", "s56", "s57", "s58", "s59", "s60", "s61", "s62", "s63", "s64", "s65", "s66", "s67", "s68", "s69", "s70", "s71", "s72", "s73", "s74", "s75", "s76", "s77", "s78", "s79", "s80", "s81", "s82", "s83", "s84", "s85", "s86", "s87", "s88", "s89", "s90", "s91", "s92", "s93", "s94", "s95", "s96", "s97", "s98", "s99"
    );
}

__global__ void __launch_bounds__(256, 1) k_layer(char* ws, const u16* xin, u16* out, int layer, int pad_) {
    const int w = __builtin_amdgcn_readfirstlane(threadIdx.x >> 6);
    asm volatile(
        "L_top:\n"
        "s_mov_b64 s[16:17], %0\n"
        "s_and_b32 s17, s17, 0xffff\n"
        "s_mov_b32 s18, 0x1a12000\n"
        "s_mov_b32 s19, 0x20000\n"
        "s_mov_b64 s[24:25], %2\n"
        "s_and_b32 s25, s25, 0xffff\n"
        "s_mov_b32 s26, 0x200000\n"
        "s_mov_b32 s27, 0x20000\n"
        "s_mov_b32 s28, %4\n"
        "s_mov_b32 s31, %5\n"
        "s_and_b32 s29, %3, 7\n"
        "s_lshr_b32 s30, %3, 3\n"
        "s_mov_b64 s[20:21], %1\n"
        "s_and_b32 s21, s21, 0xffff\n"
        "s_mov_b32 s22, 0x200000\n"
        "s_mov_b32 s23, 0x20000\n"
        "s_cmp_eq_u32 s31, 0\n"
        "s_cselect_b32 s20, s20, s16\n"
        "s_cselect_b32 s21, s21, s17\n"
        "s_cselect_b32 s22, s22, s18\n"
        "v_mbcnt_lo_u32_b32 v0, -1, 0\n"
        "v_mbcnt_hi_u32_b32 v0, -1, v0\n"
        "v_lshlrev_b32 v1, 4, v0\n"
        "s_lshl_b32 s52, s29, 5\n"
        "s_add_u32 s52, s52, s30\n"
        "v_mov_b32 v112, 0x3f803f80\n"
        "v_mov_b32 v113, 0\n"
        "v_mov_b32 v114, 0\n"
        "v_mov_b32 v115, 0\n"
        "v_mov_b32 v116, 0\n"
        "v_mov_b32 v117, 0\n"
        "v_mov_b32 v118, 0\n"
        "v_mov_b32 v119, 0\n"
        "s_mov_b32 exec_lo, 0x44444444\n"
        "s_mov_b32 exec_hi, 0\n"
        "v_mov_b32 v116, 0x3afe3f80\n"
        "s_mov_b64 exec, -1\n"
        "s_lshl_b32 s33, s31, 17\n"
        "s_lshl_b32 s36, s28, 15\n"
        "s_add_u32 s33, s33, s36\n"
        "s_add_u32 s33, s33, 0x52000\n"
        "v_mfma_f32_32x32x16_bf16 a[64:79], v[112:115], v[116:119], 0\n"
        "buffer_load_dwordx4 a[0:3], v1, s[16:19], s33 offen offset:0\n"
        "s_lshl_b32 s59, s31, 21\n"
        "s_lshl_b32 s36, s52, 13\n"
        "s_add_u32 s59, s59, s36\n"
        "s_add_u32 s59, s59, 0x712000\n"
        "buffer_load_dwordx4 v[32:35], v1, s[16:19], s59 offen offset:0\n"
        "s_lshl_b32 s34, s31, 10\n"
        "s_add_u32 s34, s34, 0x41000\n"
        "buffer_load_dwordx4 v[28:31], v1, s[16:19], s34 offen\n"
        "s_lshl_b32 s36, s31, 14\n"
        "s_lshl_b32 s37, s52, 6\n"
        "s_add_u32 s36, s36, s37\n"
        "s_add_u32 s36, s36, 0x42000\n"
        "v_and_b32 v15, 15, v0\n"
        "v_lshlrev_b32 v15, 2, v15\n"
        "buffer_load_dword v27, v15, s[16:19], s36 offen\n"
        "s_getpc_b64 s[48:49]\n"
        "L_vpc:\n"
        "s_lshl_b32 s38, s30, 2\n"
        "s_add_u32 s38, s38, s28\n"
        "s_lshl_b32 s38, s38, 7\n"
        "v_lshl_add_u32 v219, v0, 14, s38\n"
        "v_min_u32 v219, L_hot_end-L_vpc-4, v219\n"
        "s_mov_b64 exec, 7\n"
        "global_load_dword v219, v219, s[48:49]\n"
        "s_mov_b64 exec, -1\n"
        "v_mfma_f32_32x32x16_bf16 a[80:95], v[112:115], v[116:119], 0\n"
        "v_bfe_u32 v16, v0, 3, 2\n"
        "v_add_u32 v16, s28, v16\n"
        "v_and_b32 v16, 3, v16\n"
        "v_lshlrev_b32 v16, 5, v16\n"
        "v_bfe_u32 v17, v0, 2, 1\n"
        "v_lshl_add_u32 v16, v17, 7, v16\n"
        "v_and_b32 v17, 1, v0\n"
        "v_lshl_add_u32 v16, v17, 4, v16\n"
        "v_lshrrev_b32 v17, 5, v0\n"
        "v_lshl_add_u32 v16, v17, 8, v16\n"
        "v_add_u32 v4, 0x400, v16\n"
        "s_mov_b32 s46, 0x33333333\n"
        "s_mov_b32 s47, 0x33333333\n"
        "v_mfma_f32_32x32x16_bf16 a[96:111], v[112:115], v[116:119], 0\n"
        "s_mov_b64 exec, s[46:47]\n"
        "buffer_load_dwordx4 a[64:67], v4, s[16:19], s33 offen offset:0\n"
        "buffer_load_dwordx4 a[68:71], v4, s[16:19], s33 offen offset:512\n"
        "s_mov_b64 exec, -1\n"
        "v_mfma_f32_32x32x16_bf16 a[112:127], v[112:115], v[116:119], 0\n"
        "s_add_u32 s58, s59, 4096\n"
        "v_and_b32 v16, 7, v0\n"
        "s_lshl_b32 s36, s52, 3\n"
        "v_add_u32 v16, s36, v16\n"
        "v_lshrrev_b32 v17, 4, v0\n"
        "v_bfe_u32 v15, v0, 3, 1\n"
        "s_lshl_b32 s37, s31, 2\n"
        "s_mul_i32 s38, s31, 6\n"
        "s_sub_u32 s38, 10, s38\n"
        "v_lshrrev_b32 v20, 4, v16\n"
        "v_lshlrev_b32 v20, 14, v20\n"
        "v_and_b32 v21, 15, v16\n"
        "v_lshl_add_u32 v20, v21, s38, v20\n"
        "s_add_u32 s38, s37, 8\n"
        "s_lshl_b32 s38, s28, s38\n"
        "s_mul_i32 s39, s31, 0x1112000\n"
        "buffer_load_dwordx4 a[4:7], v1, s[16:19], s33 offen offset:2048\n"
        "v_mfma_f32_32x32x16_bf16 a[128:143], v[112:115], v[116:119], 0\n"
        "s_add_u32 s38, s38, s39\n"
        "v_add_u32 v20, s38, v20\n"
        "s_add_u32 s38, s37, 7\n"
        "v_lshl_add_u32 v20, v15, s38, v20\n"
        "s_add_u32 s38, s37, 4\n"
        "v_lshl_add_u32 v11, v17, s38, v20\n"
        "s_lshl_b32 s38, 64, s37\n"
        "v_add_u32 v12, s38, v11\n"
        "v_lshrrev_b32 v20, 5, v16\n"
        "v_lshlrev_b32 v20, 15, v20\n"
        "s_lshl_b32 s36, s28, 13\n"
        "v_add_u32 v20, s36, v20\n"
        "v_lshl_add_u32 v20, v15, 12, v20\n"
        "v_lshrrev_b32 v21, 1, v17\n"
        "v_lshl_add_u32 v20, v21, 10, v20\n"
        "v_and_b32 v21, 31, v16\n"
        "s_mov_b64 exec, s[46:47]\n"
        "buffer_load_dwordx4 a[72:75], v4, s[16:19], s33 offen offset:2048\n"
        "buffer_load_dwordx4 a[76:79], v4, s[16:19], s33 offen offset:2560\n"
        "s_mov_b64 exec, -1\n"
        "v_mfma_f32_32x32x16_bf16 a[144:159], v[112:115], v[116:119], 0\n"
        "v_lshl_add_u32 v20, v21, 4, v20\n"
        "v_and_b32 v21, 1, v17\n"
        "v_lshl_add_u32 v13, v21, 9, v20\n"
        "s_lshl_b32 s35, s28, 17\n"
        "s_add_u32 s35, s35, 0xd2000\n"
        "v_add_u32 v24, 0x1000, v1\n"
        "v_add_u32 v25, 0x2000, v1\n"
        "v_add_u32 v26, 0x3000, v1\n"
        "s_add_u32 s36, s28, 1\n"
        "s_and_b32 s36, s36, 3\n"
        "s_mul_i32 s36, s36, 3072\n"
        "s_add_u32 s36, s36, 147456\n"
        "v_add_u32 v5, s36, v1\n"
        "s_add_u32 s36, s28, 2\n"
        "s_and_b32 s36, s36, 3\n"
        "s_mul_i32 s36, s36, 3072\n"
        "s_add_u32 s33, s33, 4096\n"
        "buffer_load_dwordx4 a[8:11], v1, s[16:19], s33 offen offset:0\n"
        "v_mfma_f32_32x32x16_bf16 a[160:175], v[112:115], v[116:119], 0\n"
        "s_add_u32 s36, s36, 148480\n"
        "v_add_u32 v6, s36, v1\n"
        "s_add_u32 s36, s28, 3\n"
        "s_and_b32 s36, s36, 3\n"
        "s_mul_i32 s36, s36, 3072\n"
        "s_add_u32 s36, s36, 149504\n"
        "v_add_u32 v7, s36, v1\n"
        "s_mul_i32 s36, s28, 3072\n"
        "s_add_u32 s36, s36, 147456\n"
        "v_add_u32 v8, s36, v1\n"
        "v_bfe_u32 v15, v0, 3, 2\n"
        "v_lshlrev_b32 v15, 8, v15\n"
        "v_lshrrev_b32 v16, 5, v0\n"
        "v_lshl_add_u32 v15, v16, 7, v15\n"
        "v_and_b32 v16, 7, v0\n"
        "v_lshl_add_u32 v15, v16, 4, v15\n"
        "s_mov_b64 exec, s[46:47]\n"
        "buffer_load_dwordx4 a[80:83], v4, s[16:19], s33 offen offset:0\n"
        "buffer_load_dwordx4 a[84:87], v4, s[16:19], s33 offen offset:512\n"
        "s_mov_b64 exec, -1\n"
        "v_mfma_f32_32x32x16_bf16 a[176:191], v[112:115], v[116:119], 0\n"
        "s_lshl_b32 s36, s28, 2\n"
        "s_add_u32 s36, s36, 159744\n"
        "v_add_u32 v9, s36, v15\n"
        "v_lshlrev_b32 v15, 7, v17\n"
        "v_lshl_add_u32 v15, v16, 4, v15\n"
        "v_add_u32 v10, 0x27000, v15\n"
        "v_lshlrev_b32 v15, 5, v16\n"
        "v_add_u32 v21, 0x27400, v15\n"
        "s_lshl_b32 s36, s28, 3\n"
        "v_add_u32 v23, s36, v21\n"
        "s_and_b32 s37, s30, 15\n"
        "s_mul_i32 s48, s37, 0x38000\n"
        "s_add_u32 s43, s48, 0x1d2000\n"
        "s_lshr_b32 s54, s52, 4\n"
        "s_lshl_b32 s48, s54, 2\n"
        "s_add_u32 s48, s48, s28\n"
        "buffer_load_dwordx4 v[220:223], v11, s[20:23], 0 offen\n"
        "s_mul_i32 s48, s48, 112\n"
        "s_mul_i32 s37, s37, 7\n"
        "s_add_u32 s48, s48, s37\n"
        "s_lshl_b32 s48, s48, 10\n"
        "s_add_u32 s56, s48, 0x1312000\n"
        "s_lshl_b32 s50, s29, 2\n"
        "s_lshr_b32 s37, s30, 3\n"
        "s_add_u32 s50, s50, s37\n"
        "s_lshl_b32 s48, s50, 1\n"
        "s_and_b32 s49, s28, 1\n"
        "s_add_u32 s51, s48, s49\n"
        "s_and_b32 s48, s30, 7\n"
        "s_lshl_b32 s48, s48, 1\n"
        "s_lshr_b32 s49, s28, 1\n"
        "s_add_u32 s40, s48, s49\n"
        "s_mul_i32 s49, s28, 28\n"
        "buffer_load_dwordx4 v[224:227], v12, s[20:23], 0 offen\n"
        "s_mul_i32 s37, s40, 112\n"
        "s_add_u32 s37, s37, s49\n"
        "s_lshl_b32 s37, s37, 10\n"
        "s_add_u32 s44, s37, 0x552000\n"
        "s_xor_b32 s37, s40, 1\n"
        "s_mul_i32 s37, s37, 112\n"
        "s_add_u32 s37, s37, s49\n"
        "s_lshl_b32 s37, s37, 10\n"
        "s_add_u32 s45, s37, 0x552000\n"
        "s_lshr_b32 s49, s30, 3\n"
        "s_mul_i32 s49, s49, 7168\n"
        "s_add_u32 s60, s49, s44\n"
        "s_add_u32 s61, s49, s45\n"
        "s_lshr_b32 s49, s30, 3\n"
        "s_add_u32 s49, s49, 1\n"
        "s_and_b32 s49, s49, 3\n"
        "buffer_load_dwordx4 a[12:15], v1, s[16:19], s33 offen offset:2048\n"
        "s_mul_i32 s49, s49, 7168\n"
        "s_add_u32 s62, s49, s44\n"
        "s_add_u32 s63, s49, s45\n"
        "s_lshr_b32 s49, s30, 3\n"
        "s_add_u32 s49, s49, 2\n"
        "s_and_b32 s49, s49, 3\n"
        "s_mul_i32 s49, s49, 7168\n"
        "s_add_u32 s64, s49, s44\n"
        "s_add_u32 s65, s49, s45\n"
        "s_lshr_b32 s49, s30, 3\n"
        "s_add_u32 s49, s49, 3\n"
        "s_and_b32 s49, s49, 3\n"
        "s_mul_i32 s49, s49, 7168\n"
        "s_add_u32 s66, s49, s44\n"
        "s_add_u32 s67, s49, s45\n"
        "s_lshl_b32 s53, s31, 14\n"
        "s_mov_b64 exec, s[46:47]\n"
        "buffer_load_dwordx4 a[88:91], v4, s[16:19], s33 offen offset:2048\n"
        "buffer_load_dwordx4 a[92:95], v4, s[16:19], s33 offen offset:2560\n"
        "s_mov_b64 exec, -1\n"
        "s_add_u32 s53, s53, 0x0\n"
        "s_waitcnt vmcnt(13)\n"
        "v_max_f32 v2, v28, v30\n"
        "v_max_f32_dpp v27, v27, v27 quad_perm:[1,0,3,2] row_mask:0xf bank_mask:0xf\n"
        "s_nop 0\n"
        "v_max_f32_dpp v2, v2, v2 quad_perm:[1,0,3,2] row_mask:0xf bank_mask:0xf\n"
        "v_max_f32_dpp v27, v27, v27 quad_perm:[2,3,0,1] row_mask:0xf bank_mask:0xf\n"
        "s_nop 0\n"
        "v_max_f32_dpp v2, v2, v2 quad_perm:[2,3,0,1] row_mask:0xf bank_mask:0xf\n"
        "v_max_f32_dpp v27, v27, v27 row_half_mirror row_mask:0xf bank_mask:0xf\n"
        "s_nop 0\n"
        "v_max_f32_dpp v2, v2, v2 row_half_mirror row_mask:0xf bank_mask:0xf\n"
        "v_max_f32_dpp v27, v27, v27 row_mirror row_mask:0xf bank_mask:0xf\n"
        "s_nop 0\n"
        "v_max_f32_dpp v2, v2, v2 row_mirror row_mask:0xf bank_mask:0xf\n"
        "v_mov_b32 v3, v2\n"
        "s_nop 1\n"
        "v_permlane16_swap_b32 v2, v3\n"
        "v_max_f32 v2, v2, v3\n"
        "v_mov_b32 v3, v2\n"
        "s_nop 1\n"
        "v_permlane32_swap_b32 v2, v3\n"
        "s_add_u32 s33, s33, 4096\n"
        "buffer_load_dwordx4 a[16:19], v1, s[16:19], s33 offen offset:0\n"
        "v_max_f32 v2, v2, v3\n"
        "v_mul_f32 v20, v27, v2\n"
        "v_sqrt_f32 v20, v20\n"
        "v_mov_b32 v3, v21\n"
        "v_mov_b32 v21, 0x358637bd\n"
        "s_mov_b32 s36, 0x3f8147ae\n"
        "v_fma_f32 v20, v20, s36, v21\n"
        "s_mov_b32 s36, 0x42480000\n"
        "v_cmp_ge_f32 vcc, s36, v20\n"
        "v_sub_f32 v64, 0, v20\n"
        "v_sub_f32 v65, 0, v20\n"
        "v_sub_f32 v66, 0, v20\n"
        "v_sub_f32 v67, 0, v20\n"
        "v_sub_f32 v68, 0, v20\n"
        "v_sub_f32 v69, 0, v20\n"
        "v_sub_f32 v70, 0, v20\n"
        "s_mov_b64 exec, s[46:47]\n"
        "buffer_load_dwordx4 a[96:99], v4, s[16:19], s33 offen offset:0\n"
        "buffer_load_dwordx4 a[100:103], v4, s[16:19], s33 offen offset:512\n"
        "s_mov_b64 exec, -1\n"
        "v_sub_f32 v71, 0, v20\n"
        "v_sub_f32 v72, 0, v20\n"
        "v_sub_f32 v73, 0, v20\n"
        "v_sub_f32 v74, 0, v20\n"
        "v_sub_f32 v75, 0, v20\n"
        "v_sub_f32 v76, 0, v20\n"
        "v_sub_f32 v77, 0, v20\n"
        "v_sub_f32 v78, 0, v20\n"
        "v_sub_f32 v79, 0, v20\n"
        "s_cmp_eq_u64 vcc, -1\n"
        "s_cbranch_scc0 L_slow\n"
        "v_mfma_f32_32x32x16_bf16 v[80:95], a[0:3], v[32:35], v[64:79]\n"
        "s_nop 7\n"
        "s_nop 3\n"
        "v_exp_f32 v80, v80\n"
        "v_exp_f32 v81, v81\n"
        "v_exp_f32 v82, v82\n"
        "v_exp_f32 v83, v83\n"
        "buffer_load_dwordx4 a[20:23], v1, s[16:19], s33 offen offset:2048\n"
        "v_cvt_pk_bf16_f32 v112, v80, v81\n"
        "v_exp_f32 v84, v84\n"
        "v_exp_f32 v85, v85\n"
        "v_cvt_pk_bf16_f32 v113, v82, v83\n"
        "v_exp_f32 v86, v86\n"
        "v_exp_f32 v87, v87\n"
        "v_cvt_pk_bf16_f32 v114, v84, v85\n"
        "s_waitcnt vmcnt(14)\n"
        "v_mfma_f32_32x32x16_bf16 v[96:111], a[4:7], v[32:35], v[64:79]\n"
        "s_mov_b64 exec, s[46:47]\n"
        "buffer_load_dwordx4 a[104:107], v4, s[16:19], s33 offen offset:2048\n"
        "buffer_load_dwordx4 a[108:111], v4, s[16:19], s33 offen offset:2560\n"
        "s_mov_b64 exec, -1\n"
        "v_exp_f32 v88, v88\n"
        "v_exp_f32 v89, v89\n"
        "v_cvt_pk_bf16_f32 v115, v86, v87\n"
        "v_exp_f32 v90, v90\n"
        "v_exp_f32 v91, v91\n"
        "v_mfma_f32_32x32x16_bf16 v[128:143], a[64:67], v[112:115], 0\n"
        "v_cvt_pk_bf16_f32 v116, v88, v89\n"
        "v_exp_f32 v92, v92\n"
        "v_exp_f32 v93, v93\n"
        "v_cvt_pk_bf16_f32 v117, v90, v91\n"
        "v_exp_f32 v94, v94\n"
        "v_exp_f32 v95, v95\n"
        "v_cvt_pk_bf16_f32 v118, v92, v93\n"
        "v_exp_f32 v96, v96\n"
        "v_exp_f32 v97, v97\n"
        "v_cvt_pk_bf16_f32 v119, v94, v95\n"
        "v_exp_f32 v98, v98\n"
        "v_exp_f32 v99, v99\n"
        "v_mfma_f32_32x32x16_bf16 v[128:143], a[68:71], v[116:119], v[128:143]\n"
        "s_add_u32 s33, s33, 4096\n"
        "buffer_load_dwordx4 a[24:27], v1, s[16:19], s33 offen offset:0\n"
        "v_cvt_pk_bf16_f32 v112, v96, v97\n"
        "v_exp_f32 v100, v100\n"
        "v_exp_f32 v101, v101\n"
        "v_cvt_pk_bf16_f32 v113, v98, v99\n"
        "v_exp_f32 v102, v102\n"
        "v_exp_f32 v103, v103\n"
        "v_cvt_pk_bf16_f32 v114, v100, v101\n"
        "s_waitcnt vmcnt(14)\n"
        "v_mfma_f32_32x32x16_bf16 v[80:95], a[8:11], v[32:35], v[64:79]\n"
        "s_mov_b64 exec, s[46:47]\n"
        "buffer_load_dwordx4 a[112:115], v4, s[16:19], s33 offen offset:0\n"
        "buffer_load_dwordx4 a[116:119], v4, s[16:19], s33 offen offset:512\n"
        "s_mov_b64 exec, -1\n"
        "v_exp_f32 v104, v104\n"
        "v_exp_f32 v105, v105\n"
        "v_cvt_pk_bf16_f32 v115, v102, v103\n"
        "v_exp_f32 v106, v106\n"
        "v_exp_f32 v107, v107\n"
        "v_mfma_f32_32x32x16_bf16 v[128:143], a[72:75], v[112:115], v[128:143]\n"
        "v_cvt_pk_bf16_f32 v116, v104, v105\n"
        "v_exp_f32 v108, v108\n"
        "v_exp_f32 v109, v109\n"
        "v_cvt_pk_bf16_f32 v117, v106, v107\n"
        "v_exp_f32 v110, v110\n"
        "v_exp_f32 v111, v111\n"
        "v_cvt_pk_bf16_f32 v118, v108, v109\n"
        "v_exp_f32 v80, v80\n"
        "v_exp_f32 v81, v81\n"
        "v_cvt_pk_bf16_f32 v119, v110, v111\n"
        "v_exp_f32 v82, v82\n"
        "v_exp_f32 v83, v83\n"
        "v_mfma_f32_32x32x16_bf16 v[128:143], a[76:79], v[116:119], v[128:143]\n"
        "buffer_load_dwordx4 a[28:31], v1, s[16:19], s33 offen offset:2048\n"
        "v_cvt_pk_bf16_f32 v112, v80, v81\n"
        "v_exp_f32 v84, v84\n"
        "v_exp_f32 v85, v85\n"
        "v_cvt_pk_bf16_f32 v113, v82, v83\n"
        "v_exp_f32 v86, v86\n"
        "v_exp_f32 v87, v87\n"
        "v_cvt_pk_bf16_f32 v114, v84, v85\n"
        "s_waitcnt vmcnt(12)\n"
        "v_mfma_f32_32x32x16_bf16 v[96:111], a[12:15], v[32:35], v[64:79]\n"
        "s_mov_b64 exec, s[46:47]\n"
        "buffer_load_dwordx4 a[120:123], v4, s[16:19], s33 offen offset:2048\n"
        "buffer_load_dwordx4 a[124:127], v4, s[16:19], s33 offen offset:2560\n"
        "s_mov_b64 exec, -1\n"
        "v_exp_f32 v88, v88\n"
        "v_exp_f32 v89, v89\n"
        "v_cvt_pk_bf16_f32 v115, v86, v87\n"
        "v_exp_f32 v90, v90\n"
        "v_exp_f32 v91, v91\n"
        "v_mfma_f32_32x32x16_bf16 v[128:143], a[80:83], v[112:115], v[128:143]\n"
        "v_cvt_pk_bf16_f32 v116, v88, v89\n"
        "v_exp_f32 v92, v92\n"
        "v_exp_f32 v93, v93\n"
        "v_cvt_pk_bf16_f32 v117, v90, v91\n"
        "v_exp_f32 v94, v94\n"
        "v_exp_f32 v95, v95\n"
        "v_cvt_pk_bf16_f32 v118, v92, v93\n"
        "v_exp_f32 v96, v96\n"
        "v_exp_f32 v97, v97\n"
        "v_cvt_pk_bf16_f32 v119, v94, v95\n"
        "v_exp_f32 v98, v98\n"
        "v_exp_f32 v99, v99\n"
        "v_mfma_f32_32x32x16_bf16 v[128:143], a[84:87], v[116:119], v[128:143]\n"
        "s_add_u32 s33, s33, 4096\n"
        "buffer_load_dwordx4 a[32:35], v1, s[16:19], s33 offen offset:0\n"
        "v_cvt_pk_bf16_f32 v112, v96, v97\n"
        "v_exp_f32 v100, v100\n"
        "v_exp_f32 v101, v101\n"
        "v_cvt_pk_bf16_f32 v113, v98, v99\n"
        "v_exp_f32 v102, v102\n"
        "v_exp_f32 v103, v103\n"
        "v_cvt_pk_bf16_f32 v114, v100, v101\n"
        "s_waitcnt vmcnt(12)\n"
        "v_mfma_f32_32x32x16_bf16 v[80:95], a[16:19], v[32:35], v[64:79]\n"
        "s_mov_b64 exec, s[46:47]\n"
        "buffer_load_dwordx4 a[128:131], v4, s[16:19], s33 offen offset:0\n"
        "buffer_load_dwordx4 a[132:135], v4, s[16:19], s33 offen offset:512\n"
        "s_mov_b64 exec, -1\n"
        "v_exp_f32 v104, v104\n"
        "v_exp_f32 v105, v105\n"
        "v_cvt_pk_bf16_f32 v115, v102, v103\n"
        "v_exp_f32 v106, v106\n"
        "v_exp_f32 v107, v107\n"
        "v_mfma_f32_32x32x16_bf16 v[128:143], a[88:91], v[112:115], v[128:143]\n"
        "v_cvt_pk_bf16_f32 v116, v104, v105\n"
        "v_exp_f32 v108, v108\n"
        "v_exp_f32 v109, v109\n"
        "v_cvt_pk_bf16_f32 v117, v106, v107\n"
        "v_exp_f32 v110, v110\n"
        "v_exp_f32 v111, v111\n"
        "v_cvt_pk_bf16_f32 v118, v108, v109\n"
        "v_exp_f32 v80, v80\n"
        "v_exp_f32 v81, v81\n"
        "v_cvt_pk_bf16_f32 v119, v110, v111\n"
        "v_exp_f32 v82, v82\n"
        "v_exp_f32 v83, v83\n"
        "v_mfma_f32_32x32x16_bf16 v[128:143], a[92:95], v[116:119], v[128:143]\n"
        "buffer_load_dwordx4 a[36:39], v1, s[16:19], s33 offen offset:2048\n"
        "v_cvt_pk_bf16_f32 v112, v80, v81\n"
        "v_exp_f32 v84, v84\n"
        "v_exp_f32 v85, v85\n"
        "v_cvt_pk_bf16_f32 v113, v82, v83\n"
        "v_exp_f32 v86, v86\n"
        "v_exp_f32 v87, v87\n"
        "v_cvt_pk_bf16_f32 v114, v84, v85\n"
        "s_waitcnt vmcnt(12)\n"
        "v_mfma_f32_32x32x16_bf16 v[96:111], a[20:23], v[32:35], v[64:79]\n"
        "s_mov_b64 exec, s[46:47]\n"
        "buffer_load_dwordx4 a[136:139], v4, s[16:19], s33 offen offset:2048\n"
        "buffer_load_dwordx4 a[140:143], v4, s[16:19], s33 offen offset:2560\n"
        "s_mov_b64 exec, -1\n"
        "v_exp_f32 v88, v88\n"
        "v_exp_f32 v89, v89\n"
        "v_cvt_pk_bf16_f32 v115, v86, v87\n"
        "v_exp_f32 v90, v90\n"
        "v_exp_f32 v91, v91\n"
        "v_mfma_f32_32x32x16_bf16 v[128:143], a[96:99], v[112:115], v[128:143]\n"
        "v_cvt_pk_bf16_f32 v116, v88, v89\n"
        "v_exp_f32 v92, v92\n"
        "v_exp_f32 v93, v93\n"
        "v_cvt_pk_bf16_f32 v117, v90, v91\n"
        "v_exp_f32 v94, v94\n"
        "v_exp_f32 v95, v95\n"
        "v_cvt_pk_bf16_f32 v118, v92, v93\n"
        "v_exp_f32 v96, v96\n"
        "v_exp_f32 v97, v97\n"
        "v_cvt_pk_bf16_f32 v119, v94, v95\n"
        "v_exp_f32 v98, v98\n"
        "v_exp_f32 v99, v99\n"
        "v_mfma_f32_32x32x16_bf16 v[128:143], a[100:103], v[116:119], v[128:143]\n"
        "s_add_u32 s33, s33, 4096\n"
        "buffer_load_dwordx4 a[40:43], v1, s[16:19], s33 offen offset:0\n"
        "v_cvt_pk_bf16_f32 v112, v96, v97\n"
        "v_exp_f32 v100, v100\n"
        "v_exp_f32 v101, v101\n"
        "v_cvt_pk_bf16_f32 v113, v98, v99\n"
        "v_exp_f32 v102, v102\n"
        "v_exp_f32 v103, v103\n"
        "v_cvt_pk_bf16_f32 v114, v100, v101\n"
        "s_waitcnt vmcnt(12)\n"
        "v_mfma_f32_32x32x16_bf16 v[80:95], a[24:27], v[32:35], v[64:79]\n"
        "s_mov_b64 exec, s[46:47]\n"
        "buffer_load_dwordx4 a[144:147], v4, s[16:19], s33 offen offset:0\n"
        "buffer_load_dwordx4 a[148:151], v4, s[16:19], s33 offen offset:512\n"
        "s_mov_b64 exec, -1\n"
        "v_exp_f32 v104, v104\n"
        "v_exp_f32 v105, v105\n"
        "v_cvt_pk_bf16_f32 v115, v102, v103\n"
        "v_exp_f32 v106, v106\n"
        "v_exp_f32 v107, v107\n"
        "v_mfma_f32_32x32x16_bf16 v[128:143], a[104:107], v[112:115], v[128:143]\n"
        "v_cvt_pk_bf16_f32 v116, v104, v105\n"
        "v_exp_f32 v108, v108\n"
        "v_exp_f32 v109, v109\n"
        "v_cvt_pk_bf16_f32 v117, v106, v107\n"
        "v_exp_f32 v110, v110\n"
        "v_exp_f32 v111, v111\n"
        "v_cvt_pk_bf16_f32 v118, v108, v109\n"
        "v_exp_f32 v80, v80\n"
        "v_exp_f32 v81, v81\n"
        "v_cvt_pk_bf16_f32 v119, v110, v111\n"
        "v_exp_f32 v82, v82\n"
        "v_exp_f32 v83, v83\n"
        "v_mfma_f32_32x32x16_bf16 v[128:143], a[108:111], v[116:119], v[128:143]\n"
        "buffer_load_dwordx4 a[44:47], v1, s[16:19], s33 offen offset:2048\n"
        "v_cvt_pk_bf16_f32 v112, v80, v81\n"
        "v_exp_f32 v84, v84\n"
        "v_exp_f32 v85, v85\n"
        "v_cvt_pk_bf16_f32 v113, v82, v83\n"
        "v_exp_f32 v86, v86\n"
        "v_exp_f32 v87, v87\n"
        "v_cvt_pk_bf16_f32 v114, v84, v85\n"
        "s_waitcnt vmcnt(12)\n"
        "v_mfma_f32_32x32x16_bf16 v[96:111], a[28:31], v[32:35], v[64:79]\n"
        "s_mov_b64 exec, s[46:47]\n"
        "buffer_load_dwordx4 a[152:155], v4, s[16:19], s33 offen offset:2048\n"
        "buffer_load_dwordx4 a[156:159], v4, s[16:19], s33 offen offset:2560\n"
        "s_mov_b64 exec, -1\n"
        "v_exp_f32 v88, v88\n"
        "v_exp_f32 v89, v89\n"
        "v_cvt_pk_bf16_f32 v115, v86, v87\n"
        "v_exp_f32 v90, v90\n"
        "v_exp_f32 v91, v91\n"
        "v_mfma_f32_32x32x16_bf16 v[128:143], a[112:115], v[112:115], v[128:143]\n"
        "v_cvt_pk_bf16_f32 v116, v88, v89\n"
        "v_exp_f32 v92, v92\n"
        "v_exp_f32 v93, v93\n"
        "v_cvt_pk_bf16_f32 v117, v90, v91\n"
        "v_exp_f32 v94, v94\n"
        "v_exp_f32 v95, v95\n"
        "v_cvt_pk_bf16_f32 v118, v92, v93\n"
        "v_exp_f32 v96, v96\n"
        "v_exp_f32 v97, v97\n"
        "v_cvt_pk_bf16_f32 v119, v94, v95\n"
        "v_exp_f32 v98, v98\n"
        "v_exp_f32 v99, v99\n"
        "v_mfma_f32_32x32x16_bf16 v[128:143], a[116:119], v[116:119], v[128:143]\n"
        "s_add_u32 s33, s33, 4096\n"
        "buffer_load_dwordx4 a[48:51], v1, s[16:19], s33 offen offset:0\n"
        "v_cvt_pk_bf16_f32 v112, v96, v97\n"
        "v_exp_f32 v100, v100\n"
        "v_exp_f32 v101, v101\n"
        "v_cvt_pk_bf16_f32 v113, v98, v99\n"
        "v_exp_f32 v102, v102\n"
        "v_exp_f32 v103, v103\n"
        "v_cvt_pk_bf16_f32 v114, v100, v101\n"
        "s_waitcnt vmcnt(12)\n"
        "v_mfma_f32_32x32x16_bf16 v[80:95], a[32:35], v[32:35], v[64:79]\n"
        "s_mov_b64 exec, s[46:47]\n"
        "buffer_load_dwordx4 a[160:163], v4, s[16:19], s33 offen offset:0\n"
        "buffer_load_dwordx4 a[164:167], v4, s[16:19], s33 offen offset:512\n"
        "s_mov_b64 exec, -1\n"
        "v_exp_f32 v104, v104\n"
        "v_exp_f32 v105, v105\n"
        "v_cvt_pk_bf16_f32 v115, v102, v103\n"
        "v_exp_f32 v106, v106\n"
        "v_exp_f32 v107, v107\n"
        "v_mfma_f32_32x32x16_bf16 v[128:143], a[120:123], v[112:115], v[128:143]\n"
        "v_cvt_pk_bf16_f32 v116, v104, v105\n"
        "v_exp_f32 v108, v108\n"
        "v_exp_f32 v109, v109\n"
        "v_cvt_pk_bf16_f32 v117, v106, v107\n"
        "v_exp_f32 v110, v110\n"
        "v_exp_f32 v111, v111\n"
        "v_cvt_pk_bf16_f32 v118, v108, v109\n"
        "v_exp_f32 v80, v80\n"
        "v_exp_f32 v81, v81\n"
        "v_cvt_pk_bf16_f32 v119, v110, v111\n"
        "v_exp_f32 v82, v82\n"
        "v_exp_f32 v83, v83\n"
        "v_mfma_f32_32x32x16_bf16 v[128:143], a[124:127], v[116:119], v[128:143]\n"
        "buffer_load_dwordx4 a[52:55], v1, s[16:19], s33 offen offset:2048\n"
        "v_cvt_pk_bf16_f32 v112, v80, v81\n"
        "v_exp_f32 v84, v84\n"
        "v_exp_f32 v85, v85\n"
        "v_cvt_pk_bf16_f32 v113, v82, v83\n"
        "v_exp_f32 v86, v86\n"
        "v_lshlrev_b32 v160, 16, v220\n"
        "v_and_b32 v161, 0xffff0000, v220\n"
        "v_lshlrev_b32 v176, 16, v220\n"
        "v_and_b32 v177, 0xffff0000, v220\n"
        "v_exp_f32 v87, v87\n"
        "v_cvt_pk_bf16_f32 v114, v84, v85\n"
        "s_waitcnt vmcnt(12)\n"
        "v_mfma_f32_32x32x16_bf16 v[96:111], a[36:39], v[32:35], v[64:79]\n"
        "s_mov_b64 exec, s[46:47]\n"
        "buffer_load_dwordx4 a[168:171], v4, s[16:19], s33 offen offset:2048\n"
        "buffer_load_dwordx4 a[172:175], v4, s[16:19], s33 offen offset:2560\n"
        "s_mov_b64 exec, -1\n"
        "v_exp_f32 v88, v88\n"
        "v_exp_f32 v89, v89\n"
        "v_cvt_pk_bf16_f32 v115, v86, v87\n"
        "v_exp_f32 v90, v90\n"
        "v_exp_f32 v91, v91\n"
        "v_mfma_f32_32x32x16_bf16 v[128:143], a[128:131], v[112:115], v[128:143]\n"
        "v_cvt_pk_bf16_f32 v116, v88, v89\n"
        "v_exp_f32 v92, v92\n"
        "v_exp_f32 v93, v93\n"
        "v_cvt_pk_bf16_f32 v117, v90, v91\n"
        "v_exp_f32 v94, v94\n"
        "v_exp_f32 v95, v95\n"
        "v_cvt_pk_bf16_f32 v118, v92, v93\n"
        "v_exp_f32 v96, v96\n"
        "v_exp_f32 v97, v97\n"
        "v_cvt_pk_bf16_f32 v119, v94, v95\n"
        "v_exp_f32 v98, v98\n"
        "v_exp_f32 v99, v99\n"
        "v_mfma_f32_32x32x16_bf16 v[128:143], a[132:135], v[116:119], v[128:143]\n"
        "s_add_u32 s33, s33, 4096\n"
        "buffer_load_dwordx4 a[56:59], v1, s[16:19], s33 offen offset:0\n"
        "v_cvt_pk_bf16_f32 v112, v96, v97\n"
        "v_exp_f32 v100, v100\n"
        "v_exp_f32 v101, v101\n"
        "v_cvt_pk_bf16_f32 v113, v98, v99\n"
        "v_exp_f32 v102, v102\n"
        "v_lshlrev_b32 v162, 16, v221\n"
        "v_and_b32 v163, 0xffff0000, v221\n"
        "v_lshlrev_b32 v178, 16, v221\n"
        "v_and_b32 v179, 0xffff0000, v221\n"
        "v_exp_f32 v103, v103\n"
        "v_cvt_pk_bf16_f32 v114, v100, v101\n"
        "s_waitcnt vmcnt(12)\n"
        "v_mfma_f32_32x32x16_bf16 v[80:95], a[40:43], v[32:35], v[64:79]\n"
        "s_mov_b64 exec, s[46:47]\n"
        "buffer_load_dwordx4 a[176:179], v4, s[16:19], s33 offen offset:0\n"
        "buffer_load_dwordx4 a[180:183], v4, s[16:19], s33 offen offset:512\n"
        "s_mov_b64 exec, -1\n"
        "v_exp_f32 v104, v104\n"
        "v_exp_f32 v105, v105\n"
        "v_cvt_pk_bf16_f32 v115, v102, v103\n"
        "v_exp_f32 v106, v106\n"
        "v_exp_f32 v107, v107\n"
        "v_mfma_f32_32x32x16_bf16 v[128:143], a[136:139], v[112:115], v[128:143]\n"
        "buffer_load_dwordx4 v[36:39], v1, s[16:19], s59 offen offset:1024\n"
        "v_cvt_pk_bf16_f32 v116, v104, v105\n"
        "v_exp_f32 v108, v108\n"
        "v_exp_f32 v109, v109\n"
        "v_cvt_pk_bf16_f32 v117, v106, v107\n"
        "v_exp_f32 v110, v110\n"
        "v_exp_f32 v111, v111\n"
        "v_cvt_pk_bf16_f32 v118, v108, v109\n"
        "v_exp_f32 v80, v80\n"
        "v_exp_f32 v81, v81\n"
        "v_cvt_pk_bf16_f32 v119, v110, v111\n"
        "v_exp_f32 v82, v82\n"
        "v_exp_f32 v83, v83\n"
        "v_mfma_f32_32x32x16_bf16 v[128:143], a[140:143], v[116:119], v[128:143]\n"
        "buffer_load_dwordx4 a[60:63], v1, s[16:19], s33 offen offset:2048\n"
        "v_cvt_pk_bf16_f32 v112, v80, v81\n"
        "v_exp_f32 v84, v84\n"
        "v_exp_f32 v85, v85\n"
        "v_cvt_pk_bf16_f32 v113, v82, v83\n"
        "v_exp_f32 v86, v86\n"
        "v_lshlrev_b32 v164, 16, v222\n"
        "v_and_b32 v165, 0xffff0000, v222\n"
        "v_lshlrev_b32 v180, 16, v222\n"
        "v_and_b32 v181, 0xffff0000, v222\n"
        "v_exp_f32 v87, v87\n"
        "v_cvt_pk_bf16_f32 v114, v84, v85\n"
        "s_waitcnt vmcnt(13)\n"
        "v_mfma_f32_32x32x16_bf16 v[96:111], a[44:47], v[32:35], v[64:79]\n"
        "s_mov_b64 exec, s[46:47]\n"
        "buffer_load_dwordx4 a[184:187], v4, s[16:19], s33 offen offset:2048\n"
        "buffer_load_dwordx4 a[188:191], v4, s[16:19], s33 offen offset:2560\n"
        "s_mov_b64 exec, -1\n"
        "v_exp_f32 v88, v88\n"
        "v_exp_f32 v89, v89\n"
        "v_cvt_pk_bf16_f32 v115, v86, v87\n"
        "v_exp_f32 v90, v90\n"
        "v_exp_f32 v91, v91\n"
        "v_mfma_f32_32x32x16_bf16 v[128:143], a[144:147], v[112:115], v[128:143]\n"
        "v_cvt_pk_bf16_f32 v116, v88, v89\n"
        "v_exp_f32 v92, v92\n"
        "v_exp_f32 v93, v93\n"
        "v_cvt_pk_bf16_f32 v117, v90, v91\n"
        "v_exp_f32 v94, v94\n"
        "v_exp_f32 v95, v95\n"
        "v_cvt_pk_bf16_f32 v118, v92, v93\n"
        "v_exp_f32 v96, v96\n"
        "v_exp_f32 v97, v97\n"
        "v_cvt_pk_bf16_f32 v119, v94, v95\n"
        "v_exp_f32 v98, v98\n"
        "v_exp_f32 v99, v99\n"
        "v_mfma_f32_32x32x16_bf16 v[128:143], a[148:151], v[116:119], v[128:143]\n"
        "v_cvt_pk_bf16_f32 v112, v96, v97\n"
        "v_exp_f32 v100, v100\n"
        "v_exp_f32 v101, v101\n"
        "v_cvt_pk_bf16_f32 v113, v98, v99\n"
        "v_exp_f32 v102, v102\n"
        "v_lshlrev_b32 v166, 16, v223\n"
        "v_and_b32 v167, 0xffff0000, v223\n"
        "v_lshlrev_b32 v182, 16, v223\n"
        "v_and_b32 v183, 0xffff0000, v223\n"
        "v_exp_f32 v103, v103\n"
        "v_cvt_pk_bf16_f32 v114, v100, v101\n"
        "s_waitcnt vmcnt(12)\n"
        "v_mfma_f32_32x32x16_bf16 v[80:95], a[48:51], v[32:35], v[64:79]\n"
        "v_exp_f32 v104, v104\n"
        "v_exp_f32 v105, v105\n"
        "v_cvt_pk_bf16_f32 v115, v102, v103\n"
        "v_exp_f32 v106, v106\n"
        "v_exp_f32 v107, v107\n"
        "v_mfma_f32_32x32x16_bf16 v[128:143], a[152:155], v[112:115], v[128:143]\n"
        "s_add_u32 s36, s35, 0x0\n"
        "v_cvt_pk_bf16_f32 v116, v104, v105\n"
        "v_exp_f32 v108, v108\n"
        "v_exp_f32 v109, v109\n"
        "v_cvt_pk_bf16_f32 v117, v106, v107\n"
        "v_exp_f32 v110, v110\n"
        "v_exp_f32 v111, v111\n"
        "v_cvt_pk_bf16_f32 v118, v108, v109\n"
        "v_exp_f32 v80, v80\n"
        "v_exp_f32 v81, v81\n"
        "v_cvt_pk_bf16_f32 v119, v110, v111\n"
        "v_exp_f32 v82, v82\n"
        "v_exp_f32 v83, v83\n"
        "v_mfma_f32_32x32x16_bf16 v[128:143], a[156:159], v[116:119], v[128:143]\n"
        "buffer_load_dwordx4 a[192:195], v1, s[16:19], s36 offen offset:0\n"
        "v_cvt_pk_bf16_f32 v112, v80, v81\n"
        "v_exp_f32 v84, v84\n"
        "v_exp_f32 v85, v85\n"
        "v_cvt_pk_bf16_f32 v113, v82, v83\n"
        "v_exp_f32 v86, v86\n"
        "v_lshlrev_b32 v168, 16, v224\n"
        "v_and_b32 v169, 0xffff0000, v224\n"
        "v_lshlrev_b32 v184, 16, v224\n"
        "v_and_b32 v185, 0xffff0000, v224\n"
        "v_exp_f32 v87, v87\n"
        "v_cvt_pk_bf16_f32 v114, v84, v85\n"
        "s_waitcnt vmcnt(10)\n"
        "v_mfma_f32_32x32x16_bf16 v[96:111], a[52:55], v[32:35], v[64:79]\n"
        "buffer_load_dwordx4 a[196:199], v1, s[16:19], s36 offen offset:1024\n"
        "v_exp_f32 v88, v88\n"
        "v_exp_f32 v89, v89\n"
        "v_cvt_pk_bf16_f32 v115, v86, v87\n"
        "v_exp_f32 v90, v90\n"
        "v_exp_f32 v91, v91\n"
        "v_mfma_f32_32x32x16_bf16 v[128:143], a[160:163], v[112:115], v[128:143]\n"
        "buffer_load_dwordx4 a[200:203], v1, s[16:19], s36 offen offset:2048\n"
        "v_cvt_pk_bf16_f32 v116, v88, v89\n"
        "v_exp_f32 v92, v92\n"
        "v_exp_f32 v93, v93\n"
        "v_cvt_pk_bf16_f32 v117, v90, v91\n"
        "v_exp_f32 v94, v94\n"
        "v_exp_f32 v95, v95\n"
        "v_cvt_pk_bf16_f32 v118, v92, v93\n"
        "v_exp_f32 v96, v96\n"
        "v_exp_f32 v97, v97\n"
        "v_cvt_pk_bf16_f32 v119, v94, v95\n"
        "v_exp_f32 v98, v98\n"
        "v_exp_f32 v99, v99\n"
        "v_mfma_f32_32x32x16_bf16 v[128:143], a[164:167], v[116:119], v[128:143]\n"
        "buffer_load_dwordx4 a[204:207], v1, s[16:19], s36 offen offset:3072\n"
        "v_cvt_pk_bf16_f32 v112, v96, v97\n"
        "v_exp_f32 v100, v100\n"
        "v_exp_f32 v101, v101\n"
        "v_cvt_pk_bf16_f32 v113, v98, v99\n"
        "v_exp_f32 v102, v102\n"
        "v_lshlrev_b32 v170, 16, v225\n"
        "v_and_b32 v171, 0xffff0000, v225\n"
        "v_lshlrev_b32 v186, 16, v225\n"
        "v_and_b32 v187, 0xffff0000, v225\n"
        "v_exp_f32 v103, v103\n"
        "v_cvt_pk_bf16_f32 v114, v100, v101\n"
        "s_waitcnt vmcnt(10)\n"
        "v_mfma_f32_32x32x16_bf16 v[80:95], a[56:59], v[32:35], v[64:79]\n"
        "buffer_load_dwordx4 a[208:211], v24, s[16:19], s36 offen offset:0\n"
        "v_exp_f32 v104, v104\n"
        "v_exp_f32 v105, v105\n"
        "v_cvt_pk_bf16_f32 v115, v102, v103\n"
        "v_exp_f32 v106, v106\n"
        "v_exp_f32 v107, v107\n"
        "v_mfma_f32_32x32x16_bf16 v[128:143], a[168:171], v[112:115], v[128:143]\n"
        "buffer_load_dwordx4 a[212:215], v24, s[16:19], s36 offen offset:1024\n"
        "v_cvt_pk_bf16_f32 v116, v104, v105\n"
        "v_exp_f32 v108, v108\n"
        "v_exp_f32 v109, v109\n"
        "v_cvt_pk_bf16_f32 v117, v106, v107\n"
        "v_exp_f32 v110, v110\n"
        "v_exp_f32 v111, v111\n"
        "v_cvt_pk_bf16_f32 v118, v108, v109\n"
        "v_exp_f32 v80, v80\n"
        "v_exp_f32 v81, v81\n"
        "v_cvt_pk_bf16_f32 v119, v110, v111\n"
        "v_exp_f32 v82, v82\n"
        "v_exp_f32 v83, v83\n"
        "v_mfma_f32_32x32x16_bf16 v[128:143], a[172:175], v[116:119], v[128:143]\n"
        "buffer_load_dwordx4 a[216:219], v24, s[16:19], s36 offen offset:2048\n"
        "v_cvt_pk_bf16_f32 v112, v80, v81\n"
        "v_exp_f32 v84, v84\n"
        "v_exp_f32 v85, v85\n"
        "v_cvt_pk_bf16_f32 v113, v82, v83\n"
        "v_exp_f32 v86, v86\n"
        "v_lshlrev_b32 v172, 16, v226\n"
        "v_and_b32 v173, 0xffff0000, v226\n"
        "v_lshlrev_b32 v188, 16, v226\n"
        "v_and_b32 v189, 0xffff0000, v226\n"
        "v_exp_f32 v87, v87\n"
        "v_cvt_pk_bf16_f32 v114, v84, v85\n"
        "s_waitcnt vmcnt(7)\n"
        "v_mfma_f32_32x32x16_bf16 v[96:111], a[60:63], v[32:35], v[64:79]\n"
        "buffer_load_dwordx4 a[220:223], v24, s[16:19], s36 offen offset:3072\n"
        "v_exp_f32 v88, v88\n"
        "v_exp_f32 v89, v89\n"
        "v_cvt_pk_bf16_f32 v115, v86, v87\n"
        "v_exp_f32 v90, v90\n"
        "v_exp_f32 v91, v91\n"
        "v_mfma_f32_32x32x16_bf16 v[128:143], a[176:179], v[112:115], v[128:143]\n"
        "buffer_load_dwordx4 a[224:227], v25, s[16:19], s36 offen offset:0\n"
        "v_cvt_pk_bf16_f32 v116, v88, v89\n"
        "v_exp_f32 v92, v92\n"
        "v_exp_f32 v93, v93\n"
        "v_cvt_pk_bf16_f32 v117, v90, v91\n"
        "v_exp_f32 v94, v94\n"
        "v_exp_f32 v95, v95\n"
        "v_cvt_pk_bf16_f32 v118, v92, v93\n"
        "v_exp_f32 v96, v96\n"
        "v_exp_f32 v97, v97\n"
        "v_cvt_pk_bf16_f32 v119, v94, v95\n"
        "v_exp_f32 v98, v98\n"
        "v_exp_f32 v99, v99\n"
        "v_mfma_f32_32x32x16_bf16 v[128:143], a[180:183], v[116:119], v[128:143]\n"
        "buffer_load_dwordx4 a[228:231], v25, s[16:19], s36 offen offset:1024\n"
        "v_cvt_pk_bf16_f32 v112, v96, v97\n"
        "v_exp_f32 v100, v100\n"
        "v_exp_f32 v101, v101\n"
        "v_cvt_pk_bf16_f32 v113, v98, v99\n"
        "v_exp_f32 v102, v102\n"
        "v_lshlrev_b32 v174, 16, v227\n"
        "v_and_b32 v175, 0xffff0000, v227\n"
        "v_lshlrev_b32 v190, 16, v227\n"
        "v_and_b32 v191, 0xffff0000, v227\n"
        "v_exp_f32 v103, v103\n"
        "v_cvt_pk_bf16_f32 v114, v100, v101\n"
        "v_mfma_f32_32x32x16_bf16 v[80:95], a[0:3], v[36:39], v[64:79]\n"
        "buffer_load_dwordx4 a[232:235], v25, s[16:19], s36 offen offset:2048\n"
        "v_exp_f32 v104, v104\n"
        "v_exp_f32 v105, v105\n"
        "v_cvt_pk_bf16_f32 v115, v102, v103\n"
        "v_exp_f32 v106, v106\n"
        "v_exp_f32 v107, v107\n"
        "v_mfma_f32_32x32x16_bf16 v[128:143], a[184:187], v[112:115], v[128:143]\n"
        "buffer_load_dwordx4 a[236:239], v25, s[16:19], s36 offen offset:3072\n"
        "v_cvt_pk_bf16_f32 v116, v104, v105\n"
        "v_exp_f32 v108, v108\n"
        "v_exp_f32 v109, v109\n"
        "v_cvt_pk_bf16_f32 v117, v106, v107\n"
        "v_exp_f32 v110, v110\n"
        "v_exp_f32 v111, v111\n"
        "v_cvt_pk_bf16_f32 v118, v108, v109\n"
        "v_exp_f32 v80, v80\n"
        "v_exp_f32 v81, v81\n"
        "v_cvt_pk_bf16_f32 v119, v110, v111\n"
        "v_exp_f32 v82, v82\n"
        "v_exp_f32 v83, v83\n"
        "v_mfma_f32_32x32x16_bf16 v[128:143], a[188:191], v[116:119], v[128:143]\n"
        "s_mul_i32 s41, s28, 0x9000\n"
        "s_add_u32 m0, s41, 0x0\n"
        "s_add_u32 s42, m0, s43\n"
        "buffer_load_dwordx4 v1, s[16:19], s42 offen offset:0 lds\n"
        "v_cvt_pk_bf16_f32 v112, v80, v81\n"
        "v_exp_f32 v84, v84\n"
        "v_exp_f32 v85, v85\n"
        "v_cvt_pk_bf16_f32 v113, v82, v83\n"
        "v_exp_f32 v86, v86\n"
        "v_exp_f32 v87, v87\n"
        "v_cvt_pk_bf16_f32 v114, v84, v85\n"
        "v_mfma_f32_32x32x16_bf16 v[96:111], a[4:7], v[36:39], v[64:79]\n"
        "buffer_load_dwordx4 a[240:243], v26, s[16:19], s36 offen offset:0\n"
        "buffer_load_dwordx4 a[244:247], v26, s[16:19], s36 offen offset:1024\n"
        "v_exp_f32 v88, v88\n"
        "v_exp_f32 v89, v89\n"
        "v_cvt_pk_bf16_f32 v115, v86, v87\n"
        "v_exp_f32 v90, v90\n"
        "v_exp_f32 v91, v91\n"
        "v_mfma_f32_32x32x16_bf16 v[144:159], a[64:67], v[112:115], 0\n"
        "buffer_load_dwordx4 a[248:251], v26, s[16:19], s36 offen offset:2048\n"
        "buffer_load_dwordx4 a[252:255], v26, s[16:19], s36 offen offset:3072\n"
        "buffer_load_dwordx4 v1, s[16:19], s42 offen offset:1024 lds\n"
        "v_cvt_pk_bf16_f32 v116, v88, v89\n"
        "v_exp_f32 v92, v92\n"
        "v_exp_f32 v93, v93\n"
        "v_cvt_pk_bf16_f32 v117, v90, v91\n"
        "v_exp_f32 v94, v94\n"
        "v_exp_f32 v95, v95\n"
        "v_cvt_pk_bf16_f32 v118, v92, v93\n"
        "v_exp_f32 v96, v96\n"
        "v_exp_f32 v97, v97\n"
        "v_cvt_pk_bf16_f32 v119, v94, v95\n"
        "v_exp_f32 v98, v98\n"
        "v_exp_f32 v99, v99\n"
        "v_mfma_f32_32x32x16_bf16 v[144:159], a[68:71], v[116:119], v[144:159]\n"
        "ds_write_b96 v5, v[132:134]\n"
        "v_cvt_pk_bf16_f32 v112, v96, v97\n"
        "v_exp_f32 v100, v100\n"
        "v_exp_f32 v101, v101\n"
        "v_cvt_pk_bf16_f32 v113, v98, v99\n"
        "v_exp_f32 v102, v102\n"
        "v_exp_f32 v103, v103\n"
        "v_cvt_pk_bf16_f32 v114, v100, v101\n"
        "v_mfma_f32_32x32x16_bf16 v[80:95], a[8:11], v[36:39], v[64:79]\n"
        "ds_write_b96 v6, v[136:138]\n"
        "v_exp_f32 v104, v104\n"
        "v_exp_f32 v105, v105\n"
        "v_cvt_pk_bf16_f32 v115, v102, v103\n"
        "v_exp_f32 v106, v106\n"
        "v_exp_f32 v107, v107\n"
        "v_mfma_f32_32x32x16_bf16 v[144:159], a[72:75], v[112:115], v[144:159]\n"
        "ds_write_b96 v7, v[140:142]\n"
        "v_cvt_pk_bf16_f32 v116, v104, v105\n"
        "v_exp_f32 v108, v108\n"
        "v_exp_f32 v109, v109\n"
        "v_cvt_pk_bf16_f32 v117, v106, v107\n"
        "v_exp_f32 v110, v110\n"
        "v_exp_f32 v111, v111\n"
        "v_cvt_pk_bf16_f32 v118, v108, v109\n"
        "v_exp_f32 v80, v80\n"
        "v_exp_f32 v81, v81\n"
        "v_cvt_pk_bf16_f32 v119, v110, v111\n"
        "v_exp_f32 v82, v82\n"
        "v_exp_f32 v83, v83\n"
        "v_mfma_f32_32x32x16_bf16 v[144:159], a[76:79], v[116:119], v[144:159]\n"
        "s_waitcnt lgkmcnt(0)\n"
        "s_barrier\n"
        "v_cvt_pk_bf16_f32 v112, v80, v81\n"
        "v_exp_f32 v84, v84\n"
        "v_exp_f32 v85, v85\n"
        "v_cvt_pk_bf16_f32 v113, v82, v83\n"
        "v_exp_f32 v86, v86\n"
        "v_exp_f32 v87, v87\n"
        "v_cvt_pk_bf16_f32 v114, v84, v85\n"
        "v_mfma_f32_32x32x16_bf16 v[96:111], a[12:15], v[36:39], v[64:79]\n"
        "ds_read_b128 v[192:195], v8\n"
        "v_exp_f32 v88, v88\n"
        "v_exp_f32 v89, v89\n"
        "v_cvt_pk_bf16_f32 v115, v86, v87\n"
        "v_exp_f32 v90, v90\n"
        "v_exp_f32 v91, v91\n"
        "v_mfma_f32_32x32x16_bf16 v[144:159], a[80:83], v[112:115], v[144:159]\n"
        "ds_read_b128 v[196:199], v8 offset:1024\n"
        "v_cvt_pk_bf16_f32 v116, v88, v89\n"
        "v_exp_f32 v92, v92\n"
        "v_exp_f32 v93, v93\n"
        "v_cvt_pk_bf16_f32 v117, v90, v91\n"
        "v_exp_f32 v94, v94\n"
        "v_exp_f32 v95, v95\n"
        "v_cvt_pk_bf16_f32 v118, v92, v93\n"
        "v_exp_f32 v96, v96\n"
        "v_exp_f32 v97, v97\n"
        "v_cvt_pk_bf16_f32 v119, v94, v95\n"
        "v_exp_f32 v98, v98\n"
        "v_exp_f32 v99, v99\n"
        "v_mfma_f32_32x32x16_bf16 v[144:159], a[84:87], v[116:119], v[144:159]\n"
        "ds_read_b128 v[200:203], v8 offset:2048\n"
        "v_cvt_pk_bf16_f32 v112, v96, v97\n"
        "v_exp_f32 v100, v100\n"
        "v_exp_f32 v101, v101\n"
        "v_cvt_pk_bf16_f32 v113, v98, v99\n"
        "v_exp_f32 v102, v102\n"
        "v_exp_f32 v103, v103\n"
        "v_cvt_pk_bf16_f32 v114, v100, v101\n"
        "v_mfma_f32_32x32x16_bf16 v[80:95], a[16:19], v[36:39], v[64:79]\n"
        "buffer_load_dwordx4 v[32:35], v1, s[16:19], s59 offen offset:2048\n"
        "v_exp_f32 v104, v104\n"
        "v_exp_f32 v105, v105\n"
        "v_cvt_pk_bf16_f32 v115, v102, v103\n"
        "v_exp_f32 v106, v106\n"
        "v_exp_f32 v107, v107\n"
        "v_mfma_f32_32x32x16_bf16 v[144:159], a[88:91], v[112:115], v[144:159]\n"
        "s_waitcnt lgkmcnt(0)\n"
        "v_cvt_pk_bf16_f32 v116, v104, v105\n"
        "v_exp_f32 v108, v108\n"
        "v_exp_f32 v109, v109\n"
        "v_cvt_pk_bf16_f32 v117, v106, v107\n"
        "v_exp_f32 v110, v110\n"
        "v_add_f32 v212, v128, v192\n"
        "v_add_f32 v213, v129, v193\n"
        "v_add_f32 v214, v130, v194\n"
        "v_exp_f32 v111, v111\n"
        "v_cvt_pk_bf16_f32 v118, v108, v109\n"
        "v_exp_f32 v80, v80\n"
        "v_exp_f32 v81, v81\n"
        "v_cvt_pk_bf16_f32 v119, v110, v111\n"
        "v_exp_f32 v82, v82\n"
        "v_exp_f32 v83, v83\n"
        "v_mfma_f32_32x32x16_bf16 v[144:159], a[92:95], v[116:119], v[144:159]\n"
        "buffer_load_dwordx4 v1, s[16:19], s42 offen offset:2048 lds\n"
        "v_cvt_pk_bf16_f32 v112, v80, v81\n"
        "v_exp_f32 v84, v84\n"
        "v_exp_f32 v85, v85\n"
        "v_cvt_pk_bf16_f32 v113, v82, v83\n"
        "v_exp_f32 v86, v86\n"
        "v_add_f32 v196, v196, v200\n"
        "v_add_f32 v197, v197, v201\n"
        "v_add_f32 v198, v198, v202\n"
        "v_exp_f32 v87, v87\n"
        "v_cvt_pk_bf16_f32 v114, v84, v85\n"
        "v_mfma_f32_32x32x16_bf16 v[96:111], a[20:23], v[36:39], v[64:79]\n"
        "v_add_f32 v212, v212, v196\n"
        "v_add_f32 v213, v213, v197\n"
        "v_add_f32 v214, v214, v198\n"
        "v_exp_f32 v88, v88\n"
        "v_exp_f32 v89, v89\n"
        "v_cvt_pk_bf16_f32 v115, v86, v87\n"
        "v_exp_f32 v90, v90\n"
        "v_exp_f32 v91, v91\n"
        "v_mfma_f32_32x32x16_bf16 v[144:159], a[96:99], v[112:115], v[144:159]\n"
        "v_rcp_f32 v214, v214\n"
        "v_cvt_pk_bf16_f32 v116, v88, v89\n"
        "v_exp_f32 v92, v92\n"
        "v_exp_f32 v93, v93\n"
        "v_cvt_pk_bf16_f32 v117, v90, v91\n"
        "v_exp_f32 v94, v94\n"
        "v_mul_f32 v212, v212, v214\n"
        "v_mul_f32 v213, v213, v214\n"
        "v_exp_f32 v95, v95\n"
        "v_cvt_pk_bf16_f32 v118, v92, v93\n"
        "v_exp_f32 v96, v96\n"
        "v_exp_f32 v97, v97\n"
        "v_cvt_pk_bf16_f32 v119, v94, v95\n"
        "v_exp_f32 v98, v98\n"
        "v_exp_f32 v99, v99\n"
        "v_mfma_f32_32x32x16_bf16 v[144:159], a[100:103], v[116:119], v[144:159]\n"
        "v_cvt_pk_bf16_f32 v212, v212, v213\n"
        "v_cvt_pk_bf16_f32 v112, v96, v97\n"
        "v_exp_f32 v100, v100\n"
        "v_exp_f32 v101, v101\n"
        "v_cvt_pk_bf16_f32 v113, v98, v99\n"
        "v_exp_f32 v102, v102\n"
        "v_exp_f32 v103, v103\n"
        "v_cvt_pk_bf16_f32 v114, v100, v101\n"
        "v_mfma_f32_32x32x16_bf16 v[80:95], a[24:27], v[36:39], v[64:79]\n"
        "ds_write_b32 v9, v212\n"
        "v_exp_f32 v104, v104\n"
        "v_exp_f32 v105, v105\n"
        "v_cvt_pk_bf16_f32 v115, v102, v103\n"
        "v_exp_f32 v106, v106\n"
        "v_exp_f32 v107, v107\n"
        "v_mfma_f32_32x32x16_bf16 v[144:159], a[104:107], v[112:115], v[144:159]\n"
        "buffer_load_dwordx4 v1, s[16:19], s42 offen offset:3072 lds\n"
        "v_cvt_pk_bf16_f32 v116, v104, v105\n"
        "v_exp_f32 v108, v108\n"
        "v_exp_f32 v109, v109\n"
        "v_cvt_pk_bf16_f32 v117, v106, v107\n"
        "v_exp_f32 v110, v110\n"
        "v_exp_f32 v111, v111\n"
        "v_cvt_pk_bf16_f32 v118, v108, v109\n"
        "v_exp_f32 v80, v80\n"
        "v_exp_f32 v81, v81\n"
        "v_cvt_pk_bf16_f32 v119, v110, v111\n"
        "v_exp_f32 v82, v82\n"
        "v_exp_f32 v83, v83\n"
        "v_mfma_f32_32x32x16_bf16 v[144:159], a[108:111], v[116:119], v[144:159]\n"
        "s_waitcnt lgkmcnt(0)\n"
        "s_barrier\n"
        "s_add_u32 s36, s35, 0x4000\n"
        "v_cvt_pk_bf16_f32 v112, v80, v81\n"
        "v_exp_f32 v84, v84\n"
        "v_exp_f32 v85, v85\n"
        "v_cvt_pk_bf16_f32 v113, v82, v83\n"
        "v_exp_f32 v86, v86\n"
        "v_exp_f32 v87, v87\n"
        "v_cvt_pk_bf16_f32 v114, v84, v85\n"
        "v_mfma_f32_32x32x16_bf16 v[96:111], a[28:31], v[36:39], v[64:79]\n"
        "ds_read_b128 v[204:207], v10\n"
        "v_exp_f32 v88, v88\n"
        "v_exp_f32 v89, v89\n"
        "v_cvt_pk_bf16_f32 v115, v86, v87\n"
        "v_exp_f32 v90, v90\n"
        "v_exp_f32 v91, v91\n"
        "v_mfma_f32_32x32x16_bf16 v[144:159], a[112:115], v[112:115], v[144:159]\n"
        "ds_read_b128 v[208:211], v10 offset:512\n"
        "v_cvt_pk_bf16_f32 v116, v88, v89\n"
        "v_exp_f32 v92, v92\n"
        "v_exp_f32 v93, v93\n"
        "v_cvt_pk_bf16_f32 v117, v90, v91\n"
        "v_exp_f32 v94, v94\n"
        "v_exp_f32 v95, v95\n"
        "v_cvt_pk_bf16_f32 v118, v92, v93\n"
        "v_exp_f32 v96, v96\n"
        "v_exp_f32 v97, v97\n"
        "v_cvt_pk_bf16_f32 v119, v94, v95\n"
        "v_exp_f32 v98, v98\n"
        "v_exp_f32 v99, v99\n"
        "v_mfma_f32_32x32x16_bf16 v[144:159], a[116:119], v[116:119], v[144:159]\n"
        "s_waitcnt vmcnt(4) lgkmcnt(0)\n"
        "v_cvt_pk_bf16_f32 v112, v96, v97\n"
        "v_exp_f32 v100, v100\n"
        "v_exp_f32 v101, v101\n"
        "v_cvt_pk_bf16_f32 v113, v98, v99\n"
        "v_exp_f32 v102, v102\n"
        "v_mfma_f32_16x16x32_bf16 v[160:163], a[192:195], v[204:207], v[160:163]\n"
        "v_exp_f32 v103, v103\n"
        "v_cvt_pk_bf16_f32 v114, v100, v101\n"
        "v_mfma_f32_32x32x16_bf16 v[80:95], a[32:35], v[36:39], v[64:79]\n"
        "v_exp_f32 v104, v104\n"
        "v_exp_f32 v105, v105\n"
        "v_cvt_pk_bf16_f32 v115, v102, v103\n"
        "v_exp_f32 v106, v106\n"
        "v_mfma_f32_16x16x32_bf16 v[164:167], a[196:199], v[204:207], v[164:167]\n"
        "v_exp_f32 v107, v107\n"
        "v_mfma_f32_32x32x16_bf16 v[144:159], a[120:123], v[112:115], v[144:159]\n"
        "v_cvt_pk_bf16_f32 v116, v104, v105\n"
        "v_exp_f32 v108, v108\n"
        "v_exp_f32 v109, v109\n"
        "v_cvt_pk_bf16_f32 v117, v106, v107\n"
        "v_exp_f32 v110, v110\n"
        "v_mfma_f32_16x16x32_bf16 v[168:171], a[200:203], v[204:207], v[168:171]\n"
        "v_exp_f32 v111, v111\n"
        "v_cvt_pk_bf16_f32 v118, v108, v109\n"
        "v_exp_f32 v80, v80\n"
        "v_exp_f32 v81, v81\n"
        "v_cvt_pk_bf16_f32 v119, v110, v111\n"
        "v_exp_f32 v82, v82\n"
        "v_exp_f32 v83, v83\n"
        "v_mfma_f32_32x32x16_bf16 v[144:159], a[124:127], v[116:119], v[144:159]\n"
        "buffer_load_dwordx4 a[192:195], v1, s[16:19], s36 offen offset:0\n"
        "v_cvt_pk_bf16_f32 v112, v80, v81\n"
        "v_exp_f32 v84, v84\n"
        "v_exp_f32 v85, v85\n"
        "v_cvt_pk_bf16_f32 v113, v82, v83\n"
        "v_exp_f32 v86, v86\n"
        "v_mfma_f32_16x16x32_bf16 v[172:175], a[204:207], v[204:207], v[172:175]\n"
        "v_exp_f32 v87, v87\n"
        "v_cvt_pk_bf16_f32 v114, v84, v85\n"
        "v_mfma_f32_32x32x16_bf16 v[96:111], a[36:39], v[36:39], v[64:79]\n"
        "buffer_load_dwordx4 a[196:199], v1, s[16:19], s36 offen offset:1024\n"
        "v_exp_f32 v88, v88\n"
        "v_exp_f32 v89, v89\n"
        "v_cvt_pk_bf16_f32 v115, v86, v87\n"
        "v_exp_f32 v90, v90\n"
        "v_mfma_f32_16x16x32_bf16 v[176:179], a[208:211], v[204:207], v[176:179]\n"
        "v_exp_f32 v91, v91\n"
        "v_mfma_f32_32x32x16_bf16 v[144:159], a[128:131], v[112:115], v[144:159]\n"
        "buffer_load_dwordx4 a[200:203], v1, s[16:19], s36 offen offset:2048\n"
        "v_cvt_pk_bf16_f32 v116, v88, v89\n"
        "v_exp_f32 v92, v92\n"
        "v_exp_f32 v93, v93\n"
        "v_cvt_pk_bf16_f32 v117, v90, v91\n"
        "v_exp_f32 v94, v94\n"
        "v_mfma_f32_16x16x32_bf16 v[180:183], a[212:215], v[204:207], v[180:183]\n"
        "v_exp_f32 v95, v95\n"
        "v_cvt_pk_bf16_f32 v118, v92, v93\n"
        "v_exp_f32 v96, v96\n"
        "v_exp_f32 v97, v97\n"
        "v_cvt_pk_bf16_f32 v119, v94, v95\n"
        "v_exp_f32 v98, v98\n"
        "v_exp_f32 v99, v99\n"
        "v_mfma_f32_32x32x16_bf16 v[144:159], a[132:135], v[116:119], v[144:159]\n"
        "buffer_load_dwordx4 a[204:207], v1, s[16:19], s36 offen offset:3072\n"
        "v_cvt_pk_bf16_f32 v112, v96, v97\n"
        "v_exp_f32 v100, v100\n"
        "v_exp_f32 v101, v101\n"
        "v_cvt_pk_bf16_f32 v113, v98, v99\n"
        "v_exp_f32 v102, v102\n"
        "v_mfma_f32_16x16x32_bf16 v[184:187], a[216:219], v[204:207], v[184:187]\n"
        "v_exp_f32 v103, v103\n"
        "v_cvt_pk_bf16_f32 v114, v100, v101\n"
        "v_mfma_f32_32x32x16_bf16 v[80:95], a[40:43], v[36:39], v[64:79]\n"
        "buffer_load_dwordx4 a[208:211], v24, s[16:19], s36 offen offset:0\n"
        "v_exp_f32 v104, v104\n"
        "v_exp_f32 v105, v105\n"
        "v_cvt_pk_bf16_f32 v115, v102, v103\n"
        "v_exp_f32 v106, v106\n"
        "v_mfma_f32_16x16x32_bf16 v[188:191], a[220:223], v[204:207], v[188:191]\n"
        "v_exp_f32 v107, v107\n"
        "v_mfma_f32_32x32x16_bf16 v[144:159], a[136:139], v[112:115], v[144:159]\n"
        "buffer_load_dwordx4 a[212:215], v24, s[16:19], s36 offen offset:1024\n"
        "v_cvt_pk_bf16_f32 v116, v104, v105\n"
        "v_exp_f32 v108, v108\n"
        "v_exp_f32 v109, v109\n"
        "v_cvt_pk_bf16_f32 v117, v106, v107\n"
        "v_exp_f32 v110, v110\n"
        "v_mfma_f32_16x16x32_bf16 v[160:163], a[224:227], v[208:211], v[160:163]\n"
        "v_exp_f32 v111, v111\n"
        "v_cvt_pk_bf16_f32 v118, v108, v109\n"
        "v_exp_f32 v80, v80\n"
        "v_exp_f32 v81, v81\n"
        "v_cvt_pk_bf16_f32 v119, v110, v111\n"
        "v_exp_f32 v82, v82\n"
        "v_exp_f32 v83, v83\n"
        "v_mfma_f32_32x32x16_bf16 v[144:159], a[140:143], v[116:119], v[144:159]\n"
        "buffer_load_dwordx4 a[216:219], v24, s[16:19], s36 offen offset:2048\n"
        "v_cvt_pk_bf16_f32 v112, v80, v81\n"
        "v_exp_f32 v84, v84\n"
        "v_exp_f32 v85, v85\n"
        "v_cvt_pk_bf16_f32 v113, v82, v83\n"
        "v_exp_f32 v86, v86\n"
        "v_mfma_f32_16x16x32_bf16 v[164:167], a[228:231], v[208:211], v[164:167]\n"
        "v_exp_f32 v87, v87\n"
        "v_cvt_pk_bf16_f32 v114, v84, v85\n"
        "v_mfma_f32_32x32x16_bf16 v[96:111], a[44:47], v[36:39], v[64:79]\n"
        "buffer_load_dwordx4 a[220:223], v24, s[16:19], s36 offen offset:3072\n"
        "v_exp_f32 v88, v88\n"
        "v_exp_f32 v89, v89\n"
        "v_cvt_pk_bf16_f32 v115, v86, v87\n"
        "v_exp_f32 v90, v90\n"
        "v_mfma_f32_16x16x32_bf16 v[168:171], a[232:235], v[208:211], v[168:171]\n"
        "v_exp_f32 v91, v91\n"
        "v_mfma_f32_32x32x16_bf16 v[144:159], a[144:147], v[112:115], v[144:159]\n"
        "buffer_load_dwordx4 a[224:227], v25, s[16:19], s36 offen offset:0\n"
        "v_cvt_pk_bf16_f32 v116, v88, v89\n"
        "v_exp_f32 v92, v92\n"
        "v_exp_f32 v93, v93\n"
        "v_cvt_pk_bf16_f32 v117, v90, v91\n"
        "v_exp_f32 v94, v94\n"
        "v_mfma_f32_16x16x32_bf16 v[172:175], a[236:239], v[208:211], v[172:175]\n"
        "v_exp_f32 v95, v95\n"
        "v_cvt_pk_bf16_f32 v118, v92, v93\n"
        "v_exp_f32 v96, v96\n"
        "v_exp_f32 v97, v97\n"
        "v_cvt_pk_bf16_f32 v119, v94, v95\n"
        "v_exp_f32 v98, v98\n"
        "v_exp_f32 v99, v99\n"
        "v_mfma_f32_32x32x16_bf16 v[144:159], a[148:151], v[116:119], v[144:159]\n"
        "buffer_load_dwordx4 a[228:231], v25, s[16:19], s36 offen offset:1024\n"
        "v_cvt_pk_bf16_f32 v112, v96, v97\n"
        "v_exp_f32 v100, v100\n"
        "v_exp_f32 v101, v101\n"
        "v_cvt_pk_bf16_f32 v113, v98, v99\n"
        "v_exp_f32 v102, v102\n"
        "v_mfma_f32_16x16x32_bf16 v[176:179], a[240:243], v[208:211], v[176:179]\n"
        "v_exp_f32 v103, v103\n"
        "v_cvt_pk_bf16_f32 v114, v100, v101\n"
        "v_mfma_f32_32x32x16_bf16 v[80:95], a[48:51], v[36:39], v[64:79]\n"
        "buffer_load_dwordx4 a[232:235], v25, s[16:19], s36 offen offset:2048\n"
        "v_exp_f32 v104, v104\n"
        "v_exp_f32 v105, v105\n"
        "v_cvt_pk_bf16_f32 v115, v102, v103\n"
        "v_exp_f32 v106, v106\n"
        "v_mfma_f32_16x16x32_bf16 v[180:183], a[244:247], v[208:211], v[180:183]\n"
        "v_exp_f32 v107, v107\n"
        "v_mfma_f32_32x32x16_bf16 v[144:159], a[152:155], v[112:115], v[144:159]\n"
        "buffer_load_dwordx4 a[236:239], v25, s[16:19], s36 offen offset:3072\n"
        "v_cvt_pk_bf16_f32 v116, v104, v105\n"
        "v_exp_f32 v108, v108\n"
        "v_exp_f32 v109, v109\n"
        "v_cvt_pk_bf16_f32 v117, v106, v107\n"
        "v_exp_f32 v110, v110\n"
        "v_mfma_f32_16x16x32_bf16 v[184:187], a[248:251], v[208:211], v[184:187]\n"
        "v_exp_f32 v111, v111\n"
        "v_cvt_pk_bf16_f32 v118, v108, v109\n"
        "v_exp_f32 v80, v80\n"
        "v_exp_f32 v81, v81\n"
        "v_cvt_pk_bf16_f32 v119, v110, v111\n"
        "v_exp_f32 v82, v82\n"
        "v_exp_f32 v83, v83\n"
        "v_mfma_f32_32x32x16_bf16 v[144:159], a[156:159], v[116:119], v[144:159]\n"
        "buffer_load_dwordx4 a[240:243], v26, s[16:19], s36 offen offset:0\n"
        "v_cvt_pk_bf16_f32 v112, v80, v81\n"
        "v_exp_f32 v84, v84\n"
        "v_exp_f32 v85, v85\n"
        "v_cvt_pk_bf16_f32 v113, v82, v83\n"
        "v_exp_f32 v86, v86\n"
        "v_mfma_f32_16x16x32_bf16 v[188:191], a[252:255], v[208:211], v[188:191]\n"
        "v_exp_f32 v87, v87\n"
        "v_cvt_pk_bf16_f32 v114, v84, v85\n"
        "v_mfma_f32_32x32x16_bf16 v[96:111], a[52:55], v[36:39], v[64:79]\n"
        "buffer_load_dwordx4 a[244:247], v26, s[16:19], s36 offen offset:1024\n"
        "v_exp_f32 v88, v88\n"
        "v_exp_f32 v89, v89\n"
        "v_cvt_pk_bf16_f32 v115, v86, v87\n"
        "v_exp_f32 v90, v90\n"
        "v_exp_f32 v91, v91\n"
        "v_mfma_f32_32x32x16_bf16 v[144:159], a[160:163], v[112:115], v[144:159]\n"
        "buffer_load_dwordx4 a[248:251], v26, s[16:19], s36 offen offset:2048\n"
        "v_cvt_pk_bf16_f32 v116, v88, v89\n"
        "v_exp_f32 v92, v92\n"
        "v_exp_f32 v93, v93\n"
        "v_cvt_pk_bf16_f32 v117, v90, v91\n"
        "v_exp_f32 v94, v94\n"
        "v_exp_f32 v95, v95\n"
        "v_cvt_pk_bf16_f32 v118, v92, v93\n"
        "v_exp_f32 v96, v96\n"
        "v_exp_f32 v97, v97\n"
        "v_cvt_pk_bf16_f32 v119, v94, v95\n"
        "v_exp_f32 v98, v98\n"
        "v_exp_f32 v99, v99\n"
        "v_mfma_f32_32x32x16_bf16 v[144:159], a[164:167], v[116:119], v[144:159]\n"
        "buffer_load_dwordx4 a[252:255], v26, s[16:19], s36 offen offset:3072\n"
        "v_cvt_pk_bf16_f32 v112, v96, v97\n"
        "v_exp_f32 v100, v100\n"
        "v_exp_f32 v101, v101\n"
        "v_cvt_pk_bf16_f32 v113, v98, v99\n"
        "v_exp_f32 v102, v102\n"
        "v_exp_f32 v103, v103\n"
        "v_cvt_pk_bf16_f32 v114, v100, v101\n"
        "v_mfma_f32_32x32x16_bf16 v[80:95], a[56:59], v[36:39], v[64:79]\n"
        "v_mov_b32 v16, -1\n"
        "v_mov_b32 v17, -1\n"
        "v_mov_b32 v18, -1\n"
        "v_mov_b32 v19, -1\n"
        "v_exp_f32 v104, v104\n"
        "v_exp_f32 v105, v105\n"
        "v_cvt_pk_bf16_f32 v115, v102, v103\n"
        "v_exp_f32 v106, v106\n"
        "v_exp_f32 v107, v107\n"
        "v_mfma_f32_32x32x16_bf16 v[144:159], a[168:171], v[112:115], v[144:159]\n"
        "v_cvt_pk_bf16_f32 v116, v104, v105\n"
        "v_exp_f32 v108, v108\n"
        "v_exp_f32 v109, v109\n"
        "v_cvt_pk_bf16_f32 v117, v106, v107\n"
        "v_exp_f32 v110, v110\n"
        "v_exp_f32 v111, v111\n"
        "v_cvt_pk_bf16_f32 v118, v108, v109\n"
        "v_exp_f32 v80, v80\n"
        "v_exp_f32 v81, v81\n"
        "v_cvt_pk_bf16_f32 v119, v110, v111\n"
        "v_exp_f32 v82, v82\n"
        "v_exp_f32 v83, v83\n"
        "v_mfma_f32_32x32x16_bf16 v[144:159], a[172:175], v[116:119], v[144:159]\n"
        "s_add_u32 m0, s41, 0x1000\n"
        "s_add_u32 s42, m0, s43\n"
        "buffer_load_dwordx4 v1, s[16:19], s42 offen offset:0 lds\n"
        "v_cvt_pk_bf16_f32 v112, v80, v81\n"
        "v_exp_f32 v84, v84\n"
        "v_exp_f32 v85, v85\n"
        "v_cvt_pk_bf16_f32 v113, v82, v83\n"
        "v_exp_f32 v86, v86\n"
        "v_exp_f32 v87, v87\n"
        "v_cvt_pk_bf16_f32 v114, v84, v85\n"
        "v_mfma_f32_32x32x16_bf16 v[96:111], a[60:63], v[36:39], v[64:79]\n"
        "buffer_store_dwordx4 v[16:19], v1, s[16:19], s56 offen offset:3072 sc1\n"
        "v_exp_f32 v88, v88\n"
        "v_exp_f32 v89, v89\n"
        "v_cvt_pk_bf16_f32 v115, v86, v87\n"
        "v_exp_f32 v90, v90\n"
        "v_exp_f32 v91, v91\n"
        "v_mfma_f32_32x32x16_bf16 v[144:159], a[176:179], v[112:115], v[144:159]\n"
        "v_cvt_pk_bf16_f32 v116, v88, v89\n"
        "v_exp_f32 v92, v92\n"
        "v_exp_f32 v93, v93\n"
        "v_cvt_pk_bf16_f32 v117, v90, v91\n"
        "v_exp_f32 v94, v94\n"
        "v_exp_f32 v95, v95\n"
        "v_cvt_pk_bf16_f32 v118, v92, v93\n"
        "v_exp_f32 v96, v96\n"
        "v_exp_f32 v97, v97\n"
        "v_cvt_pk_bf16_f32 v119, v94, v95\n"
        "v_exp_f32 v98, v98\n"
        "v_exp_f32 v99, v99\n"
        "v_mfma_f32_32x32x16_bf16 v[144:159], a[180:183], v[116:119], v[144:159]\n"
        "buffer_load_dwordx4 v1, s[16:19], s42 offen offset:1024 lds\n"
        "v_cvt_pk_bf16_f32 v112, v96, v97\n"
        "v_exp_f32 v100, v100\n"
        "v_exp_f32 v101, v101\n"
        "v_cvt_pk_bf16_f32 v113, v98, v99\n"
        "v_exp_f32 v102, v102\n"
        "v_exp_f32 v103, v103\n"
        "v_cvt_pk_bf16_f32 v114, v100, v101\n"
        "s_waitcnt vmcnt(21)\n"
        "v_mfma_f32_32x32x16_bf16 v[80:95], a[0:3], v[32:35], v[64:79]\n"
        "buffer_store_dwordx4 v[16:19], v24, s[16:19], s56 offen offset:0 sc1\n"
        "v_exp_f32 v104, v104\n"
        "v_exp_f32 v105, v105\n"
        "v_cvt_pk_bf16_f32 v115, v102, v103\n"
        "v_exp_f32 v106, v106\n"
        "v_exp_f32 v107, v107\n"
        "v_mfma_f32_32x32x16_bf16 v[144:159], a[184:187], v[112:115], v[144:159]\n"
        "v_cvt_pk_bf16_f32 v116, v104, v105\n"
        "v_exp_f32 v108, v108\n"
        "v_exp_f32 v109, v109\n"
        "v_cvt_pk_bf16_f32 v117, v106, v107\n"
        "v_exp_f32 v110, v110\n"
        "v_exp_f32 v111, v111\n"
        "v_cvt_pk_bf16_f32 v118, v108, v109\n"
        "v_exp_f32 v80, v80\n"
        "v_exp_f32 v81, v81\n"
        "v_cvt_pk_bf16_f32 v119, v110, v111\n"
        "v_exp_f32 v82, v82\n"
        "v_exp_f32 v83, v83\n"
        "v_mfma_f32_32x32x16_bf16 v[144:159], a[188:191], v[116:119], v[144:159]\n"
        "buffer_load_dwordx4 v1, s[16:19], s42 offen offset:2048 lds\n"
        "v_cvt_pk_bf16_f32 v112, v80, v81\n"
        "v_exp_f32 v84, v84\n"
        "v_exp_f32 v85, v85\n"
        "v_cvt_pk_bf16_f32 v113, v82, v83\n"
        "v_exp_f32 v86, v86\n"
        "v_exp_f32 v87, v87\n"
        "v_cvt_pk_bf16_f32 v114, v84, v85\n"
        "v_mfma_f32_32x32x16_bf16 v[96:111], a[4:7], v[32:35], v[64:79]\n"
        "v_exp_f32 v88, v88\n"
        "v_exp_f32 v89, v89\n"
        "v_cvt_pk_bf16_f32 v115, v86, v87\n"
        "v_exp_f32 v90, v90\n"
        "v_exp_f32 v91, v91\n"
        "v_mfma_f32_32x32x16_bf16 v[128:143], a[64:67], v[112:115], 0\n"
        "buffer_load_dwordx4 v1, s[16:19], s42 offen offset:3072 lds\n"
        "v_cvt_pk_bf16_f32 v116, v88, v89\n"
        "v_exp_f32 v92, v92\n"
        "v_exp_f32 v93, v93\n"
        "v_cvt_pk_bf16_f32 v117, v90, v91\n"
        "v_exp_f32 v94, v94\n"
        "v_exp_f32 v95, v95\n"
        "v_cvt_pk_bf16_f32 v118, v92, v93\n"
        "v_exp_f32 v96, v96\n"
        "v_exp_f32 v97, v97\n"
        "v_cvt_pk_bf16_f32 v119, v94, v95\n"
        "v_exp_f32 v98, v98\n"
        "v_exp_f32 v99, v99\n"
        "v_mfma_f32_32x32x16_bf16 v[128:143], a[68:71], v[116:119], v[128:143]\n"
        "ds_write_b96 v5, v[148:150]\n"
        "v_cvt_pk_bf16_f32 v112, v96, v97\n"
        "v_exp_f32 v100, v100\n"
        "v_exp_f32 v101, v101\n"
        "v_cvt_pk_bf16_f32 v113, v98, v99\n"
        "v_exp_f32 v102, v102\n"
        "v_exp_f32 v103, v103\n"
        "v_cvt_pk_bf16_f32 v114, v100, v101\n"
        "v_mfma_f32_32x32x16_bf16 v[80:95], a[8:11], v[32:35], v[64:79]\n"
        "ds_write_b96 v6, v[152:154]\n"
        "v_exp_f32 v104, v104\n"
        "v_exp_f32 v105, v105\n"
        "v_cvt_pk_bf16_f32 v115, v102, v103\n"
        "v_exp_f32 v106, v106\n"
        "v_exp_f32 v107, v107\n"
        "v_mfma_f32_32x32x16_bf16 v[128:143], a[72:75], v[112:115], v[128:143]\n"
        "ds_write_b96 v7, v[156:158]\n"
        "v_cvt_pk_bf16_f32 v116, v104, v105\n"
        "v_exp_f32 v108, v108\n"
        "v_exp_f32 v109, v109\n"
        "v_cvt_pk_bf16_f32 v117, v106, v107\n"
        "v_exp_f32 v110, v110\n"
        "v_exp_f32 v111, v111\n"
        "v_cvt_pk_bf16_f32 v118, v108, v109\n"
        "v_exp_f32 v80, v80\n"
        "v_exp_f32 v81, v81\n"
        "v_cvt_pk_bf16_f32 v119, v110, v111\n"
        "v_exp_f32 v82, v82\n"
        "v_exp_f32 v83, v83\n"
        "v_mfma_f32_32x32x16_bf16 v[128:143], a[76:79], v[116:119], v[128:143]\n"
        "s_waitcnt lgkmcnt(0)\n"
        "s_barrier\n"
        "v_cvt_pk_bf16_f32 v112, v80, v81\n"
        "v_exp_f32 v84, v84\n"
        "v_exp_f32 v85, v85\n"
        "v_cvt_pk_bf16_f32 v113, v82, v83\n"
        "v_exp_f32 v86, v86\n"
        "v_exp_f32 v87, v87\n"
        "v_cvt_pk_bf16_f32 v114, v84, v85\n"
        "v_mfma_f32_32x32x16_bf16 v[96:111], a[12:15], v[32:35], v[64:79]\n"
        "ds_read_b128 v[192:195], v8\n"
        "v_exp_f32 v88, v88\n"
        "v_exp_f32 v89, v89\n"
        "v_cvt_pk_bf16_f32 v115, v86, v87\n"
        "v_exp_f32 v90, v90\n"
        "v_exp_f32 v91, v91\n"
        "v_mfma_f32_32x32x16_bf16 v[128:143], a[80:83], v[112:115], v[128:143]\n"
        "ds_read_b128 v[196:199], v8 offset:1024\n"
        "v_cvt_pk_bf16_f32 v116, v88, v89\n"
        "v_exp_f32 v92, v92\n"
        "v_exp_f32 v93, v93\n"
        "v_cvt_pk_bf16_f32 v117, v90, v91\n"
        "v_exp_f32 v94, v94\n"
        "v_exp_f32 v95, v95\n"
        "v_cvt_pk_bf16_f32 v118, v92, v93\n"
        "v_exp_f32 v96, v96\n"
        "v_exp_f32 v97, v97\n"
        "v_cvt_pk_bf16_f32 v119, v94, v95\n"
        "v_exp_f32 v98, v98\n"
        "v_exp_f32 v99, v99\n"
        "v_mfma_f32_32x32x16_bf16 v[128:143], a[84:87], v[116:119], v[128:143]\n"
        "ds_read_b128 v[200:203], v8 offset:2048\n"
        "v_cvt_pk_bf16_f32 v112, v96, v97\n"
        "v_exp_f32 v100, v100\n"
        "v_exp_f32 v101, v101\n"
        "v_cvt_pk_bf16_f32 v113, v98, v99\n"
        "v_exp_f32 v102, v102\n"
        "v_exp_f32 v103, v103\n"
        "v_cvt_pk_bf16_f32 v114, v100, v101\n"
        "v_mfma_f32_32x32x16_bf16 v[80:95], a[16:19], v[32:35], v[64:79]\n"
        "buffer_load_dwordx4 v[36:39], v1, s[16:19], s59 offen offset:3072\n"
        "v_exp_f32 v104, v104\n"
        "v_exp_f32 v105, v105\n"
        "v_cvt_pk_bf16_f32 v115, v102, v103\n"
        "v_exp_f32 v106, v106\n"
        "v_exp_f32 v107, v107\n"
        "v_mfma_f32_32x32x16_bf16 v[128:143], a[88:91], v[112:115], v[128:143]\n"
        "s_waitcnt lgkmcnt(0)\n"
        "v_cvt_pk_bf16_f32 v116, v104, v105\n"
        "v_exp_f32 v108, v108\n"
        "v_exp_f32 v109, v109\n"
        "v_cvt_pk_bf16_f32 v117, v106, v107\n"
        "v_exp_f32 v110, v110\n"
        "v_add_f32 v212, v144, v192\n"
        "v_add_f32 v213, v145, v193\n"
        "v_add_f32 v214, v146, v194\n"
        "v_exp_f32 v111, v111\n"
        "v_cvt_pk_bf16_f32 v118, v108, v109\n"
        "v_exp_f32 v80, v80\n"
        "v_exp_f32 v81, v81\n"
        "v_cvt_pk_bf16_f32 v119, v110, v111\n"
        "v_exp_f32 v82, v82\n"
        "v_exp_f32 v83, v83\n"
        "v_mfma_f32_32x32x16_bf16 v[128:143], a[92:95], v[116:119], v[128:143]\n"
        "s_add_u32 m0, s41, 0x2000\n"
        "s_add_u32 s42, m0, s43\n"
        "buffer_load_dwordx4 v1, s[16:19], s42 offen offset:0 lds\n"
        "v_cvt_pk_bf16_f32 v112, v80, v81\n"
        "v_exp_f32 v84, v84\n"
        "v_exp_f32 v85, v85\n"
        "v_cvt_pk_bf16_f32 v113, v82, v83\n"
        "v_exp_f32 v86, v86\n"
        "v_add_f32 v196, v196, v200\n"
        "v_add_f32 v197, v197, v201\n"
        "v_add_f32 v198, v198, v202\n"
        "v_exp_f32 v87, v87\n"
        "v_cvt_pk_bf16_f32 v114, v84, v85\n"
        "v_mfma_f32_32x32x16_bf16 v[96:111], a[20:23], v[32:35], v[64:79]\n"
        "v_add_f32 v212, v212, v196\n"
        "v_add_f32 v213, v213, v197\n"
        "v_add_f32 v214, v214, v198\n"
        "v_exp_f32 v88, v88\n"
        "v_exp_f32 v89, v89\n"
        "v_cvt_pk_bf16_f32 v115, v86, v87\n"
        "v_exp_f32 v90, v90\n"
        "v_exp_f32 v91, v91\n"
        "v_mfma_f32_32x32x16_bf16 v[128:143], a[96:99], v[112:115], v[128:143]\n"
        "v_rcp_f32 v214, v214\n"
        "v_cvt_pk_bf16_f32 v116, v88, v89\n"
        "v_exp_f32 v92, v92\n"
        "v_exp_f32 v93, v93\n"
        "v_cvt_pk_bf16_f32 v117, v90, v91\n"
        "v_exp_f32 v94, v94\n"
        "v_mul_f32 v212, v212, v214\n"
        "v_mul_f32 v213, v213, v214\n"
        "v_exp_f32 v95, v95\n"
        "v_cvt_pk_bf16_f32 v118, v92, v93\n"
        "v_exp_f32 v96, v96\n"
        "v_exp_f32 v97, v97\n"
        "v_cvt_pk_bf16_f32 v119, v94, v95\n"
        "v_exp_f32 v98, v98\n"
        "v_exp_f32 v99, v99\n"
        "v_mfma_f32_32x32x16_bf16 v[128:143], a[100:103], v[116:119], v[128:143]\n"
        "v_cvt_pk_bf16_f32 v212, v212, v213\n"
        "v_cvt_pk_bf16_f32 v112, v96, v97\n"
        "v_exp_f32 v100, v100\n"
        "v_exp_f32 v101, v101\n"
        "v_cvt_pk_bf16_f32 v113, v98, v99\n"
        "v_exp_f32 v102, v102\n"
        "v_exp_f32 v103, v103\n"
        "v_cvt_pk_bf16_f32 v114, v100, v101\n"
        "v_mfma_f32_32x32x16_bf16 v[80:95], a[24:27], v[32:35], v[64:79]\n"
        "ds_write_b32 v9, v212\n"
        "v_exp_f32 v104, v104\n"
        "v_exp_f32 v105, v105\n"
        "v_cvt_pk_bf16_f32 v115, v102, v103\n"
        "v_exp_f32 v106, v106\n"
        "v_exp_f32 v107, v107\n"
        "v_mfma_f32_32x32x16_bf16 v[128:143], a[104:107], v[112:115], v[128:143]\n"
        "buffer_load_dwordx4 v1, s[16:19], s42 offen offset:1024 lds\n"
        "v_cvt_pk_bf16_f32 v116, v104, v105\n"
        "v_exp_f32 v108, v108\n"
        "v_exp_f32 v109, v109\n"
        "v_cvt_pk_bf16_f32 v117, v106, v107\n"
        "v_exp_f32 v110, v110\n"
        "v_exp_f32 v111, v111\n"
        "v_cvt_pk_bf16_f32 v118, v108, v109\n"
        "v_exp_f32 v80, v80\n"
        "v_exp_f32 v81, v81\n"
        "v_cvt_pk_bf16_f32 v119, v110, v111\n"
        "v_exp_f32 v82, v82\n"
        "v_exp_f32 v83, v83\n"
        "v_mfma_f32_32x32x16_bf16 v[128:143], a[108:111], v[116:119], v[128:143]\n"
        "s_waitcnt lgkmcnt(0)\n"
        "s_barrier\n"
        "s_add_u32 s36, s35, 0x8000\n"
        "v_cvt_pk_bf16_f32 v112, v80, v81\n"
        "v_exp_f32 v84, v84\n"
        "v_exp_f32 v85, v85\n"
        "v_cvt_pk_bf16_f32 v113, v82, v83\n"
        "v_exp_f32 v86, v86\n"
        "v_exp_f32 v87, v87\n"
        "v_cvt_pk_bf16_f32 v114, v84, v85\n"
        "v_mfma_f32_32x32x16_bf16 v[96:111], a[28:31], v[32:35], v[64:79]\n"
        "ds_read_b128 v[204:207], v10\n"
        "v_exp_f32 v88, v88\n"
        "v_exp_f32 v89, v89\n"
        "v_cvt_pk_bf16_f32 v115, v86, v87\n"
        "v_exp_f32 v90, v90\n"
        "v_exp_f32 v91, v91\n"
        "v_mfma_f32_32x32x16_bf16 v[128:143], a[112:115], v[112:115], v[128:143]\n"
        "ds_read_b128 v[208:211], v10 offset:512\n"
        "v_cvt_pk_bf16_f32 v116, v88, v89\n"
        "v_exp_f32 v92, v92\n"
        "v_exp_f32 v93, v93\n"
        "v_cvt_pk_bf16_f32 v117, v90, v91\n"
        "v_exp_f32 v94, v94\n"
        "v_exp_f32 v95, v95\n"
        "v_cvt_pk_bf16_f32 v118, v92, v93\n"
        "v_exp_f32 v96, v96\n"
        "v_exp_f32 v97, v97\n"
        "v_cvt_pk_bf16_f32 v119, v94, v95\n"
        "v_exp_f32 v98, v98\n"
        "v_exp_f32 v99, v99\n"
        "v_mfma_f32_32x32x16_bf16 v[128:143], a[116:119], v[116:119], v[128:143]\n"
        "s_waitcnt vmcnt(9) lgkmcnt(0)\n"
        "v_cvt_pk_bf16_f32 v112, v96, v97\n"
        "v_exp_f32 v100, v100\n"
        "v_exp_f32 v101, v101\n"
        "v_cvt_pk_bf16_f32 v113, v98, v99\n"
        "v_exp_f32 v102, v102\n"
        "v_mfma_f32_16x16x32_bf16 v[160:163], a[192:195], v[204:207], v[160:163]\n"
        "v_exp_f32 v103, v103\n"
        "v_cvt_pk_bf16_f32 v114, v100, v101\n"
        "v_mfma_f32_32x32x16_bf16 v[80:95], a[32:35], v[32:35], v[64:79]\n"
        "v_exp_f32 v104, v104\n"
        "v_exp_f32 v105, v105\n"
        "v_cvt_pk_bf16_f32 v115, v102, v103\n"
        "v_exp_f32 v106, v106\n"
        "v_mfma_f32_16x16x32_bf16 v[164:167], a[196:199], v[204:207], v[164:167]\n"
        "v_exp_f32 v107, v107\n"
        "v_mfma_f32_32x32x16_bf16 v[128:143], a[120:123], v[112:115], v[128:143]\n"
        "v_cvt_pk_bf16_f32 v116, v104, v105\n"
        "v_exp_f32 v108, v108\n"
        "v_exp_f32 v109, v109\n"
        "v_cvt_pk_bf16_f32 v117, v106, v107\n"
        "v_exp_f32 v110, v110\n"
        "v_mfma_f32_16x16x32_bf16 v[168:171], a[200:203], v[204:207], v[168:171]\n"
        "v_exp_f32 v111, v111\n"
        "v_cvt_pk_bf16_f32 v118, v108, v109\n"
        "v_exp_f32 v80, v80\n"
        "v_exp_f32 v81, v81\n"
        "v_cvt_pk_bf16_f32 v119, v110, v111\n"
        "v_exp_f32 v82, v82\n"
        "v_exp_f32 v83, v83\n"
        "v_mfma_f32_32x32x16_bf16 v[128:143], a[124:127], v[116:119], v[128:143]\n"
        "buffer_load_dwordx4 a[192:195], v1, s[16:19], s36 offen offset:0\n"
        "v_cvt_pk_bf16_f32 v112, v80, v81\n"
        "v_exp_f32 v84, v84\n"
        "v_exp_f32 v85, v85\n"
        "v_cvt_pk_bf16_f32 v113, v82, v83\n"
        "v_exp_f32 v86, v86\n"
        "v_mfma_f32_16x16x32_bf16 v[172:175], a[204:207], v[204:207], v[172:175]\n"
        "v_exp_f32 v87, v87\n"
        "v_cvt_pk_bf16_f32 v114, v84, v85\n"
        "v_mfma_f32_32x32x16_bf16 v[96:111], a[36:39], v[32:35], v[64:79]\n"
        "buffer_load_dwordx4 a[196:199], v1, s[16:19], s36 offen offset:1024\n"
        "v_exp_f32 v88, v88\n"
        "v_exp_f32 v89, v89\n"
        "v_cvt_pk_bf16_f32 v115, v86, v87\n"
        "v_exp_f32 v90, v90\n"
        "v_mfma_f32_16x16x32_bf16 v[176:179], a[208:211], v[204:207], v[176:179]\n"
        "v_exp_f32 v91, v91\n"
        "v_mfma_f32_32x32x16_bf16 v[128:143], a[128:131], v[112:115], v[128:143]\n"
        "buffer_load_dwordx4 a[200:203], v1, s[16:19], s36 offen offset:2048\n"
        "v_cvt_pk_bf16_f32 v116, v88, v89\n"
        "v_exp_f32 v92, v92\n"
        "v_exp_f32 v93, v93\n"
        "v_cvt_pk_bf16_f32 v117, v90, v91\n"
        "v_exp_f32 v94, v94\n"
        "v_mfma_f32_16x16x32_bf16 v[180:183], a[212:215], v[204:207], v[180:183]\n"
        "v_exp_f32 v95, v95\n"
        "v_cvt_pk_bf16_f32 v118, v92, v93\n"
        "v_exp_f32 v96, v96\n"
        "v_exp_f32 v97, v97\n"
        "v_cvt_pk_bf16_f32 v119, v94, v95\n"
        "v_exp_f32 v98, v98\n"
        "v_exp_f32 v99, v99\n"
        "v_mfma_f32_32x32x16_bf16 v[128:143], a[132:135], v[116:119], v[128:143]\n"
        "buffer_load_dwordx4 a[204:207], v1, s[16:19], s36 offen offset:3072\n"
        "v_cvt_pk_bf16_f32 v112, v96, v97\n"
        "v_exp_f32 v100, v100\n"
        "v_exp_f32 v101, v101\n"
        "v_cvt_pk_bf16_f32 v113, v98, v99\n"
        "v_exp_f32 v102, v102\n"
        "v_mfma_f32_16x16x32_bf16 v[184:187], a[216:219], v[204:207], v[184:187]\n"
        "v_exp_f32 v103, v103\n"
        "v_cvt_pk_bf16_f32 v114, v100, v101\n"
        "v_mfma_f32_32x32x16_bf16 v[80:95], a[40:43], v[32:35], v[64:79]\n"
        "buffer_load_dwordx4 a[208:211], v24, s[16:19], s36 offen offset:0\n"
        "v_exp_f32 v104, v104\n"
        "v_exp_f32 v105, v105\n"
        "v_cvt_pk_bf16_f32 v115, v102, v103\n"
        "v_exp_f32 v106, v106\n"
        "v_mfma_f32_16x16x32_bf16 v[188:191], a[220:223], v[204:207], v[188:191]\n"
        "v_exp_f32 v107, v107\n"
        "v_mfma_f32_32x32x16_bf16 v[128:143], a[136:139], v[112:115], v[128:143]\n"
        "buffer_load_dwordx4 a[212:215], v24, s[16:19], s36 offen offset:1024\n"
        "v_cvt_pk_bf16_f32 v116, v104, v105\n"
        "v_exp_f32 v108, v108\n"
        "v_exp_f32 v109, v109\n"
        "v_cvt_pk_bf16_f32 v117, v106, v107\n"
        "v_exp_f32 v110, v110\n"
        "v_mfma_f32_16x16x32_bf16 v[160:163], a[224:227], v[208:211], v[160:163]\n"
        "v_exp_f32 v111, v111\n"
        "v_cvt_pk_bf16_f32 v118, v108, v109\n"
        "v_exp_f32 v80, v80\n"
        "v_exp_f32 v81, v81\n"
        "v_cvt_pk_bf16_f32 v119, v110, v111\n"
        "v_exp_f32 v82, v82\n"
        "v_exp_f32 v83, v83\n"
        "v_mfma_f32_32x32x16_bf16 v[128:143], a[140:143], v[116:119], v[128:143]\n"
        "buffer_load_dwordx4 a[216:219], v24, s[16:19], s36 offen offset:2048\n"
        "v_cvt_pk_bf16_f32 v112, v80, v81\n"
        "v_exp_f32 v84, v84\n"
        "v_exp_f32 v85, v85\n"
        "v_cvt_pk_bf16_f32 v113, v82, v83\n"
        "v_exp_f32 v86, v86\n"
        "v_mfma_f32_16x16x32_bf16 v[164:167], a[228:231], v[208:211], v[164:167]\n"
        "v_exp_f32 v87, v87\n"
        "v_cvt_pk_bf16_f32 v114, v84, v85\n"
        "v_mfma_f32_32x32x16_bf16 v[96:111], a[44:47], v[32:35], v[64:79]\n"
        "buffer_load_dwordx4 a[220:223], v24, s[16:19], s36 offen offset:3072\n"
        "v_exp_f32 v88, v88\n"
        "v_exp_f32 v89, v89\n"
        "v_cvt_pk_bf16_f32 v115, v86, v87\n"
        "v_exp_f32 v90, v90\n"
        "v_mfma_f32_16x16x32_bf16 v[168:171], a[232:235], v[208:211], v[168:171]\n"
        "v_exp_f32 v91, v91\n"
        "v_mfma_f32_32x32x16_bf16 v[128:143], a[144:147], v[112:115], v[128:143]\n"
        "buffer_load_dwordx4 a[224:227], v25, s[16:19], s36 offen offset:0\n"
        "v_cvt_pk_bf16_f32 v116, v88, v89\n"
        "v_exp_f32 v92, v92\n"
        "v_exp_f32 v93, v93\n"
        "v_cvt_pk_bf16_f32 v117, v90, v91\n"
        "v_exp_f32 v94, v94\n"
        "v_mfma_f32_16x16x32_bf16 v[172:175], a[236:239], v[208:211], v[172:175]\n"
        "v_exp_f32 v95, v95\n"
        "v_cvt_pk_bf16_f32 v118, v92, v93\n"
        "v_exp_f32 v96, v96\n"
        "v_exp_f32 v97, v97\n"
        "v_cvt_pk_bf16_f32 v119, v94, v95\n"
        "v_exp_f32 v98, v98\n"
        "v_exp_f32 v99, v99\n"
        "v_mfma_f32_32x32x16_bf16 v[128:143], a[148:151], v[116:119], v[128:143]\n"
        "buffer_load_dwordx4 a[228:231], v25, s[16:19], s36 offen offset:1024\n"
        "v_cvt_pk_bf16_f32 v112, v96, v97\n"
        "v_exp_f32 v100, v100\n"
        "v_exp_f32 v101, v101\n"
        "v_cvt_pk_bf16_f32 v113, v98, v99\n"
        "v_exp_f32 v102, v102\n"
        "v_mfma_f32_16x16x32_bf16 v[176:179], a[240:243], v[208:211], v[176:179]\n"
        "v_exp_f32 v103, v103\n"
        "v_cvt_pk_bf16_f32 v114, v100, v101\n"
        "v_mfma_f32_32x32x16_bf16 v[80:95], a[48:51], v[32:35], v[64:79]\n"
        "buffer_load_dwordx4 a[232:235], v25, s[16:19], s36 offen offset:2048\n"
        "v_exp_f32 v104, v104\n"
        "v_exp_f32 v105, v105\n"
        "v_cvt_pk_bf16_f32 v115, v102, v103\n"
        "v_exp_f32 v106, v106\n"
        "v_mfma_f32_16x16x32_bf16 v[180:183], a[244:247], v[208:211], v[180:183]\n"
        "v_exp_f32 v107, v107\n"
        "v_mfma_f32_32x32x16_bf16 v[128:143], a[152:155], v[112:115], v[128:143]\n"
        "buffer_load_dwordx4 a[236:239], v25, s[16:19], s36 offen offset:3072\n"
        "v_cvt_pk_bf16_f32 v116, v104, v105\n"
        "v_exp_f32 v108, v108\n"
        "v_exp_f32 v109, v109\n"
        "v_cvt_pk_bf16_f32 v117, v106, v107\n"
        "v_exp_f32 v110, v110\n"
        "v_mfma_f32_16x16x32_bf16 v[184:187], a[248:251], v[208:211], v[184:187]\n"
        "v_exp_f32 v111, v111\n"
        "v_cvt_pk_bf16_f32 v118, v108, v109\n"
        "v_exp_f32 v80, v80\n"
        "v_exp_f32 v81, v81\n"
        "v_cvt_pk_bf16_f32 v119, v110, v111\n"
        "v_exp_f32 v82, v82\n"
        "v_exp_f32 v83, v83\n"
        "v_mfma_f32_32x32x16_bf16 v[128:143], a[156:159], v[116:119], v[128:143]\n"
        "buffer_load_dwordx4 a[240:243], v26, s[16:19], s36 offen offset:0\n"
        "v_cvt_pk_bf16_f32 v112, v80, v81\n"
        "v_exp_f32 v84, v84\n"
        "v_exp_f32 v85, v85\n"
        "v_cvt_pk_bf16_f32 v113, v82, v83\n"
        "v_exp_f32 v86, v86\n"
        "v_mfma_f32_16x16x32_bf16 v[188:191], a[252:255], v[208:211], v[188:191]\n"
        "v_exp_f32 v87, v87\n"
        "v_cvt_pk_bf16_f32 v114, v84, v85\n"
        "v_mfma_f32_32x32x16_bf16 v[96:111], a[52:55], v[32:35], v[64:79]\n"
        "buffer_load_dwordx4 a[244:247], v26, s[16:19], s36 offen offset:1024\n"
        "v_exp_f32 v88, v88\n"
        "v_exp_f32 v89, v89\n"
        "v_cvt_pk_bf16_f32 v115, v86, v87\n"
        "v_exp_f32 v90, v90\n"
        "v_exp_f32 v91, v91\n"
        "v_mfma_f32_32x32x16_bf16 v[128:143], a[160:163], v[112:115], v[128:143]\n"
        "buffer_load_dwordx4 a[248:251], v26, s[16:19], s36 offen offset:2048\n"
        "v_cvt_pk_bf16_f32 v116, v88, v89\n"
        "v_exp_f32 v92, v92\n"
        "v_exp_f32 v93, v93\n"
        "v_cvt_pk_bf16_f32 v117, v90, v91\n"
        "v_exp_f32 v94, v94\n"
        "v_exp_f32 v95, v95\n"
        "v_cvt_pk_bf16_f32 v118, v92, v93\n"
        "v_exp_f32 v96, v96\n"
        "v_exp_f32 v97, v97\n"
        "v_cvt_pk_bf16_f32 v119, v94, v95\n"
        "v_exp_f32 v98, v98\n"
        "v_exp_f32 v99, v99\n"
        "v_mfma_f32_32x32x16_bf16 v[128:143], a[164:167], v[116:119], v[128:143]\n"
        "buffer_load_dwordx4 a[252:255], v26, s[16:19], s36 offen offset:3072\n"
        "v_cvt_pk_bf16_f32 v112, v96, v97\n"
        "v_exp_f32 v100, v100\n"
        "v_exp_f32 v101, v101\n"
        "v_cvt_pk_bf16_f32 v113, v98, v99\n"
        "v_exp_f32 v102, v102\n"
        "v_exp_f32 v103, v103\n"
        "v_cvt_pk_bf16_f32 v114, v100, v101\n"
        "v_mfma_f32_32x32x16_bf16 v[80:95], a[56:59], v[32:35], v[64:79]\n"
        "buffer_store_dwordx4 v[16:19], v24, s[16:19], s56 offen offset:1024 sc1\n"
        "v_exp_f32 v104, v104\n"
        "v_exp_f32 v105, v105\n"
        "v_cvt_pk_bf16_f32 v115, v102, v103\n"
        "v_exp_f32 v106, v106\n"
        "v_exp_f32 v107, v107\n"
        "v_mfma_f32_32x32x16_bf16 v[128:143], a[168:171], v[112:115], v[128:143]\n"
        "v_cvt_pk_bf16_f32 v116, v104, v105\n"
        "v_exp_f32 v108, v108\n"
        "v_exp_f32 v109, v109\n"
        "v_cvt_pk_bf16_f32 v117, v106, v107\n"
        "v_exp_f32 v110, v110\n"
        "v_exp_f32 v111, v111\n"
        "v_cvt_pk_bf16_f32 v118, v108, v109\n"
        "v_exp_f32 v80, v80\n"
        "v_exp_f32 v81, v81\n"
        "v_cvt_pk_bf16_f32 v119, v110, v111\n"
        "v_exp_f32 v82, v82\n"
        "v_exp_f32 v83, v83\n"
        "v_mfma_f32_32x32x16_bf16 v[128:143], a[172:175], v[116:119], v[128:143]\n"
        "buffer_load_dwordx4 v1, s[16:19], s42 offen offset:2048 lds\n"
        "v_cvt_pk_bf16_f32 v112, v80, v81\n"
        "v_exp_f32 v84, v84\n"
        "v_exp_f32 v85, v85\n"
        "v_cvt_pk_bf16_f32 v113, v82, v83\n"
        "v_exp_f32 v86, v86\n"
        "v_exp_f32 v87, v87\n"
        "v_cvt_pk_bf16_f32 v114, v84, v85\n"
        "v_mfma_f32_32x32x16_bf16 v[96:111], a[60:63], v[32:35], v[64:79]\n"
        "buffer_store_dwordx4 v[16:19], v24, s[16:19], s56 offen offset:2048 sc1\n"
        "v_exp_f32 v88, v88\n"
        "v_exp_f32 v89, v89\n"
        "v_cvt_pk_bf16_f32 v115, v86, v87\n"
        "v_exp_f32 v90, v90\n"
        "v_exp_f32 v91, v91\n"
        "v_mfma_f32_32x32x16_bf16 v[128:143], a[176:179], v[112:115], v[128:143]\n"
        "v_cvt_pk_bf16_f32 v116, v88, v89\n"
        "v_exp_f32 v92, v92\n"
        "v_exp_f32 v93, v93\n"
        "v_cvt_pk_bf16_f32 v117, v90, v91\n"
        "v_exp_f32 v94, v94\n"
        "v_exp_f32 v95, v95\n"
        "v_cvt_pk_bf16_f32 v118, v92, v93\n"
        "v_exp_f32 v96, v96\n"
        "v_exp_f32 v97, v97\n"
        "v_cvt_pk_bf16_f32 v119, v94, v95\n"
        "v_exp_f32 v98, v98\n"
        "v_exp_f32 v99, v99\n"
        "v_mfma_f32_32x32x16_bf16 v[128:143], a[180:183], v[116:119], v[128:143]\n"
        "buffer_load_dwordx4 v1, s[16:19], s42 offen offset:3072 lds\n"
        "v_cvt_pk_bf16_f32 v112, v96, v97\n"
        "v_exp_f32 v100, v100\n"
        "v_exp_f32 v101, v101\n"
        "v_cvt_pk_bf16_f32 v113, v98, v99\n"
        "v_exp_f32 v102, v102\n"
        "v_exp_f32 v103, v103\n"
        "v_cvt_pk_bf16_f32 v114, v100, v101\n"
        "s_waitcnt vmcnt(22)\n"
        "v_mfma_f32_32x32x16_bf16 v[80:95], a[0:3], v[36:39], v[64:79]\n"
        "s_cmp_eq_u32 s31, 0\n"
        "s_cselect_b32 s22, s18, 0\n"
        "s_mov_b64 s[20:21], s[16:17]\n"
        "s_lshl_b32 s38, s51, 15\n"
        "s_lshl_b32 s39, s40, 10\n"
        "s_add_u32 s38, s38, s39\n"
        "s_add_u32 s38, s38, 0x1112000\n"
        "buffer_store_dwordx4 v[16:19], v1, s[20:23], s38 offen sc1\n"
        "v_exp_f32 v104, v104\n"
        "v_exp_f32 v105, v105\n"
        "v_cvt_pk_bf16_f32 v115, v102, v103\n"
        "v_exp_f32 v106, v106\n"
        "v_exp_f32 v107, v107\n"
        "v_mfma_f32_32x32x16_bf16 v[128:143], a[184:187], v[112:115], v[128:143]\n"
        "v_cvt_pk_bf16_f32 v116, v104, v105\n"
        "v_exp_f32 v108, v108\n"
        "v_exp_f32 v109, v109\n"
        "v_cvt_pk_bf16_f32 v117, v106, v107\n"
        "v_exp_f32 v110, v110\n"
        "v_exp_f32 v111, v111\n"
        "v_cvt_pk_bf16_f32 v118, v108, v109\n"
        "v_exp_f32 v80, v80\n"
        "v_exp_f32 v81, v81\n"
        "v_cvt_pk_bf16_f32 v119, v110, v111\n"
        "v_exp_f32 v82, v82\n"
        "v_exp_f32 v83, v83\n"
        "v_mfma_f32_32x32x16_bf16 v[128:143], a[188:191], v[116:119], v[128:143]\n"
        "s_add_u32 m0, s41, 0x3000\n"
        "s_add_u32 s42, m0, s43\n"
        "buffer_load_dwordx4 v1, s[16:19], s42 offen offset:0 lds\n"
        "v_cvt_pk_bf16_f32 v112, v80, v81\n"
        "v_exp_f32 v84, v84\n"
        "v_exp_f32 v85, v85\n"
        "v_cvt_pk_bf16_f32 v113, v82, v83\n"
        "v_exp_f32 v86, v86\n"
        "v_exp_f32 v87, v87\n"
        "v_cvt_pk_bf16_f32 v114, v84, v85\n"
        "v_mfma_f32_32x32x16_bf16 v[96:111], a[4:7], v[36:39], v[64:79]\n"
        "v_exp_f32 v88, v88\n"
        "v_exp_f32 v89, v89\n"
        "v_cvt_pk_bf16_f32 v115, v86, v87\n"
        "v_exp_f32 v90, v90\n"
        "v_exp_f32 v91, v91\n"
        "v_mfma_f32_32x32x16_bf16 v[144:159], a[64:67], v[112:115], 0\n"
        "buffer_load_dwordx4 v1, s[16:19], s42 offen offset:1024 lds\n"
        "v_cvt_pk_bf16_f32 v116, v88, v89\n"
        "v_exp_f32 v92, v92\n"
        "v_exp_f32 v93, v93\n"
        "v_cvt_pk_bf16_f32 v117, v90, v91\n"
        "v_exp_f32 v94, v94\n"
        "v_exp_f32 v95, v95\n"
        "v_cvt_pk_bf16_f32 v118, v92, v93\n"
        "v_exp_f32 v96, v96\n"
        "v_exp_f32 v97, v97\n"
        "v_cvt_pk_bf16_f32 v119, v94, v95\n"
        "v_exp_f32 v98, v98\n"
        "v_exp_f32 v99, v99\n"
        "v_mfma_f32_32x32x16_bf16 v[144:159], a[68:71], v[116:119], v[144:159]\n"
        "ds_write_b96 v5, v[132:134]\n"
        "v_cvt_pk_bf16_f32 v112, v96, v97\n"
        "v_exp_f32 v100, v100\n"
        "v_exp_f32 v101, v101\n"
        "v_cvt_pk_bf16_f32 v113, v98, v99\n"
        "v_exp_f32 v102, v102\n"
        "v_exp_f32 v103, v103\n"
        "v_cvt_pk_bf16_f32 v114, v100, v101\n"
        "v_mfma_f32_32x32x16_bf16 v[80:95], a[8:11], v[36:39], v[64:79]\n"
        "ds_write_b96 v6, v[136:138]\n"
        "v_exp_f32 v104, v104\n"
        "v_exp_f32 v105, v105\n"
        "v_cvt_pk_bf16_f32 v115, v102, v103\n"
        "v_exp_f32 v106, v106\n"
        "v_exp_f32 v107, v107\n"
        "v_mfma_f32_32x32x16_bf16 v[144:159], a[72:75], v[112:115], v[144:159]\n"
        "ds_write_b96 v7, v[140:142]\n"
        "v_cvt_pk_bf16_f32 v116, v104, v105\n"
        "v_exp_f32 v108, v108\n"
        "v_exp_f32 v109, v109\n"
        "v_cvt_pk_bf16_f32 v117, v106, v107\n"
        "v_exp_f32 v110, v110\n"
        "v_exp_f32 v111, v111\n"
        "v_cvt_pk_bf16_f32 v118, v108, v109\n"
        "v_exp_f32 v80, v80\n"
        "v_exp_f32 v81, v81\n"
        "v_cvt_pk_bf16_f32 v119, v110, v111\n"
        "v_exp_f32 v82, v82\n"
        "v_exp_f32 v83, v83\n"
        "v_mfma_f32_32x32x16_bf16 v[144:159], a[76:79], v[116:119], v[144:159]\n"
        "s_waitcnt lgkmcnt(0)\n"
        "s_barrier\n"
        "v_cvt_pk_bf16_f32 v112, v80, v81\n"
        "v_exp_f32 v84, v84\n"
        "v_exp_f32 v85, v85\n"
        "v_cvt_pk_bf16_f32 v113, v82, v83\n"
        "v_exp_f32 v86, v86\n"
        "v_exp_f32 v87, v87\n"
        "v_cvt_pk_bf16_f32 v114, v84, v85\n"
        "v_mfma_f32_32x32x16_bf16 v[96:111], a[12:15], v[36:39], v[64:79]\n"
        "ds_read_b128 v[192:195], v8\n"
        "v_exp_f32 v88, v88\n"
        "v_exp_f32 v89, v89\n"
        "v_cvt_pk_bf16_f32 v115, v86, v87\n"
        "v_exp_f32 v90, v90\n"
        "v_exp_f32 v91, v91\n"
        "v_mfma_f32_32x32x16_bf16 v[144:159], a[80:83], v[112:115], v[144:159]\n"
        "ds_read_b128 v[196:199], v8 offset:1024\n"
        "v_cvt_pk_bf16_f32 v116, v88, v89\n"
        "v_exp_f32 v92, v92\n"
        "v_exp_f32 v93, v93\n"
        "v_cvt_pk_bf16_f32 v117, v90, v91\n"
        "v_exp_f32 v94, v94\n"
        "v_exp_f32 v95, v95\n"
        "v_cvt_pk_bf16_f32 v118, v92, v93\n"
        "v_exp_f32 v96, v96\n"
        "v_exp_f32 v97, v97\n"
        "v_cvt_pk_bf16_f32 v119, v94, v95\n"
        "v_exp_f32 v98, v98\n"
        "v_exp_f32 v99, v99\n"
        "v_mfma_f32_32x32x16_bf16 v[144:159], a[84:87], v[116:119], v[144:159]\n"
        "ds_read_b128 v[200:203], v8 offset:2048\n"
        "v_cvt_pk_bf16_f32 v112, v96, v97\n"
        "v_exp_f32 v100, v100\n"
        "v_exp_f32 v101, v101\n"
        "v_cvt_pk_bf16_f32 v113, v98, v99\n"
        "v_exp_f32 v102, v102\n"
        "v_exp_f32 v103, v103\n"
        "v_cvt_pk_bf16_f32 v114, v100, v101\n"
        "v_mfma_f32_32x32x16_bf16 v[80:95], a[16:19], v[36:39], v[64:79]\n"
        "buffer_load_dwordx4 v[32:35], v1, s[16:19], s58 offen offset:0\n"
        "v_exp_f32 v104, v104\n"
        "v_exp_f32 v105, v105\n"
        "v_cvt_pk_bf16_f32 v115, v102, v103\n"
        "v_exp_f32 v106, v106\n"
        "v_exp_f32 v107, v107\n"
        "v_mfma_f32_32x32x16_bf16 v[144:159], a[88:91], v[112:115], v[144:159]\n"
        "s_waitcnt lgkmcnt(0)\n"
        "v_cvt_pk_bf16_f32 v116, v104, v105\n"
        "v_exp_f32 v108, v108\n"
        "v_exp_f32 v109, v109\n"
        "v_cvt_pk_bf16_f32 v117, v106, v107\n"
        "v_exp_f32 v110, v110\n"
        "v_add_f32 v212, v128, v192\n"
        "v_add_f32 v213, v129, v193\n"
        "v_add_f32 v214, v130, v194\n"
        "v_exp_f32 v111, v111\n"
        "v_cvt_pk_bf16_f32 v118, v108, v109\n"
        "v_exp_f32 v80, v80\n"
        "v_exp_f32 v81, v81\n"
        "v_cvt_pk_bf16_f32 v119, v110, v111\n"
        "v_exp_f32 v82, v82\n"
        "v_exp_f32 v83, v83\n"
        "v_mfma_f32_32x32x16_bf16 v[144:159], a[92:95], v[116:119], v[144:159]\n"
        "buffer_load_dwordx4 v1, s[16:19], s42 offen offset:2048 lds\n"
        "v_cvt_pk_bf16_f32 v112, v80, v81\n"
        "v_exp_f32 v84, v84\n"
        "v_exp_f32 v85, v85\n"
        "v_cvt_pk_bf16_f32 v113, v82, v83\n"
        "v_exp_f32 v86, v86\n"
        "v_add_f32 v196, v196, v200\n"
        "v_add_f32 v197, v197, v201\n"
        "v_add_f32 v198, v198, v202\n"
        "v_exp_f32 v87, v87\n"
        "v_cvt_pk_bf16_f32 v114, v84, v85\n"
        "v_mfma_f32_32x32x16_bf16 v[96:111], a[20:23], v[36:39], v[64:79]\n"
        "v_add_f32 v212, v212, v196\n"
        "v_add_f32 v213, v213, v197\n"
        "v_add_f32 v214, v214, v198\n"
        "v_exp_f32 v88, v88\n"
        "v_exp_f32 v89, v89\n"
        "v_cvt_pk_bf16_f32 v115, v86, v87\n"
        "v_exp_f32 v90, v90\n"
        "v_exp_f32 v91, v91\n"
        "v_mfma_f32_32x32x16_bf16 v[144:159], a[96:99], v[112:115], v[144:159]\n"
        "v_rcp_f32 v214, v214\n"
        "v_cvt_pk_bf16_f32 v116, v88, v89\n"
        "v_exp_f32 v92, v92\n"
        "v_exp_f32 v93, v93\n"
        "v_cvt_pk_bf16_f32 v117, v90, v91\n"
        "v_exp_f32 v94, v94\n"
        "v_mul_f32 v212, v212, v214\n"
        "v_mul_f32 v213, v213, v214\n"
        "v_exp_f32 v95, v95\n"
        "v_cvt_pk_bf16_f32 v118, v92, v93\n"
        "v_exp_f32 v96, v96\n"
        "v_exp_f32 v97, v97\n"
        "v_cvt_pk_bf16_f32 v119, v94, v95\n"
        "v_exp_f32 v98, v98\n"
        "v_exp_f32 v99, v99\n"
        "v_mfma_f32_32x32x16_bf16 v[144:159], a[100:103], v[116:119], v[144:159]\n"
        "v_cvt_pk_bf16_f32 v212, v212, v213\n"
        "v_cvt_pk_bf16_f32 v112, v96, v97\n"
        "v_exp_f32 v100, v100\n"
        "v_exp_f32 v101, v101\n"
        "v_cvt_pk_bf16_f32 v113, v98, v99\n"
        "v_exp_f32 v102, v102\n"
        "v_exp_f32 v103, v103\n"
        "v_cvt_pk_bf16_f32 v114, v100, v101\n"
        "v_mfma_f32_32x32x16_bf16 v[80:95], a[24:27], v[36:39], v[64:79]\n"
        "ds_write_b32 v9, v212\n"
        "v_exp_f32 v104, v104\n"
        "v_exp_f32 v105, v105\n"
        "v_cvt_pk_bf16_f32 v115, v102, v103\n"
        "v_exp_f32 v106, v106\n"
        "v_exp_f32 v107, v107\n"
        "v_mfma_f32_32x32x16_bf16 v[144:159], a[104:107], v[112:115], v[144:159]\n"
        "buffer_load_dwordx4 v1, s[16:19], s42 offen offset:3072 lds\n"
        "v_cvt_pk_bf16_f32 v116, v104, v105\n"
        "v_exp_f32 v108, v108\n"
        "v_exp_f32 v109, v109\n"
        "v_cvt_pk_bf16_f32 v117, v106, v107\n"
        "v_exp_f32 v110, v110\n"
        "v_exp_f32 v111, v111\n"
        "v_cvt_pk_bf16_f32 v118, v108, v109\n"
        "v_exp_f32 v80, v80\n"
        "v_exp_f32 v81, v81\n"
        "v_cvt_pk_bf16_f32 v119, v110, v111\n"
        "v_exp_f32 v82, v82\n"
        "v_exp_f32 v83, v83\n"
        "v_mfma_f32_32x32x16_bf16 v[144:159], a[108:111], v[116:119], v[144:159]\n"
        "s_waitcnt lgkmcnt(0)\n"
        "s_barrier\n"
        "s_add_u32 s36, s35, 0xc000\n"
        "v_cvt_pk_bf16_f32 v112, v80, v81\n"
        "v_exp_f32 v84, v84\n"
        "v_exp_f32 v85, v85\n"
        "v_cvt_pk_bf16_f32 v113, v82, v83\n"
        "v_exp_f32 v86, v86\n"
        "v_exp_f32 v87, v87\n"
        "v_cvt_pk_bf16_f32 v114, v84, v85\n"
        "v_mfma_f32_32x32x16_bf16 v[96:111], a[28:31], v[36:39], v[64:79]\n"
        "ds_read_b128 v[204:207], v10\n"
        "v_exp_f32 v88, v88\n"
        "v_exp_f32 v89, v89\n"
        "v_cvt_pk_bf16_f32 v115, v86, v87\n"
        "v_exp_f32 v90, v90\n"
        "v_exp_f32 v91, v91\n"
        "v_mfma_f32_32x32x16_bf16 v[144:159], a[112:115], v[112:115], v[144:159]\n"
        "ds_read_b128 v[208:211], v10 offset:512\n"
        "v_cvt_pk_bf16_f32 v116, v88, v89\n"
        "v_exp_f32 v92, v92\n"
        "v_exp_f32 v93, v93\n"
        "v_cvt_pk_bf16_f32 v117, v90, v91\n"
        "v_exp_f32 v94, v94\n"
        "v_exp_f32 v95, v95\n"
        "v_cvt_pk_bf16_f32 v118, v92, v93\n"
        "v_exp_f32 v96, v96\n"
        "v_exp_f32 v97, v97\n"
        "v_cvt_pk_bf16_f32 v119, v94, v95\n"
        "v_exp_f32 v98, v98\n"
        "v_exp_f32 v99, v99\n"
        "v_mfma_f32_32x32x16_bf16 v[144:159], a[116:119], v[116:119], v[144:159]\n"
        "s_waitcnt vmcnt(10) lgkmcnt(0)\n"
        "v_cvt_pk_bf16_f32 v112, v96, v97\n"
        "v_exp_f32 v100, v100\n"
        "v_exp_f32 v101, v101\n"
        "v_cvt_pk_bf16_f32 v113, v98, v99\n"
        "v_exp_f32 v102, v102\n"
        "v_mfma_f32_16x16x32_bf16 v[160:163], a[192:195], v[204:207], v[160:163]\n"
        "v_exp_f32 v103, v103\n"
        "v_cvt_pk_bf16_f32 v114, v100, v101\n"
        "v_mfma_f32_32x32x16_bf16 v[80:95], a[32:35], v[36:39], v[64:79]\n"
        "v_exp_f32 v104, v104\n"
        "v_exp_f32 v105, v105\n"
        "v_cvt_pk_bf16_f32 v115, v102, v103\n"
        "v_exp_f32 v106, v106\n"
        "v_mfma_f32_16x16x32_bf16 v[164:167], a[196:199], v[204:207], v[164:167]\n"
        "v_exp_f32 v107, v107\n"
        "v_mfma_f32_32x32x16_bf16 v[144:159], a[120:123], v[112:115], v[144:159]\n"
        "v_cvt_pk_bf16_f32 v116, v104, v105\n"
        "v_exp_f32 v108, v108\n"
        "v_exp_f32 v109, v109\n"
        "v_cvt_pk_bf16_f32 v117, v106, v107\n"
        "v_exp_f32 v110, v110\n"
        "v_mfma_f32_16x16x32_bf16 v[168:171], a[200:203], v[204:207], v[168:171]\n"
        "v_exp_f32 v111, v111\n"
        "v_cvt_pk_bf16_f32 v118, v108, v109\n"
        "v_exp_f32 v80, v80\n"
        "v_exp_f32 v81, v81\n"
        "v_cvt_pk_bf16_f32 v119, v110, v111\n"
        "v_exp_f32 v82, v82\n"
        "v_exp_f32 v83, v83\n"
        "v_mfma_f32_32x32x16_bf16 v[144:159], a[124:127], v[116:119], v[144:159]\n"
        "buffer_load_dwordx4 a[192:195], v1, s[16:19], s36 offen offset:0\n"
        "v_cvt_pk_bf16_f32 v112, v80, v81\n"
        "v_exp_f32 v84, v84\n"
        "v_exp_f32 v85, v85\n"
        "v_cvt_pk_bf16_f32 v113, v82, v83\n"
        "v_exp_f32 v86, v86\n"
        "v_mfma_f32_16x16x32_bf16 v[172:175], a[204:207], v[204:207], v[172:175]\n"
        "v_exp_f32 v87, v87\n"
        "v_cvt_pk_bf16_f32 v114, v84, v85\n"
        "v_mfma_f32_32x32x16_bf16 v[96:111], a[36:39], v[36:39], v[64:79]\n"
        "buffer_load_dwordx4 a[196:199], v1, s[16:19], s36 offen offset:1024\n"
        "v_exp_f32 v88, v88\n"
        "v_exp_f32 v89, v89\n"
        "v_cvt_pk_bf16_f32 v115, v86, v87\n"
        "v_exp_f32 v90, v90\n"
        "v_mfma_f32_16x16x32_bf16 v[176:179], a[208:211], v[204:207], v[176:179]\n"
        "v_exp_f32 v91, v91\n"
        "v_mfma_f32_32x32x16_bf16 v[144:159], a[128:131], v[112:115], v[144:159]\n"
        "buffer_load_dwordx4 a[200:203], v1, s[16:19], s36 offen offset:2048\n"
        "v_cvt_pk_bf16_f32 v116, v88, v89\n"
        "v_exp_f32 v92, v92\n"
        "v_exp_f32 v93, v93\n"
        "v_cvt_pk_bf16_f32 v117, v90, v91\n"
        "v_exp_f32 v94, v94\n"
        "v_mfma_f32_16x16x32_bf16 v[180:183], a[212:215], v[204:207], v[180:183]\n"
        "v_exp_f32 v95, v95\n"
        "v_cvt_pk_bf16_f32 v118, v92, v93\n"
        "v_exp_f32 v96, v96\n"
        "v_exp_f32 v97, v97\n"
        "v_cvt_pk_bf16_f32 v119, v94, v95\n"
        "v_exp_f32 v98, v98\n"
        "v_exp_f32 v99, v99\n"
        "v_mfma_f32_32x32x16_bf16 v[144:159], a[132:135], v[116:119], v[144:159]\n"
        "buffer_load_dwordx4 a[204:207], v1, s[16:19], s36 offen offset:3072\n"
        "v_cvt_pk_bf16_f32 v112, v96, v97\n"
        "v_exp_f32 v100, v100\n"
        "v_exp_f32 v101, v101\n"
        "v_cvt_pk_bf16_f32 v113, v98, v99\n"
        "v_exp_f32 v102, v102\n"
        "v_mfma_f32_16x16x32_bf16 v[184:187], a[216:219], v[204:207], v[184:187]\n"
        "v_exp_f32 v103, v103\n"
        "v_cvt_pk_bf16_f32 v114, v100, v101\n"
        "v_mfma_f32_32x32x16_bf16 v[80:95], a[40:43], v[36:39], v[64:79]\n"
        "buffer_load_dwordx4 a[208:211], v24, s[16:19], s36 offen offset:0\n"
        "v_exp_f32 v104, v104\n"
        "v_exp_f32 v105, v105\n"
        "v_cvt_pk_bf16_f32 v115, v102, v103\n"
        "v_exp_f32 v106, v106\n"
        "v_mfma_f32_16x16x32_bf16 v[188:191], a[220:223], v[204:207], v[188:191]\n"
        "v_exp_f32 v107, v107\n"
        "v_mfma_f32_32x32x16_bf16 v[144:159], a[136:139], v[112:115], v[144:159]\n"
        "buffer_load_dwordx4 a[212:215], v24, s[16:19], s36 offen offset:1024\n"
        "v_cvt_pk_bf16_f32 v116, v104, v105\n"
        "v_exp_f32 v108, v108\n"
        "v_exp_f32 v109, v109\n"
        "v_cvt_pk_bf16_f32 v117, v106, v107\n"
        "v_exp_f32 v110, v110\n"
        "v_mfma_f32_16x16x32_bf16 v[160:163], a[224:227], v[208:211], v[160:163]\n"
        "v_exp_f32 v111, v111\n"
        "v_cvt_pk_bf16_f32 v118, v108, v109\n"
        "v_exp_f32 v80, v80\n"
        "v_exp_f32 v81, v81\n"
        "v_cvt_pk_bf16_f32 v119, v110, v111\n"
        "v_exp_f32 v82, v82\n"
        "v_exp_f32 v83, v83\n"
        "v_mfma_f32_32x32x16_bf16 v[144:159], a[140:143], v[116:119], v[144:159]\n"
        "buffer_load_dwordx4 a[216:219], v24, s[16:19], s36 offen offset:2048\n"
        "v_cvt_pk_bf16_f32 v112, v80, v81\n"
        "v_exp_f32 v84, v84\n"
        "v_exp_f32 v85, v85\n"
        "v_cvt_pk_bf16_f32 v113, v82, v83\n"
        "v_exp_f32 v86, v86\n"
        "v_mfma_f32_16x16x32_bf16 v[164:167], a[228:231], v[208:211], v[164:167]\n"
        "v_exp_f32 v87, v87\n"
        "v_cvt_pk_bf16_f32 v114, v84, v85\n"
        "v_mfma_f32_32x32x16_bf16 v[96:111], a[44:47], v[36:39], v[64:79]\n"
        "buffer_load_dwordx4 a[220:223], v24, s[16:19], s36 offen offset:3072\n"
        "v_exp_f32 v88, v88\n"
        "v_exp_f32 v89, v89\n"
        "v_cvt_pk_bf16_f32 v115, v86, v87\n"
        "v_exp_f32 v90, v90\n"
        "v_mfma_f32_16x16x32_bf16 v[168:171], a[232:235], v[208:211], v[168:171]\n"
        "v_exp_f32 v91, v91\n"
        "v_mfma_f32_32x32x16_bf16 v[144:159], a[144:147], v[112:115], v[144:159]\n"
        "buffer_load_dwordx4 a[224:227], v25, s[16:19], s36 offen offset:0\n"
        "v_cvt_pk_bf16_f32 v116, v88, v89\n"
        "v_exp_f32 v92, v92\n"
        "v_exp_f32 v93, v93\n"
        "v_cvt_pk_bf16_f32 v117, v90, v91\n"
        "v_exp_f32 v94, v94\n"
        "v_mfma_f32_16x16x32_bf16 v[172:175], a[236:239], v[208:211], v[172:175]\n"
        "v_exp_f32 v95, v95\n"
        "v_cvt_pk_bf16_f32 v118, v92, v93\n"
        "v_exp_f32 v96, v96\n"
        "v_exp_f32 v97, v97\n"
        "v_cvt_pk_bf16_f32 v119, v94, v95\n"
        "v_exp_f32 v98, v98\n"
        "v_exp_f32 v99, v99\n"
        "v_mfma_f32_32x32x16_bf16 v[144:159], a[148:151], v[116:119], v[144:159]\n"
        "buffer_load_dwordx4 a[228:231], v25, s[16:19], s36 offen offset:1024\n"
        "v_cvt_pk_bf16_f32 v112, v96, v97\n"
        "v_exp_f32 v100, v100\n"
        "v_exp_f32 v101, v101\n"
        "v_cvt_pk_bf16_f32 v113, v98, v99\n"
        "v_exp_f32 v102, v102\n"
        "v_mfma_f32_16x16x32_bf16 v[176:179], a[240:243], v[208:211], v[176:179]\n"
        "v_exp_f32 v103, v103\n"
        "v_cvt_pk_bf16_f32 v114, v100, v101\n"
        "v_mfma_f32_32x32x16_bf16 v[80:95], a[48:51], v[36:39], v[64:79]\n"
        "buffer_load_dwordx4 a[232:235], v25, s[16:19], s36 offen offset:2048\n"
        "v_exp_f32 v104, v104\n"
        "v_exp_f32 v105, v105\n"
        "v_cvt_pk_bf16_f32 v115, v102, v103\n"
        "v_exp_f32 v106, v106\n"
        "v_mfma_f32_16x16x32_bf16 v[180:183], a[244:247], v[208:211], v[180:183]\n"
        "v_exp_f32 v107, v107\n"
        "v_mfma_f32_32x32x16_bf16 v[144:159], a[152:155], v[112:115], v[144:159]\n"
        "buffer_load_dwordx4 a[236:239], v25, s[16:19], s36 offen offset:3072\n"
        "v_cvt_pk_bf16_f32 v116, v104, v105\n"
        "v_exp_f32 v108, v108\n"
        "v_exp_f32 v109, v109\n"
        "v_cvt_pk_bf16_f32 v117, v106, v107\n"
        "v_exp_f32 v110, v110\n"
        "v_mfma_f32_16x16x32_bf16 v[184:187], a[248:251], v[208:211], v[184:187]\n"
        "v_exp_f32 v111, v111\n"
        "v_cvt_pk_bf16_f32 v118, v108, v109\n"
        "v_exp_f32 v80, v80\n"
        "v_exp_f32 v81, v81\n"
        "v_cvt_pk_bf16_f32 v119, v110, v111\n"
        "v_exp_f32 v82, v82\n"
        "v_exp_f32 v83, v83\n"
        "v_mfma_f32_32x32x16_bf16 v[144:159], a[156:159], v[116:119], v[144:159]\n"
        "buffer_load_dwordx4 a[240:243], v26, s[16:19], s36 offen offset:0\n"
        "v_cvt_pk_bf16_f32 v112, v80, v81\n"
        "v_exp_f32 v84, v84\n"
        "v_exp_f32 v85, v85\n"
        "v_cvt_pk_bf16_f32 v113, v82, v83\n"
        "v_exp_f32 v86, v86\n"
        "v_mfma_f32_16x16x32_bf16 v[188:191], a[252:255], v[208:211], v[188:191]\n"
        "v_exp_f32 v87, v87\n"
        "v_cvt_pk_bf16_f32 v114, v84, v85\n"
        "v_mfma_f32_32x32x16_bf16 v[96:111], a[52:55], v[36:39], v[64:79]\n"
        "buffer_load_dwordx4 a[244:247], v26, s[16:19], s36 offen offset:1024\n"
        "v_exp_f32 v88, v88\n"
        "v_exp_f32 v89, v89\n"
        "v_cvt_pk_bf16_f32 v115, v86, v87\n"
        "v_exp_f32 v90, v90\n"
        "v_exp_f32 v91, v91\n"
        "v_mfma_f32_32x32x16_bf16 v[144:159], a[160:163], v[112:115], v[144:159]\n"
        "buffer_load_dwordx4 a[248:251], v26, s[16:19], s36 offen offset:2048\n"
        "v_cvt_pk_bf16_f32 v116, v88, v89\n"
        "v_exp_f32 v92, v92\n"
        "v_exp_f32 v93, v93\n"
        "v_cvt_pk_bf16_f32 v117, v90, v91\n"
        "v_exp_f32 v94, v94\n"
        "v_exp_f32 v95, v95\n"
        "v_cvt_pk_bf16_f32 v118, v92, v93\n"
        "v_exp_f32 v96, v96\n"
        "v_exp_f32 v97, v97\n"
        "v_cvt_pk_bf16_f32 v119, v94, v95\n"
        "v_exp_f32 v98, v98\n"
        "v_exp_f32 v99, v99\n"
        "v_mfma_f32_32x32x16_bf16 v[144:159], a[164:167], v[116:119], v[144:159]\n"
        "buffer_load_dwordx4 a[252:255], v26, s[16:19], s36 offen offset:3072\n"
        "v_cvt_pk_bf16_f32 v112, v96, v97\n"
        "v_exp_f32 v100, v100\n"
        "v_exp_f32 v101, v101\n"
        "v_cvt_pk_bf16_f32 v113, v98, v99\n"
        "v_exp_f32 v102, v102\n"
        "v_exp_f32 v103, v103\n"
        "v_cvt_pk_bf16_f32 v114, v100, v101\n"
        "v_mfma_f32_32x32x16_bf16 v[80:95], a[56:59], v[36:39], v[64:79]\n"
        "s_add_u32 s38, s38, 16384\n"
        "buffer_store_dwordx4 v[16:19], v1, s[20:23], s38 offen sc1\n"
        "v_exp_f32 v104, v104\n"
        "v_exp_f32 v105, v105\n"
        "v_cvt_pk_bf16_f32 v115, v102, v103\n"
        "v_exp_f32 v106, v106\n"
        "v_exp_f32 v107, v107\n"
        "v_mfma_f32_32x32x16_bf16 v[144:159], a[168:171], v[112:115], v[144:159]\n"
        "v_cvt_pk_bf16_f32 v116, v104, v105\n"
        "v_exp_f32 v108, v108\n"
        "v_exp_f32 v109, v109\n"
        "v_cvt_pk_bf16_f32 v117, v106, v107\n"
        "v_exp_f32 v110, v110\n"
        "v_exp_f32 v111, v111\n"
        "v_cvt_pk_bf16_f32 v118, v108, v109\n"
        "v_exp_f32 v80, v80\n"
        "v_exp_f32 v81, v81\n"
        "v_cvt_pk_bf16_f32 v119, v110, v111\n"
        "v_exp_f32 v82, v82\n"
        "v_exp_f32 v83, v83\n"
        "v_mfma_f32_32x32x16_bf16 v[144:159], a[172:175], v[116:119], v[144:159]\n"
        "s_add_u32 m0, s41, 0x4000\n"
        "s_add_u32 s42, m0, s43\n"
        "buffer_load_dwordx4 v1, s[16:19], s42 offen offset:0 lds\n"
        "v_cvt_pk_bf16_f32 v112, v80, v81\n"
        "v_exp_f32 v84, v84\n"
        "v_exp_f32 v85, v85\n"
        "v_cvt_pk_bf16_f32 v113, v82, v83\n"
        "v_exp_f32 v86, v86\n"
        "v_exp_f32 v87, v87\n"
        "v_cvt_pk_bf16_f32 v114, v84, v85\n"
        "v_mfma_f32_32x32x16_bf16 v[96:111], a[60:63], v[36:39], v[64:79]\n"
        "s_lshl_b32 s38, s51, 12\n"
        "s_lshl_b32 s39, s40, 8\n"
        "s_add_u32 s38, s38, s39\n"
        "s_add_u32 s38, s38, 0x92000\n"
        "s_mov_b64 exec, 0xffff\n"
        "buffer_store_dwordx4 v[16:19], v1, s[20:23], s38 offen sc1\n"
        "s_mov_b64 exec, -1\n"
        "v_exp_f32 v88, v88\n"
        "v_exp_f32 v89, v89\n"
        "v_cvt_pk_bf16_f32 v115, v86, v87\n"
        "v_exp_f32 v90, v90\n"
        "v_exp_f32 v91, v91\n"
        "v_mfma_f32_32x32x16_bf16 v[144:159], a[176:179], v[112:115], v[144:159]\n"
        "v_cvt_pk_bf16_f32 v116, v88, v89\n"
        "v_exp_f32 v92, v92\n"
        "v_exp_f32 v93, v93\n"
        "v_cvt_pk_bf16_f32 v117, v90, v91\n"
        "v_exp_f32 v94, v94\n"
        "v_exp_f32 v95, v95\n"
        "v_cvt_pk_bf16_f32 v118, v92, v93\n"
        "v_exp_f32 v96, v96\n"
        "v_exp_f32 v97, v97\n"
        "v_cvt_pk_bf16_f32 v119, v94, v95\n"
        "v_exp_f32 v98, v98\n"
        "v_exp_f32 v99, v99\n"
        "v_mfma_f32_32x32x16_bf16 v[144:159], a[180:183], v[116:119], v[144:159]\n"
        "buffer_load_dwordx4 v1, s[16:19], s42 offen offset:1024 lds\n"
        "v_cvt_pk_bf16_f32 v112, v96, v97\n"
        "v_exp_f32 v100, v100\n"
        "v_exp_f32 v101, v101\n"
        "v_cvt_pk_bf16_f32 v113, v98, v99\n"
        "v_exp_f32 v102, v102\n"
        "v_exp_f32 v103, v103\n"
        "v_cvt_pk_bf16_f32 v114, v100, v101\n"
        "s_waitcnt vmcnt(22)\n"
        "v_mfma_f32_32x32x16_bf16 v[80:95], a[0:3], v[32:35], v[64:79]\n"
        "s_lshl_b32 s38, s52, 2\n"
        "s_add_u32 s38, s38, s28\n"
        "s_lshl_b32 s38, s38, 11\n"
        "s_add_u32 s38, s38, 0xf12000\n"
        "buffer_store_dwordx4 v[16:19], v1, s[20:23], s38 offen offset:0 sc1\n"
        "v_exp_f32 v104, v104\n"
        "v_exp_f32 v105, v105\n"
        "v_cvt_pk_bf16_f32 v115, v102, v103\n"
        "v_exp_f32 v106, v106\n"
        "v_exp_f32 v107, v107\n"
        "v_mfma_f32_32x32x16_bf16 v[144:159], a[184:187], v[112:115], v[144:159]\n"
        "v_cvt_pk_bf16_f32 v116, v104, v105\n"
        "v_exp_f32 v108, v108\n"
        "v_exp_f32 v109, v109\n"
        "v_cvt_pk_bf16_f32 v117, v106, v107\n"
        "v_exp_f32 v110, v110\n"
        "v_exp_f32 v111, v111\n"
        "v_cvt_pk_bf16_f32 v118, v108, v109\n"
        "v_exp_f32 v80, v80\n"
        "v_exp_f32 v81, v81\n"
        "v_cvt_pk_bf16_f32 v119, v110, v111\n"
        "v_exp_f32 v82, v82\n"
        "v_exp_f32 v83, v83\n"
        "v_mfma_f32_32x32x16_bf16 v[144:159], a[188:191], v[116:119], v[144:159]\n"
        "buffer_load_dwordx4 v1, s[16:19], s42 offen offset:2048 lds\n"
        "v_cvt_pk_bf16_f32 v112, v80, v81\n"
        "v_exp_f32 v84, v84\n"
        "v_exp_f32 v85, v85\n"
        "v_cvt_pk_bf16_f32 v113, v82, v83\n"
        "v_exp_f32 v86, v86\n"
        "v_exp_f32 v87, v87\n"
        "v_cvt_pk_bf16_f32 v114, v84, v85\n"
        "v_mfma_f32_32x32x16_bf16 v[96:111], a[4:7], v[32:35], v[64:79]\n"
        "v_exp_f32 v88, v88\n"
        "v_exp_f32 v89, v89\n"
        "v_cvt_pk_bf16_f32 v115, v86, v87\n"
        "v_exp_f32 v90, v90\n"
        "v_exp_f32 v91, v91\n"
        "v_mfma_f32_32x32x16_bf16 v[128:143], a[64:67], v[112:115], 0\n"
        "buffer_load_dwordx4 v1, s[16:19], s42 offen offset:3072 lds\n"
        "v_cvt_pk_bf16_f32 v116, v88, v89\n"
        "v_exp_f32 v92, v92\n"
        "v_exp_f32 v93, v93\n"
        "v_cvt_pk_bf16_f32 v117, v90, v91\n"
        "v_exp_f32 v94, v94\n"
        "v_exp_f32 v95, v95\n"
        "v_cvt_pk_bf16_f32 v118, v92, v93\n"
        "v_exp_f32 v96, v96\n"
        "v_exp_f32 v97, v97\n"
        "v_cvt_pk_bf16_f32 v119, v94, v95\n"
        "v_exp_f32 v98, v98\n"
        "v_exp_f32 v99, v99\n"
        "v_mfma_f32_32x32x16_bf16 v[128:143], a[68:71], v[116:119], v[128:143]\n"
        "ds_write_b96 v5, v[148:150]\n"
        "v_cvt_pk_bf16_f32 v112, v96, v97\n"
        "v_exp_f32 v100, v100\n"
        "v_exp_f32 v101, v101\n"
        "v_cvt_pk_bf16_f32 v113, v98, v99\n"
        "v_exp_f32 v102, v102\n"
        "v_exp_f32 v103, v103\n"
        "v_cvt_pk_bf16_f32 v114, v100, v101\n"
        "v_mfma_f32_32x32x16_bf16 v[80:95], a[8:11], v[32:35], v[64:79]\n"
        "ds_write_b96 v6, v[152:154]\n"
        "v_exp_f32 v104, v104\n"
        "v_exp_f32 v105, v105\n"
        "v_cvt_pk_bf16_f32 v115, v102, v103\n"
        "v_exp_f32 v106, v106\n"
        "v_exp_f32 v107, v107\n"
        "v_mfma_f32_32x32x16_bf16 v[128:143], a[72:75], v[112:115], v[128:143]\n"
        "ds_write_b96 v7, v[156:158]\n"
        "v_cvt_pk_bf16_f32 v116, v104, v105\n"
        "v_exp_f32 v108, v108\n"
        "v_exp_f32 v109, v109\n"
        "v_cvt_pk_bf16_f32 v117, v106, v107\n"
        "v_exp_f32 v110, v110\n"
        "v_exp_f32 v111, v111\n"
        "v_cvt_pk_bf16_f32 v118, v108, v109\n"
        "v_exp_f32 v80, v80\n"
        "v_exp_f32 v81, v81\n"
        "v_cvt_pk_bf16_f32 v119, v110, v111\n"
        "v_exp_f32 v82, v82\n"
        "v_exp_f32 v83, v83\n"
        "v_mfma_f32_32x32x16_bf16 v[128:143], a[76:79], v[116:119], v[128:143]\n"
        "s_waitcnt lgkmcnt(0)\n"
        "s_barrier\n"
        "v_cvt_pk_bf16_f32 v112, v80, v81\n"
        "v_exp_f32 v84, v84\n"
        "v_exp_f32 v85, v85\n"
        "v_cvt_pk_bf16_f32 v113, v82, v83\n"
        "v_exp_f32 v86, v86\n"
        "v_exp_f32 v87, v87\n"
        "v_cvt_pk_bf16_f32 v114, v84, v85\n"
        "v_mfma_f32_32x32x16_bf16 v[96:111], a[12:15], v[32:35], v[64:79]\n"
        "ds_read_b128 v[192:195], v8\n"
        "v_exp_f32 v88, v88\n"
        "v_exp_f32 v89, v89\n"
        "v_cvt_pk_bf16_f32 v115, v86, v87\n"
        "v_exp_f32 v90, v90\n"
        "v_exp_f32 v91, v91\n"
        "v_mfma_f32_32x32x16_bf16 v[128:143], a[80:83], v[112:115], v[128:143]\n"
        "ds_read_b128 v[196:199], v8 offset:1024\n"
        "v_cvt_pk_bf16_f32 v116, v88, v89\n"
        "v_exp_f32 v92, v92\n"
        "v_exp_f32 v93, v93\n"
        "v_cvt_pk_bf16_f32 v117, v90, v91\n"
        "v_exp_f32 v94, v94\n"
        "v_exp_f32 v95, v95\n"
        "v_cvt_pk_bf16_f32 v118, v92, v93\n"
        "v_exp_f32 v96, v96\n"
        "v_exp_f32 v97, v97\n"
        "v_cvt_pk_bf16_f32 v119, v94, v95\n"
        "v_exp_f32 v98, v98\n"
        "v_exp_f32 v99, v99\n"
        "v_mfma_f32_32x32x16_bf16 v[128:143], a[84:87], v[116:119], v[128:143]\n"
        "ds_read_b128 v[200:203], v8 offset:2048\n"
        "v_cvt_pk_bf16_f32 v112, v96, v97\n"
        "v_exp_f32 v100, v100\n"
        "v_exp_f32 v101, v101\n"
        "v_cvt_pk_bf16_f32 v113, v98, v99\n"
        "v_exp_f32 v102, v102\n"
        "v_exp_f32 v103, v103\n"
        "v_cvt_pk_bf16_f32 v114, v100, v101\n"
        "v_mfma_f32_32x32x16_bf16 v[80:95], a[16:19], v[32:35], v[64:79]\n"
        "buffer_load_dwordx4 v[36:39], v1, s[16:19], s58 offen offset:1024\n"
        "v_exp_f32 v104, v104\n"
        "v_exp_f32 v105, v105\n"
        "v_cvt_pk_bf16_f32 v115, v102, v103\n"
        "v_exp_f32 v106, v106\n"
        "v_exp_f32 v107, v107\n"
        "v_mfma_f32_32x32x16_bf16 v[128:143], a[88:91], v[112:115], v[128:143]\n"
        "s_waitcnt lgkmcnt(0)\n"
        "v_cvt_pk_bf16_f32 v116, v104, v105\n"
        "v_exp_f32 v108, v108\n"
        "v_exp_f32 v109, v109\n"
        "v_cvt_pk_bf16_f32 v117, v106, v107\n"
        "v_exp_f32 v110, v110\n"
        "v_add_f32 v212, v144, v192\n"
        "v_add_f32 v213, v145, v193\n"
        "v_add_f32 v214, v146, v194\n"
        "v_exp_f32 v111, v111\n"
        "v_cvt_pk_bf16_f32 v118, v108, v109\n"
        "v_exp_f32 v80, v80\n"
        "v_exp_f32 v81, v81\n"
        "v_cvt_pk_bf16_f32 v119, v110, v111\n"
        "v_exp_f32 v82, v82\n"
        "v_exp_f32 v83, v83\n"
        "v_mfma_f32_32x32x16_bf16 v[128:143], a[92:95], v[116:119], v[128:143]\n"
        "s_add_u32 m0, s41, 0x5000\n"
        "s_add_u32 s42, m0, s43\n"
        "buffer_load_dwordx4 v1, s[16:19], s42 offen offset:0 lds\n"
        "v_cvt_pk_bf16_f32 v112, v80, v81\n"
        "v_exp_f32 v84, v84\n"
        "v_exp_f32 v85, v85\n"
        "v_cvt_pk_bf16_f32 v113, v82, v83\n"
        "v_exp_f32 v86, v86\n"
        "v_add_f32 v196, v196, v200\n"
        "v_add_f32 v197, v197, v201\n"
        "v_add_f32 v198, v198, v202\n"
        "v_exp_f32 v87, v87\n"
        "v_cvt_pk_bf16_f32 v114, v84, v85\n"
        "v_mfma_f32_32x32x16_bf16 v[96:111], a[20:23], v[32:35], v[64:79]\n"
        "v_add_f32 v212, v212, v196\n"
        "v_add_f32 v213, v213, v197\n"
        "v_add_f32 v214, v214, v198\n"
        "v_exp_f32 v88, v88\n"
        "v_exp_f32 v89, v89\n"
        "v_cvt_pk_bf16_f32 v115, v86, v87\n"
        "v_exp_f32 v90, v90\n"
        "v_exp_f32 v91, v91\n"
        "v_mfma_f32_32x32x16_bf16 v[128:143], a[96:99], v[112:115], v[128:143]\n"
        "v_rcp_f32 v214, v214\n"
        "v_cvt_pk_bf16_f32 v116, v88, v89\n"
        "v_exp_f32 v92, v92\n"
        "v_exp_f32 v93, v93\n"
        "v_cvt_pk_bf16_f32 v117, v90, v91\n"
        "v_exp_f32 v94, v94\n"
        "v_mul_f32 v212, v212, v214\n"
        "v_mul_f32 v213, v213, v214\n"
        "v_exp_f32 v95, v95\n"
        "v_cvt_pk_bf16_f32 v118, v92, v93\n"
        "v_exp_f32 v96, v96\n"
        "v_exp_f32 v97, v97\n"
        "v_cvt_pk_bf16_f32 v119, v94, v95\n"
        "v_exp_f32 v98, v98\n"
        "v_exp_f32 v99, v99\n"
        "v_mfma_f32_32x32x16_bf16 v[128:143], a[100:103], v[116:119], v[128:143]\n"
        "v_cvt_pk_bf16_f32 v212, v212, v213\n"
        "v_cvt_pk_bf16_f32 v112, v96, v97\n"
        "v_exp_f32 v100, v100\n"
        "v_exp_f32 v101, v101\n"
        "v_cvt_pk_bf16_f32 v113, v98, v99\n"
        "v_exp_f32 v102, v102\n"
        "v_exp_f32 v103, v103\n"
        "v_cvt_pk_bf16_f32 v114, v100, v101\n"
        "v_mfma_f32_32x32x16_bf16 v[80:95], a[24:27], v[32:35], v[64:79]\n"
        "ds_write_b32 v9, v212\n"
        "v_exp_f32 v104, v104\n"
        "v_exp_f32 v105, v105\n"
        "v_cvt_pk_bf16_f32 v115, v102, v103\n"
        "v_exp_f32 v106, v106\n"
        "v_exp_f32 v107, v107\n"
        "v_mfma_f32_32x32x16_bf16 v[128:143], a[104:107], v[112:115], v[128:143]\n"
        "buffer_load_dwordx4 v1, s[16:19], s42 offen offset:1024 lds\n"
        "v_cvt_pk_bf16_f32 v116, v104, v105\n"
        "v_exp_f32 v108, v108\n"
        "v_exp_f32 v109, v109\n"
        "v_cvt_pk_bf16_f32 v117, v106, v107\n"
        "v_exp_f32 v110, v110\n"
        "v_exp_f32 v111, v111\n"
        "v_cvt_pk_bf16_f32 v118, v108, v109\n"
        "v_exp_f32 v80, v80\n"
        "v_exp_f32 v81, v81\n"
        "v_cvt_pk_bf16_f32 v119, v110, v111\n"
        "v_exp_f32 v82, v82\n"
        "v_exp_f32 v83, v83\n"
        "v_mfma_f32_32x32x16_bf16 v[128:143], a[108:111], v[116:119], v[128:143]\n"
        "s_waitcnt lgkmcnt(0)\n"
        "s_barrier\n"
        "s_add_u32 s36, s35, 0x10000\n"
        "v_cvt_pk_bf16_f32 v112, v80, v81\n"
        "v_exp_f32 v84, v84\n"
        "v_exp_f32 v85, v85\n"
        "v_cvt_pk_bf16_f32 v113, v82, v83\n"
        "v_exp_f32 v86, v86\n"
        "v_exp_f32 v87, v87\n"
        "v_cvt_pk_bf16_f32 v114, v84, v85\n"
        "v_mfma_f32_32x32x16_bf16 v[96:111], a[28:31], v[32:35], v[64:79]\n"
        "ds_read_b128 v[204:207], v10\n"
        "v_exp_f32 v88, v88\n"
        "v_exp_f32 v89, v89\n"
        "v_cvt_pk_bf16_f32 v115, v86, v87\n"
        "v_exp_f32 v90, v90\n"
        "v_exp_f32 v91, v91\n"
        "v_mfma_f32_32x32x16_bf16 v[128:143], a[112:115], v[112:115], v[128:143]\n"
        "ds_read_b128 v[208:211], v10 offset:512\n"
        "v_cvt_pk_bf16_f32 v116, v88, v89\n"
        "v_exp_f32 v92, v92\n"
        "v_exp_f32 v93, v93\n"
        "v_cvt_pk_bf16_f32 v117, v90, v91\n"
        "v_exp_f32 v94, v94\n"
        "v_exp_f32 v95, v95\n"
        "v_cvt_pk_bf16_f32 v118, v92, v93\n"
        "v_exp_f32 v96, v96\n"
        "v_exp_f32 v97, v97\n"
        "v_cvt_pk_bf16_f32 v119, v94, v95\n"
        "v_exp_f32 v98, v98\n"
        "v_exp_f32 v99, v99\n"
        "v_mfma_f32_32x32x16_bf16 v[128:143], a[116:119], v[116:119], v[128:143]\n"
        "s_waitcnt vmcnt(10) lgkmcnt(0)\n"
        "v_cvt_pk_bf16_f32 v112, v96, v97\n"
        "v_exp_f32 v100, v100\n"
        "v_exp_f32 v101, v101\n"
        "v_cvt_pk_bf16_f32 v113, v98, v99\n"
        "v_exp_f32 v102, v102\n"
        "v_mfma_f32_16x16x32_bf16 v[160:163], a[192:195], v[204:207], v[160:163]\n"
        "v_exp_f32 v103, v103\n"
        "v_cvt_pk_bf16_f32 v114, v100, v101\n"
        "v_mfma_f32_32x32x16_bf16 v[80:95], a[32:35], v[32:35], v[64:79]\n"
        "v_exp_f32 v104, v104\n"
        "v_exp_f32 v105, v105\n"
        "v_cvt_pk_bf16_f32 v115, v102, v103\n"
        "v_exp_f32 v106, v106\n"
        "v_mfma_f32_16x16x32_bf16 v[164:167], a[196:199], v[204:207], v[164:167]\n"
        "v_exp_f32 v107, v107\n"
        "v_mfma_f32_32x32x16_bf16 v[128:143], a[120:123], v[112:115], v[128:143]\n"
        "v_cvt_pk_bf16_f32 v116, v104, v105\n"
        "v_exp_f32 v108, v108\n"
        "v_exp_f32 v109, v109\n"
        "v_cvt_pk_bf16_f32 v117, v106, v107\n"
        "v_exp_f32 v110, v110\n"
        "v_mfma_f32_16x16x32_bf16 v[168:171], a[200:203], v[204:207], v[168:171]\n"
        "v_exp_f32 v111, v111\n"
        "v_cvt_pk_bf16_f32 v118, v108, v109\n"
        "v_exp_f32 v80, v80\n"
        "v_exp_f32 v81, v81\n"
        "v_cvt_pk_bf16_f32 v119, v110, v111\n"
        "v_exp_f32 v82, v82\n"
        "v_exp_f32 v83, v83\n"
        "v_mfma_f32_32x32x16_bf16 v[128:143], a[124:127], v[116:119], v[128:143]\n"
        "buffer_load_dwordx4 a[192:195], v1, s[16:19], s36 offen offset:0\n"
        "v_cvt_pk_bf16_f32 v112, v80, v81\n"
        "v_exp_f32 v84, v84\n"
        "v_exp_f32 v85, v85\n"
        "v_cvt_pk_bf16_f32 v113, v82, v83\n"
        "v_exp_f32 v86, v86\n"
        "v_mfma_f32_16x16x32_bf16 v[172:175], a[204:207], v[204:207], v[172:175]\n"
        "v_exp_f32 v87, v87\n"
        "v_cvt_pk_bf16_f32 v114, v84, v85\n"
        "v_mfma_f32_32x32x16_bf16 v[96:111], a[36:39], v[32:35], v[64:79]\n"
        "buffer_load_dwordx4 a[196:199], v1, s[16:19], s36 offen offset:1024\n"
        "v_exp_f32 v88, v88\n"
        "v_exp_f32 v89, v89\n"
        "v_cvt_pk_bf16_f32 v115, v86, v87\n"
        "v_exp_f32 v90, v90\n"
        "v_mfma_f32_16x16x32_bf16 v[176:179], a[208:211], v[204:207], v[176:179]\n"
        "v_exp_f32 v91, v91\n"
        "v_mfma_f32_32x32x16_bf16 v[128:143], a[128:131], v[112:115], v[128:143]\n"
        "buffer_load_dwordx4 a[200:203], v1, s[16:19], s36 offen offset:2048\n"
        "v_cvt_pk_bf16_f32 v116, v88, v89\n"
        "v_exp_f32 v92, v92\n"
        "v_exp_f32 v93, v93\n"
        "v_cvt_pk_bf16_f32 v117, v90, v91\n"
        "v_exp_f32 v94, v94\n"
        "v_mfma_f32_16x16x32_bf16 v[180:183], a[212:215], v[204:207], v[180:183]\n"
        "v_exp_f32 v95, v95\n"
        "v_cvt_pk_bf16_f32 v118, v92, v93\n"
        "v_exp_f32 v96, v96\n"
        "v_exp_f32 v97, v97\n"
        "v_cvt_pk_bf16_f32 v119, v94, v95\n"
        "v_exp_f32 v98, v98\n"
        "v_exp_f32 v99, v99\n"
        "v_mfma_f32_32x32x16_bf16 v[128:143], a[132:135], v[116:119], v[128:143]\n"
        "buffer_load_dwordx4 a[204:207], v1, s[16:19], s36 offen offset:3072\n"
        "v_cvt_pk_bf16_f32 v112, v96, v97\n"
        "v_exp_f32 v100, v100\n"
        "v_exp_f32 v101, v101\n"
        "v_cvt_pk_bf16_f32 v113, v98, v99\n"
        "v_exp_f32 v102, v102\n"
        "v_mfma_f32_16x16x32_bf16 v[184:187], a[216:219], v[204:207], v[184:187]\n"
        "v_exp_f32 v103, v103\n"
        "v_cvt_pk_bf16_f32 v114, v100, v101\n"
        "v_mfma_f32_32x32x16_bf16 v[80:95], a[40:43], v[32:35], v[64:79]\n"
        "buffer_load_dwordx4 a[208:211], v24, s[16:19], s36 offen offset:0\n"
        "v_exp_f32 v104, v104\n"
        "v_exp_f32 v105, v105\n"
        "v_cvt_pk_bf16_f32 v115, v102, v103\n"
        "v_exp_f32 v106, v106\n"
        "v_mfma_f32_16x16x32_bf16 v[188:191], a[220:223], v[204:207], v[188:191]\n"
        "v_exp_f32 v107, v107\n"
        "v_mfma_f32_32x32x16_bf16 v[128:143], a[136:139], v[112:115], v[128:143]\n"
        "buffer_load_dwordx4 a[212:215], v24, s[16:19], s36 offen offset:1024\n"
        "v_cvt_pk_bf16_f32 v116, v104, v105\n"
        "v_exp_f32 v108, v108\n"
        "v_exp_f32 v109, v109\n"
        "v_cvt_pk_bf16_f32 v117, v106, v107\n"
        "v_exp_f32 v110, v110\n"
        "v_mfma_f32_16x16x32_bf16 v[160:163], a[224:227], v[208:211], v[160:163]\n"
        "v_exp_f32 v111, v111\n"
        "v_cvt_pk_bf16_f32 v118, v108, v109\n"
        "v_exp_f32 v80, v80\n"
        "v_exp_f32 v81, v81\n"
        "v_cvt_pk_bf16_f32 v119, v110, v111\n"
        "v_exp_f32 v82, v82\n"
        "v_exp_f32 v83, v83\n"
        "v_mfma_f32_32x32x16_bf16 v[128:143], a[140:143], v[116:119], v[128:143]\n"
        "buffer_load_dwordx4 a[216:219], v24, s[16:19], s36 offen offset:2048\n"
        "v_cvt_pk_bf16_f32 v112, v80, v81\n"
        "v_exp_f32 v84, v84\n"
        "v_exp_f32 v85, v85\n"
        "v_cvt_pk_bf16_f32 v113, v82, v83\n"
        "v_exp_f32 v86, v86\n"
        "v_mfma_f32_16x16x32_bf16 v[164:167], a[228:231], v[208:211], v[164:167]\n"
        "v_exp_f32 v87, v87\n"
        "v_cvt_pk_bf16_f32 v114, v84, v85\n"
        "v_mfma_f32_32x32x16_bf16 v[96:111], a[44:47], v[32:35], v[64:79]\n"
        "buffer_load_dwordx4 a[220:223], v24, s[16:19], s36 offen offset:3072\n"
        "v_exp_f32 v88, v88\n"
        "v_exp_f32 v89, v89\n"
        "v_cvt_pk_bf16_f32 v115, v86, v87\n"
        "v_exp_f32 v90, v90\n"
        "v_mfma_f32_16x16x32_bf16 v[168:171], a[232:235], v[208:211], v[168:171]\n"
        "v_exp_f32 v91, v91\n"
        "v_mfma_f32_32x32x16_bf16 v[128:143], a[144:147], v[112:115], v[128:143]\n"
        "buffer_load_dwordx4 a[224:227], v25, s[16:19], s36 offen offset:0\n"
        "v_cvt_pk_bf16_f32 v116, v88, v89\n"
        "v_exp_f32 v92, v92\n"
        "v_exp_f32 v93, v93\n"
        "v_cvt_pk_bf16_f32 v117, v90, v91\n"
        "v_exp_f32 v94, v94\n"
        "v_mfma_f32_16x16x32_bf16 v[172:175], a[236:239], v[208:211], v[172:175]\n"
        "v_exp_f32 v95, v95\n"
        "v_cvt_pk_bf16_f32 v118, v92, v93\n"
        "v_exp_f32 v96, v96\n"
        "v_exp_f32 v97, v97\n"
        "v_cvt_pk_bf16_f32 v119, v94, v95\n"
        "v_exp_f32 v98, v98\n"
        "v_exp_f32 v99, v99\n"
        "v_mfma_f32_32x32x16_bf16 v[128:143], a[148:151], v[116:119], v[128:143]\n"
        "buffer_load_dwordx4 a[228:231], v25, s[16:19], s36 offen offset:1024\n"
        "v_cvt_pk_bf16_f32 v112, v96, v97\n"
        "v_exp_f32 v100, v100\n"
        "v_exp_f32 v101, v101\n"
        "v_cvt_pk_bf16_f32 v113, v98, v99\n"
        "v_exp_f32 v102, v102\n"
        "v_mfma_f32_16x16x32_bf16 v[176:179], a[240:243], v[208:211], v[176:179]\n"
        "v_exp_f32 v103, v103\n"
        "v_cvt_pk_bf16_f32 v114, v100, v101\n"
        "v_mfma_f32_32x32x16_bf16 v[80:95], a[48:51], v[32:35], v[64:79]\n"
        "buffer_load_dwordx4 a[232:235], v25, s[16:19], s36 offen offset:2048\n"
        "v_exp_f32 v104, v104\n"
        "v_exp_f32 v105, v105\n"
        "v_cvt_pk_bf16_f32 v115, v102, v103\n"
        "v_exp_f32 v106, v106\n"
        "v_mfma_f32_16x16x32_bf16 v[180:183], a[244:247], v[208:211], v[180:183]\n"
        "v_exp_f32 v107, v107\n"
        "v_mfma_f32_32x32x16_bf16 v[128:143], a[152:155], v[112:115], v[128:143]\n"
        "buffer_load_dwordx4 a[236:239], v25, s[16:19], s36 offen offset:3072\n"
        "v_cvt_pk_bf16_f32 v116, v104, v105\n"
        "v_exp_f32 v108, v108\n"
        "v_exp_f32 v109, v109\n"
        "v_cvt_pk_bf16_f32 v117, v106, v107\n"
        "v_exp_f32 v110, v110\n"
        "v_mfma_f32_16x16x32_bf16 v[184:187], a[248:251], v[208:211], v[184:187]\n"
        "v_exp_f32 v111, v111\n"
        "v_cvt_pk_bf16_f32 v118, v108, v109\n"
        "v_exp_f32 v80, v80\n"
        "v_exp_f32 v81, v81\n"
        "v_cvt_pk_bf16_f32 v119, v110, v111\n"
        "v_exp_f32 v82, v82\n"
        "v_exp_f32 v83, v83\n"
        "v_mfma_f32_32x32x16_bf16 v[128:143], a[156:159], v[116:119], v[128:143]\n"
        "buffer_load_dwordx4 a[240:243], v26, s[16:19], s36 offen offset:0\n"
        "v_cvt_pk_bf16_f32 v112, v80, v81\n"
        "v_exp_f32 v84, v84\n"
        "v_exp_f32 v85, v85\n"
        "v_cvt_pk_bf16_f32 v113, v82, v83\n"
        "v_exp_f32 v86, v86\n"
        "v_mfma_f32_16x16x32_bf16 v[188:191], a[252:255], v[208:211], v[188:191]\n"
        "v_exp_f32 v87, v87\n"
        "v_cvt_pk_bf16_f32 v114, v84, v85\n"
        "v_mfma_f32_32x32x16_bf16 v[96:111], a[52:55], v[32:35], v[64:79]\n"
        "buffer_load_dwordx4 a[244:247], v26, s[16:19], s36 offen offset:1024\n"
        "v_exp_f32 v88, v88\n"
        "v_exp_f32 v89, v89\n"
        "v_cvt_pk_bf16_f32 v115, v86, v87\n"
        "v_exp_f32 v90, v90\n"
        "v_exp_f32 v91, v91\n"
        "v_mfma_f32_32x32x16_bf16 v[128:143], a[160:163], v[112:115], v[128:143]\n"
        "buffer_load_dwordx4 a[248:251], v26, s[16:19], s36 offen offset:2048\n"
        "v_cvt_pk_bf16_f32 v116, v88, v89\n"
        "v_exp_f32 v92, v92\n"
        "v_exp_f32 v93, v93\n"
        "v_cvt_pk_bf16_f32 v117, v90, v91\n"
        "v_exp_f32 v94, v94\n"
        "v_exp_f32 v95, v95\n"
        "v_cvt_pk_bf16_f32 v118, v92, v93\n"
        "v_exp_f32 v96, v96\n"
        "v_exp_f32 v97, v97\n"
        "v_cvt_pk_bf16_f32 v119, v94, v95\n"
        "v_exp_f32 v98, v98\n"
        "v_exp_f32 v99, v99\n"
        "v_mfma_f32_32x32x16_bf16 v[128:143], a[164:167], v[116:119], v[128:143]\n"
        "buffer_load_dwordx4 a[252:255], v26, s[16:19], s36 offen offset:3072\n"
        "v_cvt_pk_bf16_f32 v112, v96, v97\n"
        "v_exp_f32 v100, v100\n"
        "v_exp_f32 v101, v101\n"
        "v_cvt_pk_bf16_f32 v113, v98, v99\n"
        "v_exp_f32 v102, v102\n"
        "v_exp_f32 v103, v103\n"
        "v_cvt_pk_bf16_f32 v114, v100, v101\n"
        "v_mfma_f32_32x32x16_bf16 v[80:95], a[56:59], v[32:35], v[64:79]\n"
        "buffer_store_dwordx4 v[16:19], v1, s[20:23], s38 offen offset:1024 sc1\n"
        "v_exp_f32 v104, v104\n"
        "v_exp_f32 v105, v105\n"
        "v_cvt_pk_bf16_f32 v115, v102, v103\n"
        "v_exp_f32 v106, v106\n"
        "v_exp_f32 v107, v107\n"
        "v_mfma_f32_32x32x16_bf16 v[128:143], a[168:171], v[112:115], v[128:143]\n"
        "v_cvt_pk_bf16_f32 v116, v104, v105\n"
        "v_exp_f32 v108, v108\n"
        "v_exp_f32 v109, v109\n"
        "v_cvt_pk_bf16_f32 v117, v106, v107\n"
        "v_exp_f32 v110, v110\n"
        "v_exp_f32 v111, v111\n"
        "v_cvt_pk_bf16_f32 v118, v108, v109\n"
        "v_exp_f32 v80, v80\n"
        "v_exp_f32 v81, v81\n"
        "v_cvt_pk_bf16_f32 v119, v110, v111\n"
        "v_exp_f32 v82, v82\n"
        "v_exp_f32 v83, v83\n"
        "v_mfma_f32_32x32x16_bf16 v[128:143], a[172:175], v[116:119], v[128:143]\n"
        "buffer_load_dwordx4 v1, s[16:19], s42 offen offset:2048 lds\n"
        "v_cvt_pk_bf16_f32 v112, v80, v81\n"
        "v_exp_f32 v84, v84\n"
        "v_exp_f32 v85, v85\n"
        "v_cvt_pk_bf16_f32 v113, v82, v83\n"
        "v_exp_f32 v86, v86\n"
        "v_exp_f32 v87, v87\n"
        "v_cvt_pk_bf16_f32 v114, v84, v85\n"
        "v_mfma_f32_32x32x16_bf16 v[96:111], a[60:63], v[32:35], v[64:79]\n"
        "v_exp_f32 v88, v88\n"
        "v_exp_f32 v89, v89\n"
        "v_cvt_pk_bf16_f32 v115, v86, v87\n"
        "v_exp_f32 v90, v90\n"
        "v_exp_f32 v91, v91\n"
        "v_mfma_f32_32x32x16_bf16 v[128:143], a[176:179], v[112:115], v[128:143]\n"
        "v_cvt_pk_bf16_f32 v116, v88, v89\n"
        "v_exp_f32 v92, v92\n"
        "v_exp_f32 v93, v93\n"
        "v_cvt_pk_bf16_f32 v117, v90, v91\n"
        "v_exp_f32 v94, v94\n"
        "v_exp_f32 v95, v95\n"
        "v_cvt_pk_bf16_f32 v118, v92, v93\n"
        "v_exp_f32 v96, v96\n"
        "v_exp_f32 v97, v97\n"
        "v_cvt_pk_bf16_f32 v119, v94, v95\n"
        "v_exp_f32 v98, v98\n"
        "v_exp_f32 v99, v99\n"
        "v_mfma_f32_32x32x16_bf16 v[128:143], a[180:183], v[116:119], v[128:143]\n"
        "buffer_load_dwordx4 v1, s[16:19], s42 offen offset:3072 lds\n"
        "v_cvt_pk_bf16_f32 v112, v96, v97\n"
        "v_exp_f32 v100, v100\n"
        "v_exp_f32 v101, v101\n"
        "v_cvt_pk_bf16_f32 v113, v98, v99\n"
        "v_exp_f32 v102, v102\n"
        "v_exp_f32 v103, v103\n"
        "v_cvt_pk_bf16_f32 v114, v100, v101\n"
        "s_waitcnt vmcnt(21)\n"
        "v_mfma_f32_32x32x16_bf16 v[80:95], a[0:3], v[36:39], v[64:79]\n"
        "v_exp_f32 v104, v104\n"
        "v_exp_f32 v105, v105\n"
        "v_cvt_pk_bf16_f32 v115, v102, v103\n"
        "v_exp_f32 v106, v106\n"
        "v_exp_f32 v107, v107\n"
        "v_mfma_f32_32x32x16_bf16 v[128:143], a[184:187], v[112:115], v[128:143]\n"
        "v_cvt_pk_bf16_f32 v116, v104, v105\n"
        "v_exp_f32 v108, v108\n"
        "v_exp_f32 v109, v109\n"
        "v_cvt_pk_bf16_f32 v117, v106, v107\n"
        "v_exp_f32 v110, v110\n"
        "v_exp_f32 v111, v111\n"
        "v_cvt_pk_bf16_f32 v118, v108, v109\n"
        "v_exp_f32 v80, v80\n"
        "v_exp_f32 v81, v81\n"
        "v_cvt_pk_bf16_f32 v119, v110, v111\n"
        "v_exp_f32 v82, v82\n"
        "v_exp_f32 v83, v83\n"
        "v_mfma_f32_32x32x16_bf16 v[128:143], a[188:191], v[116:119], v[128:143]\n"
        "s_add_u32 m0, s41, 0x6000\n"
        "s_add_u32 s42, m0, s43\n"
        "buffer_load_dwordx4 v1, s[16:19], s42 offen offset:0 lds\n"
        "v_cvt_pk_bf16_f32 v112, v80, v81\n"
        "v_exp_f32 v84, v84\n"
        "v_exp_f32 v85, v85\n"
        "v_cvt_pk_bf16_f32 v113, v82, v83\n"
        "v_exp_f32 v86, v86\n"
        "v_exp_f32 v87, v87\n"
        "v_cvt_pk_bf16_f32 v114, v84, v85\n"
        "v_mfma_f32_32x32x16_bf16 v[96:111], a[4:7], v[36:39], v[64:79]\n"
        "v_exp_f32 v88, v88\n"
        "v_exp_f32 v89, v89\n"
        "v_cvt_pk_bf16_f32 v115, v86, v87\n"
        "v_exp_f32 v90, v90\n"
        "v_exp_f32 v91, v91\n"
        "v_mfma_f32_32x32x16_bf16 v[144:159], a[64:67], v[112:115], 0\n"
        "buffer_load_dwordx4 v1, s[16:19], s42 offen offset:1024 lds\n"
        "v_cvt_pk_bf16_f32 v116, v88, v89\n"
        "v_exp_f32 v92, v92\n"
        "v_exp_f32 v93, v93\n"
        "v_cvt_pk_bf16_f32 v117, v90, v91\n"
        "v_exp_f32 v94, v94\n"
        "v_exp_f32 v95, v95\n"
        "v_cvt_pk_bf16_f32 v118, v92, v93\n"
        "v_exp_f32 v96, v96\n"
        "v_exp_f32 v97, v97\n"
        "v_cvt_pk_bf16_f32 v119, v94, v95\n"
        "v_exp_f32 v98, v98\n"
        "v_exp_f32 v99, v99\n"
        "v_mfma_f32_32x32x16_bf16 v[144:159], a[68:71], v[116:119], v[144:159]\n"
        "ds_write_b96 v5, v[132:134]\n"
        "v_cvt_pk_bf16_f32 v112, v96, v97\n"
        "v_exp_f32 v100, v100\n"
        "v_exp_f32 v101, v101\n"
        "v_cvt_pk_bf16_f32 v113, v98, v99\n"
        "v_exp_f32 v102, v102\n"
        "v_exp_f32 v103, v103\n"
        "v_cvt_pk_bf16_f32 v114, v100, v101\n"
        "v_mfma_f32_32x32x16_bf16 v[80:95], a[8:11], v[36:39], v[64:79]\n"
        "ds_write_b96 v6, v[136:138]\n"
        "v_exp_f32 v104, v104\n"
        "v_exp_f32 v105, v105\n"
        "v_cvt_pk_bf16_f32 v115, v102, v103\n"
        "v_exp_f32 v106, v106\n"
        "v_exp_f32 v107, v107\n"
        "v_mfma_f32_32x32x16_bf16 v[144:159], a[72:75], v[112:115], v[144:159]\n"
        "ds_write_b96 v7, v[140:142]\n"
        "v_cvt_pk_bf16_f32 v116, v104, v105\n"
        "v_exp_f32 v108, v108\n"
        "v_exp_f32 v109, v109\n"
        "v_cvt_pk_bf16_f32 v117, v106, v107\n"
        "v_exp_f32 v110, v110\n"
        "v_exp_f32 v111, v111\n"
        "v_cvt_pk_bf16_f32 v118, v108, v109\n"
        "v_exp_f32 v80, v80\n"
        "v_exp_f32 v81, v81\n"
        "v_cvt_pk_bf16_f32 v119, v110, v111\n"
        "v_exp_f32 v82, v82\n"
        "v_exp_f32 v83, v83\n"
        "v_mfma_f32_32x32x16_bf16 v[144:159], a[76:79], v[116:119], v[144:159]\n"
        "s_waitcnt lgkmcnt(0)\n"
        "s_barrier\n"
        "v_cvt_pk_bf16_f32 v112, v80, v81\n"
        "v_exp_f32 v84, v84\n"
        "v_exp_f32 v85, v85\n"
        "v_cvt_pk_bf16_f32 v113, v82, v83\n"
        "v_exp_f32 v86, v86\n"
        "v_exp_f32 v87, v87\n"
        "v_cvt_pk_bf16_f32 v114, v84, v85\n"
        "v_mfma_f32_32x32x16_bf16 v[96:111], a[12:15], v[36:39], v[64:79]\n"
        "ds_read_b128 v[192:195], v8\n"
        "v_exp_f32 v88, v88\n"
        "v_exp_f32 v89, v89\n"
        "v_cvt_pk_bf16_f32 v115, v86, v87\n"
        "v_exp_f32 v90, v90\n"
        "v_exp_f32 v91, v91\n"
        "v_mfma_f32_32x32x16_bf16 v[144:159], a[80:83], v[112:115], v[144:159]\n"
        "ds_read_b128 v[196:199], v8 offset:1024\n"
        "v_cvt_pk_bf16_f32 v116, v88, v89\n"
        "v_exp_f32 v92, v92\n"
        "v_exp_f32 v93, v93\n"
        "v_cvt_pk_bf16_f32 v117, v90, v91\n"
        "v_exp_f32 v94, v94\n"
        "v_exp_f32 v95, v95\n"
        "v_cvt_pk_bf16_f32 v118, v92, v93\n"
        "v_exp_f32 v96, v96\n"
        "v_exp_f32 v97, v97\n"
        "v_cvt_pk_bf16_f32 v119, v94, v95\n"
        "v_exp_f32 v98, v98\n"
        "v_exp_f32 v99, v99\n"
        "v_mfma_f32_32x32x16_bf16 v[144:159], a[84:87], v[116:119], v[144:159]\n"
        "ds_read_b128 v[200:203], v8 offset:2048\n"
        "v_cvt_pk_bf16_f32 v112, v96, v97\n"
        "v_exp_f32 v100, v100\n"
        "v_exp_f32 v101, v101\n"
        "v_cvt_pk_bf16_f32 v113, v98, v99\n"
        "v_exp_f32 v102, v102\n"
        "v_exp_f32 v103, v103\n"
        "v_cvt_pk_bf16_f32 v114, v100, v101\n"
        "v_mfma_f32_32x32x16_bf16 v[80:95], a[16:19], v[36:39], v[64:79]\n"
        "buffer_load_dwordx4 v[32:35], v1, s[16:19], s58 offen offset:2048\n"
        "v_exp_f32 v104, v104\n"
        "v_exp_f32 v105, v105\n"
        "v_cvt_pk_bf16_f32 v115, v102, v103\n"
        "v_exp_f32 v106, v106\n"
        "v_exp_f32 v107, v107\n"
        "v_mfma_f32_32x32x16_bf16 v[144:159], a[88:91], v[112:115], v[144:159]\n"
        "s_waitcnt lgkmcnt(0)\n"
        "v_cvt_pk_bf16_f32 v116, v104, v105\n"
        "v_exp_f32 v108, v108\n"
        "v_exp_f32 v109, v109\n"
        "v_cvt_pk_bf16_f32 v117, v106, v107\n"
        "v_exp_f32 v110, v110\n"
        "v_add_f32 v212, v128, v192\n"
        "v_add_f32 v213, v129, v193\n"
        "v_add_f32 v214, v130, v194\n"
        "v_exp_f32 v111, v111\n"
        "v_cvt_pk_bf16_f32 v118, v108, v109\n"
        "v_exp_f32 v80, v80\n"
        "v_exp_f32 v81, v81\n"
        "v_cvt_pk_bf16_f32 v119, v110, v111\n"
        "v_exp_f32 v82, v82\n"
        "v_exp_f32 v83, v83\n"
        "v_mfma_f32_32x32x16_bf16 v[144:159], a[92:95], v[116:119], v[144:159]\n"
        "buffer_load_dwordx4 v1, s[16:19], s42 offen offset:2048 lds\n"
        "v_cvt_pk_bf16_f32 v112, v80, v81\n"
        "v_exp_f32 v84, v84\n"
        "v_exp_f32 v85, v85\n"
        "v_cvt_pk_bf16_f32 v113, v82, v83\n"
        "v_exp_f32 v86, v86\n"
        "v_add_f32 v196, v196, v200\n"
        "v_add_f32 v197, v197, v201\n"
        "v_add_f32 v198, v198, v202\n"
        "v_exp_f32 v87, v87\n"
        "v_cvt_pk_bf16_f32 v114, v84, v85\n"
        "v_mfma_f32_32x32x16_bf16 v[96:111], a[20:23], v[36:39], v[64:79]\n"
        "v_add_f32 v212, v212, v196\n"
        "v_add_f32 v213, v213, v197\n"
        "v_add_f32 v214, v214, v198\n"
        "v_exp_f32 v88, v88\n"
        "v_exp_f32 v89, v89\n"
        "v_cvt_pk_bf16_f32 v115, v86, v87\n"
        "v_exp_f32 v90, v90\n"
        "v_exp_f32 v91, v91\n"
        "v_mfma_f32_32x32x16_bf16 v[144:159], a[96:99], v[112:115], v[144:159]\n"
        "v_rcp_f32 v214, v214\n"
        "v_cvt_pk_bf16_f32 v116, v88, v89\n"
        "v_exp_f32 v92, v92\n"
        "v_exp_f32 v93, v93\n"
        "v_cvt_pk_bf16_f32 v117, v90, v91\n"
        "v_exp_f32 v94, v94\n"
        "v_mul_f32 v212, v212, v214\n"
        "v_mul_f32 v213, v213, v214\n"
        "v_exp_f32 v95, v95\n"
        "v_cvt_pk_bf16_f32 v118, v92, v93\n"
        "v_exp_f32 v96, v96\n"
        "v_exp_f32 v97, v97\n"
        "v_cvt_pk_bf16_f32 v119, v94, v95\n"
        "v_exp_f32 v98, v98\n"
        "v_exp_f32 v99, v99\n"
        "v_mfma_f32_32x32x16_bf16 v[144:159], a[100:103], v[116:119], v[144:159]\n"
        "v_cvt_pk_bf16_f32 v212, v212, v213\n"
        "v_cvt_pk_bf16_f32 v112, v96, v97\n"
        "v_exp_f32 v100, v100\n"
        "v_exp_f32 v101, v101\n"
        "v_cvt_pk_bf16_f32 v113, v98, v99\n"
        "v_exp_f32 v102, v102\n"
        "v_exp_f32 v103, v103\n"
        "v_cvt_pk_bf16_f32 v114, v100, v101\n"
        "v_mfma_f32_32x32x16_bf16 v[80:95], a[24:27], v[36:39], v[64:79]\n"
        "ds_write_b32 v9, v212\n"
        "v_exp_f32 v104, v104\n"
        "v_exp_f32 v105, v105\n"
        "v_cvt_pk_bf16_f32 v115, v102, v103\n"
        "v_exp_f32 v106, v106\n"
        "v_exp_f32 v107, v107\n"
        "v_mfma_f32_32x32x16_bf16 v[144:159], a[104:107], v[112:115], v[144:159]\n"
        "buffer_load_dwordx4 v1, s[16:19], s42 offen offset:3072 lds\n"
        "v_cvt_pk_bf16_f32 v116, v104, v105\n"
        "v_exp_f32 v108, v108\n"
        "v_exp_f32 v109, v109\n"
        "v_cvt_pk_bf16_f32 v117, v106, v107\n"
        "v_exp_f32 v110, v110\n"
        "v_exp_f32 v111, v111\n"
        "v_cvt_pk_bf16_f32 v118, v108, v109\n"
        "v_exp_f32 v80, v80\n"
        "v_exp_f32 v81, v81\n"
        "v_cvt_pk_bf16_f32 v119, v110, v111\n"
        "v_exp_f32 v82, v82\n"
        "v_exp_f32 v83, v83\n"
        "v_mfma_f32_32x32x16_bf16 v[144:159], a[108:111], v[116:119], v[144:159]\n"
        "s_waitcnt lgkmcnt(0)\n"
        "s_barrier\n"
        "s_add_u32 s36, s35, 0x14000\n"
        "v_cvt_pk_bf16_f32 v112, v80, v81\n"
        "v_exp_f32 v84, v84\n"
        "v_exp_f32 v85, v85\n"
        "v_cvt_pk_bf16_f32 v113, v82, v83\n"
        "v_exp_f32 v86, v86\n"
        "v_exp_f32 v87, v87\n"
        "v_cvt_pk_bf16_f32 v114, v84, v85\n"
        "v_mfma_f32_32x32x16_bf16 v[96:111], a[28:31], v[36:39], v[64:79]\n"
        "ds_read_b128 v[204:207], v10\n"
        "v_exp_f32 v88, v88\n"
        "v_exp_f32 v89, v89\n"
        "v_cvt_pk_bf16_f32 v115, v86, v87\n"
        "v_exp_f32 v90, v90\n"
        "v_exp_f32 v91, v91\n"
        "v_mfma_f32_32x32x16_bf16 v[144:159], a[112:115], v[112:115], v[144:159]\n"
        "ds_read_b128 v[208:211], v10 offset:512\n"
        "v_cvt_pk_bf16_f32 v116, v88, v89\n"
        "v_exp_f32 v92, v92\n"
        "v_exp_f32 v93, v93\n"
        "v_cvt_pk_bf16_f32 v117, v90, v91\n"
        "v_exp_f32 v94, v94\n"
        "v_exp_f32 v95, v95\n"
        "v_cvt_pk_bf16_f32 v118, v92, v93\n"
        "v_exp_f32 v96, v96\n"
        "v_exp_f32 v97, v97\n"
        "v_cvt_pk_bf16_f32 v119, v94, v95\n"
        "v_exp_f32 v98, v98\n"
        "v_exp_f32 v99, v99\n"
        "v_mfma_f32_32x32x16_bf16 v[144:159], a[116:119], v[116:119], v[144:159]\n"
        "s_waitcnt vmcnt(8) lgkmcnt(0)\n"
        "v_cvt_pk_bf16_f32 v112, v96, v97\n"
        "v_exp_f32 v100, v100\n"
        "v_exp_f32 v101, v101\n"
        "v_cvt_pk_bf16_f32 v113, v98, v99\n"
        "v_exp_f32 v102, v102\n"
        "v_mfma_f32_16x16x32_bf16 v[160:163], a[192:195], v[204:207], v[160:163]\n"
        "v_exp_f32 v103, v103\n"
        "v_cvt_pk_bf16_f32 v114, v100, v101\n"
        "v_mfma_f32_32x32x16_bf16 v[80:95], a[32:35], v[36:39], v[64:79]\n"
        "v_exp_f32 v104, v104\n"
        "v_exp_f32 v105, v105\n"
        "v_cvt_pk_bf16_f32 v115, v102, v103\n"
        "v_exp_f32 v106, v106\n"
        "v_mfma_f32_16x16x32_bf16 v[164:167], a[196:199], v[204:207], v[164:167]\n"
        "v_exp_f32 v107, v107\n"
        "v_mfma_f32_32x32x16_bf16 v[144:159], a[120:123], v[112:115], v[144:159]\n"
        "v_cvt_pk_bf16_f32 v116, v104, v105\n"
        "v_exp_f32 v108, v108\n"
        "v_exp_f32 v109, v109\n"
        "v_cvt_pk_bf16_f32 v117, v106, v107\n"
        "v_exp_f32 v110, v110\n"
        "v_mfma_f32_16x16x32_bf16 v[168:171], a[200:203], v[204:207], v[168:171]\n"
        "v_exp_f32 v111, v111\n"
        "v_cvt_pk_bf16_f32 v118, v108, v109\n"
        "v_exp_f32 v80, v80\n"
        "v_exp_f32 v81, v81\n"
        "v_cvt_pk_bf16_f32 v119, v110, v111\n"
        "v_exp_f32 v82, v82\n"
        "v_exp_f32 v83, v83\n"
        "v_mfma_f32_32x32x16_bf16 v[144:159], a[124:127], v[116:119], v[144:159]\n"
        "buffer_load_dwordx4 a[192:195], v1, s[16:19], s36 offen offset:0\n"
        "v_cvt_pk_bf16_f32 v112, v80, v81\n"
        "v_exp_f32 v84, v84\n"
        "v_exp_f32 v85, v85\n"
        "v_cvt_pk_bf16_f32 v113, v82, v83\n"
        "v_exp_f32 v86, v86\n"
        "v_mfma_f32_16x16x32_bf16 v[172:175], a[204:207], v[204:207], v[172:175]\n"
        "v_exp_f32 v87, v87\n"
        "v_cvt_pk_bf16_f32 v114, v84, v85\n"
        "v_mfma_f32_32x32x16_bf16 v[96:111], a[36:39], v[36:39], v[64:79]\n"
        "buffer_load_dwordx4 a[196:199], v1, s[16:19], s36 offen offset:1024\n"
        "v_exp_f32 v88, v88\n"
        "v_exp_f32 v89, v89\n"
        "v_cvt_pk_bf16_f32 v115, v86, v87\n"
        "v_exp_f32 v90, v90\n"
        "v_mfma_f32_16x16x32_bf16 v[176:179], a[208:211], v[204:207], v[176:179]\n"
        "v_exp_f32 v91, v91\n"
        "v_mfma_f32_32x32x16_bf16 v[144:159], a[128:131], v[112:115], v[144:159]\n"
        "buffer_load_dwordx4 a[200:203], v1, s[16:19], s36 offen offset:2048\n"
        "v_cvt_pk_bf16_f32 v116, v88, v89\n"
        "v_exp_f32 v92, v92\n"
        "v_exp_f32 v93, v93\n"
        "v_cvt_pk_bf16_f32 v117, v90, v91\n"
        "v_exp_f32 v94, v94\n"
        "v_mfma_f32_16x16x32_bf16 v[180:183], a[212:215], v[204:207], v[180:183]\n"
        "v_exp_f32 v95, v95\n"
        "v_cvt_pk_bf16_f32 v118, v92, v93\n"
        "v_exp_f32 v96, v96\n"
        "v_exp_f32 v97, v97\n"
        "v_cvt_pk_bf16_f32 v119, v94, v95\n"
        "v_exp_f32 v98, v98\n"
        "v_exp_f32 v99, v99\n"
        "v_mfma_f32_32x32x16_bf16 v[144:159], a[132:135], v[116:119], v[144:159]\n"
        "buffer_load_dwordx4 a[204:207], v1, s[16:19], s36 offen offset:3072\n"
        "v_cvt_pk_bf16_f32 v112, v96, v97\n"
        "v_exp_f32 v100, v100\n"
        "v_exp_f32 v101, v101\n"
        "v_cvt_pk_bf16_f32 v113, v98, v99\n"
        "v_exp_f32 v102, v102\n"
        "v_mfma_f32_16x16x32_bf16 v[184:187], a[216:219], v[204:207], v[184:187]\n"
        "v_exp_f32 v103, v103\n"
        "v_cvt_pk_bf16_f32 v114, v100, v101\n"
        "v_mfma_f32_32x32x16_bf16 v[80:95], a[40:43], v[36:39], v[64:79]\n"
        "buffer_load_dwordx4 a[208:211], v24, s[16:19], s36 offen offset:0\n"
        "v_exp_f32 v104, v104\n"
        "v_exp_f32 v105, v105\n"
        "v_cvt_pk_bf16_f32 v115, v102, v103\n"
        "v_exp_f32 v106, v106\n"
        "v_mfma_f32_16x16x32_bf16 v[188:191], a[220:223], v[204:207], v[188:191]\n"
        "v_exp_f32 v107, v107\n"
        "v_mfma_f32_32x32x16_bf16 v[144:159], a[136:139], v[112:115], v[144:159]\n"
        "buffer_load_dwordx4 a[212:215], v24, s[16:19], s36 offen offset:1024\n"
        "v_cvt_pk_bf16_f32 v116, v104, v105\n"
        "v_exp_f32 v108, v108\n"
        "v_exp_f32 v109, v109\n"
        "v_cvt_pk_bf16_f32 v117, v106, v107\n"
        "v_exp_f32 v110, v110\n"
        "v_mfma_f32_16x16x32_bf16 v[160:163], a[224:227], v[208:211], v[160:163]\n"
        "v_exp_f32 v111, v111\n"
        "v_cvt_pk_bf16_f32 v118, v108, v109\n"
        "v_exp_f32 v80, v80\n"
        "v_exp_f32 v81, v81\n"
        "v_cvt_pk_bf16_f32 v119, v110, v111\n"
        "v_exp_f32 v82, v82\n"
        "v_exp_f32 v83, v83\n"
        "v_mfma_f32_32x32x16_bf16 v[144:159], a[140:143], v[116:119], v[144:159]\n"
        "buffer_load_dwordx4 a[216:219], v24, s[16:19], s36 offen offset:2048\n"
        "v_cvt_pk_bf16_f32 v112, v80, v81\n"
        "v_exp_f32 v84, v84\n"
        "v_exp_f32 v85, v85\n"
        "v_cvt_pk_bf16_f32 v113, v82, v83\n"
        "v_exp_f32 v86, v86\n"
        "v_mfma_f32_16x16x32_bf16 v[164:167], a[228:231], v[208:211], v[164:167]\n"
        "v_exp_f32 v87, v87\n"
        "v_cvt_pk_bf16_f32 v114, v84, v85\n"
        "v_mfma_f32_32x32x16_bf16 v[96:111], a[44:47], v[36:39], v[64:79]\n"
        "buffer_load_dwordx4 a[220:223], v24, s[16:19], s36 offen offset:3072\n"
        "v_exp_f32 v88, v88\n"
        "v_exp_f32 v89, v89\n"
        "v_cvt_pk_bf16_f32 v115, v86, v87\n"
        "v_exp_f32 v90, v90\n"
        "v_mfma_f32_16x16x32_bf16 v[168:171], a[232:235], v[208:211], v[168:171]\n"
        "v_exp_f32 v91, v91\n"
        "v_mfma_f32_32x32x16_bf16 v[144:159], a[144:147], v[112:115], v[144:159]\n"
        "buffer_load_dwordx4 a[224:227], v25, s[16:19], s36 offen offset:0\n"
        "v_cvt_pk_bf16_f32 v116, v88, v89\n"
        "v_exp_f32 v92, v92\n"
        "v_exp_f32 v93, v93\n"
        "v_cvt_pk_bf16_f32 v117, v90, v91\n"
        "v_exp_f32 v94, v94\n"
        "v_mfma_f32_16x16x32_bf16 v[172:175], a[236:239], v[208:211], v[172:175]\n"
        "v_exp_f32 v95, v95\n"
        "v_cvt_pk_bf16_f32 v118, v92, v93\n"
        "v_exp_f32 v96, v96\n"
        "v_exp_f32 v97, v97\n"
        "v_cvt_pk_bf16_f32 v119, v94, v95\n"
        "v_exp_f32 v98, v98\n"
        "v_exp_f32 v99, v99\n"
        "v_mfma_f32_32x32x16_bf16 v[144:159], a[148:151], v[116:119], v[144:159]\n"
        "buffer_load_dwordx4 a[228:231], v25, s[16:19], s36 offen offset:1024\n"
        "v_cvt_pk_bf16_f32 v112, v96, v97\n"
        "v_exp_f32 v100, v100\n"
        "v_exp_f32 v101, v101\n"
        "v_cvt_pk_bf16_f32 v113, v98, v99\n"
        "v_exp_f32 v102, v102\n"
        "v_mfma_f32_16x16x32_bf16 v[176:179], a[240:243], v[208:211], v[176:179]\n"
        "v_exp_f32 v103, v103\n"
        "v_cvt_pk_bf16_f32 v114, v100, v101\n"
        "v_mfma_f32_32x32x16_bf16 v[80:95], a[48:51], v[36:39], v[64:79]\n"
        "buffer_load_dwordx4 a[232:235], v25, s[16:19], s36 offen offset:2048\n"
        "v_exp_f32 v104, v104\n"
        "v_exp_f32 v105, v105\n"
        "v_cvt_pk_bf16_f32 v115, v102, v103\n"
        "v_exp_f32 v106, v106\n"
        "v_mfma_f32_16x16x32_bf16 v[180:183], a[244:247], v[208:211], v[180:183]\n"
        "v_exp_f32 v107, v107\n"
        "v_mfma_f32_32x32x16_bf16 v[144:159], a[152:155], v[112:115], v[144:159]\n"
        "buffer_load_dwordx4 a[236:239], v25, s[16:19], s36 offen offset:3072\n"
        "v_cvt_pk_bf16_f32 v116, v104, v105\n"
        "v_exp_f32 v108, v108\n"
        "v_exp_f32 v109, v109\n"
        "v_cvt_pk_bf16_f32 v117, v106, v107\n"
        "v_exp_f32 v110, v110\n"
        "v_mfma_f32_16x16x32_bf16 v[184:187], a[248:251], v[208:211], v[184:187]\n"
        "v_exp_f32 v111, v111\n"
        "v_cvt_pk_bf16_f32 v118, v108, v109\n"
        "v_exp_f32 v80, v80\n"
        "v_exp_f32 v81, v81\n"
        "v_cvt_pk_bf16_f32 v119, v110, v111\n"
        "v_exp_f32 v82, v82\n"
        "v_exp_f32 v83, v83\n"
        "v_mfma_f32_32x32x16_bf16 v[144:159], a[156:159], v[116:119], v[144:159]\n"
        "buffer_load_dwordx4 a[240:243], v26, s[16:19], s36 offen offset:0\n"
        "v_cvt_pk_bf16_f32 v112, v80, v81\n"
        "v_exp_f32 v84, v84\n"
        "v_exp_f32 v85, v85\n"
        "v_cvt_pk_bf16_f32 v113, v82, v83\n"
        "v_exp_f32 v86, v86\n"
        "v_mfma_f32_16x16x32_bf16 v[188:191], a[252:255], v[208:211], v[188:191]\n"
        "v_exp_f32 v87, v87\n"
        "v_cvt_pk_bf16_f32 v114, v84, v85\n"
        "v_mfma_f32_32x32x16_bf16 v[96:111], a[52:55], v[36:39], v[64:79]\n"
        "buffer_load_dwordx4 a[244:247], v26, s[16:19], s36 offen offset:1024\n"
        "v_exp_f32 v88, v88\n"
        "v_exp_f32 v89, v89\n"
        "v_cvt_pk_bf16_f32 v115, v86, v87\n"
        "v_exp_f32 v90, v90\n"
        "v_exp_f32 v91, v91\n"
        "v_mfma_f32_32x32x16_bf16 v[144:159], a[160:163], v[112:115], v[144:159]\n"
        "buffer_load_dwordx4 a[248:251], v26, s[16:19], s36 offen offset:2048\n"
        "v_cvt_pk_bf16_f32 v116, v88, v89\n"
        "v_exp_f32 v92, v92\n"
        "v_exp_f32 v93, v93\n"
        "v_cvt_pk_bf16_f32 v117, v90, v91\n"
        "v_exp_f32 v94, v94\n"
        "v_exp_f32 v95, v95\n"
        "v_cvt_pk_bf16_f32 v118, v92, v93\n"
        "v_exp_f32 v96, v96\n"
        "v_exp_f32 v97, v97\n"
        "v_cvt_pk_bf16_f32 v119, v94, v95\n"
        "v_exp_f32 v98, v98\n"
        "v_exp_f32 v99, v99\n"
        "v_mfma_f32_32x32x16_bf16 v[144:159], a[164:167], v[116:119], v[144:159]\n"
        "buffer_load_dwordx4 a[252:255], v26, s[16:19], s36 offen offset:3072\n"
        "v_cvt_pk_bf16_f32 v112, v96, v97\n"
        "v_exp_f32 v100, v100\n"
        "v_exp_f32 v101, v101\n"
        "v_cvt_pk_bf16_f32 v113, v98, v99\n"
        "v_exp_f32 v102, v102\n"
        "v_exp_f32 v103, v103\n"
        "v_cvt_pk_bf16_f32 v114, v100, v101\n"
        "v_mfma_f32_32x32x16_bf16 v[80:95], a[56:59], v[36:39], v[64:79]\n"
        "buffer_load_dwordx4 v[40:43], v1, s[16:19], s60 offen offset:0\n"
        "v_exp_f32 v104, v104\n"
        "v_exp_f32 v105, v105\n"
        "v_cvt_pk_bf16_f32 v115, v102, v103\n"
        "v_exp_f32 v106, v106\n"
        "v_exp_f32 v107, v107\n"
        "v_mfma_f32_32x32x16_bf16 v[144:159], a[168:171], v[112:115], v[144:159]\n"
        "buffer_load_dwordx4 v[44:47], v1, s[16:19], s61 offen offset:0\n"
        "v_cvt_pk_bf16_f32 v116, v104, v105\n"
        "v_exp_f32 v108, v108\n"
        "v_exp_f32 v109, v109\n"
        "v_cvt_pk_bf16_f32 v117, v106, v107\n"
        "v_exp_f32 v110, v110\n"
        "v_exp_f32 v111, v111\n"
        "v_cvt_pk_bf16_f32 v118, v108, v109\n"
        "v_exp_f32 v80, v80\n"
        "v_exp_f32 v81, v81\n"
        "v_cvt_pk_bf16_f32 v119, v110, v111\n"
        "v_exp_f32 v82, v82\n"
        "v_exp_f32 v83, v83\n"
        "v_mfma_f32_32x32x16_bf16 v[144:159], a[172:175], v[116:119], v[144:159]\n"
        "s_add_u32 m0, s41, 0x7000\n"
        "s_add_u32 s42, m0, s43\n"
        "buffer_load_dwordx4 v1, s[16:19], s42 offen offset:0 lds\n"
        "v_cvt_pk_bf16_f32 v112, v80, v81\n"
        "v_exp_f32 v84, v84\n"
        "v_exp_f32 v85, v85\n"
        "v_cvt_pk_bf16_f32 v113, v82, v83\n"
        "v_exp_f32 v86, v86\n"
        "v_exp_f32 v87, v87\n"
        "v_cvt_pk_bf16_f32 v114, v84, v85\n"
        "v_mfma_f32_32x32x16_bf16 v[96:111], a[60:63], v[36:39], v[64:79]\n"
        "buffer_load_dwordx4 v[48:51], v1, s[16:19], s60 offen offset:1024\n"
        "v_exp_f32 v88, v88\n"
        "v_exp_f32 v89, v89\n"
        "v_cvt_pk_bf16_f32 v115, v86, v87\n"
        "v_exp_f32 v90, v90\n"
        "v_exp_f32 v91, v91\n"
        "v_mfma_f32_32x32x16_bf16 v[144:159], a[176:179], v[112:115], v[144:159]\n"
        "buffer_load_dwordx4 v[52:55], v1, s[16:19], s61 offen offset:1024\n"
        "v_cvt_pk_bf16_f32 v116, v88, v89\n"
        "v_exp_f32 v92, v92\n"
        "v_exp_f32 v93, v93\n"
        "v_cvt_pk_bf16_f32 v117, v90, v91\n"
        "v_exp_f32 v94, v94\n"
        "v_exp_f32 v95, v95\n"
        "v_cvt_pk_bf16_f32 v118, v92, v93\n"
        "v_exp_f32 v96, v96\n"
        "v_exp_f32 v97, v97\n"
        "v_cvt_pk_bf16_f32 v119, v94, v95\n"
        "v_exp_f32 v98, v98\n"
        "v_exp_f32 v99, v99\n"
        "v_mfma_f32_32x32x16_bf16 v[144:159], a[180:183], v[116:119], v[144:159]\n"
        "buffer_load_dwordx4 v1, s[16:19], s42 offen offset:1024 lds\n"
        "v_cvt_pk_bf16_f32 v112, v96, v97\n"
        "v_exp_f32 v100, v100\n"
        "v_exp_f32 v101, v101\n"
        "v_cvt_pk_bf16_f32 v113, v98, v99\n"
        "v_exp_f32 v102, v102\n"
        "v_exp_f32 v103, v103\n"
        "v_cvt_pk_bf16_f32 v114, v100, v101\n"
        "s_waitcnt vmcnt(24)\n"
        "v_mfma_f32_32x32x16_bf16 v[80:95], a[0:3], v[32:35], v[64:79]\n"
        "buffer_load_dwordx4 v[56:59], v1, s[16:19], s60 offen offset:2048\n"
        "v_exp_f32 v104, v104\n"
        "v_exp_f32 v105, v105\n"
        "v_cvt_pk_bf16_f32 v115, v102, v103\n"
        "v_exp_f32 v106, v106\n"
        "v_exp_f32 v107, v107\n"
        "v_mfma_f32_32x32x16_bf16 v[144:159], a[184:187], v[112:115], v[144:159]\n"
        "v_cvt_pk_bf16_f32 v116, v104, v105\n"
        "v_exp_f32 v108, v108\n"
        "v_exp_f32 v109, v109\n"
        "v_cvt_pk_bf16_f32 v117, v106, v107\n"
        "v_exp_f32 v110, v110\n"
        "v_exp_f32 v111, v111\n"
        "v_cvt_pk_bf16_f32 v118, v108, v109\n"
        "v_exp_f32 v80, v80\n"
        "v_exp_f32 v81, v81\n"
        "v_cvt_pk_bf16_f32 v119, v110, v111\n"
        "v_exp_f32 v82, v82\n"
        "v_exp_f32 v83, v83\n"
        "v_mfma_f32_32x32x16_bf16 v[144:159], a[188:191], v[116:119], v[144:159]\n"
        "buffer_load_dwordx4 v1, s[16:19], s42 offen offset:2048 lds\n"
        "v_cvt_pk_bf16_f32 v112, v80, v81\n"
        "v_exp_f32 v84, v84\n"
        "v_exp_f32 v85, v85\n"
        "v_cvt_pk_bf16_f32 v113, v82, v83\n"
        "v_exp_f32 v86, v86\n"
        "v_exp_f32 v87, v87\n"
        "v_cvt_pk_bf16_f32 v114, v84, v85\n"
        "v_mfma_f32_32x32x16_bf16 v[96:111], a[4:7], v[32:35], v[64:79]\n"
        "v_exp_f32 v88, v88\n"
        "v_exp_f32 v89, v89\n"
        "v_cvt_pk_bf16_f32 v115, v86, v87\n"
        "v_exp_f32 v90, v90\n"
        "v_exp_f32 v91, v91\n"
        "v_mfma_f32_32x32x16_bf16 v[128:143], a[64:67], v[112:115], 0\n"
        "buffer_load_dwordx4 v1, s[16:19], s42 offen offset:3072 lds\n"
        "v_cvt_pk_bf16_f32 v116, v88, v89\n"
        "v_exp_f32 v92, v92\n"
        "v_exp_f32 v93, v93\n"
        "v_cvt_pk_bf16_f32 v117, v90, v91\n"
        "v_exp_f32 v94, v94\n"
        "v_exp_f32 v95, v95\n"
        "v_cvt_pk_bf16_f32 v118, v92, v93\n"
        "v_exp_f32 v96, v96\n"
        "v_exp_f32 v97, v97\n"
        "v_cvt_pk_bf16_f32 v119, v94, v95\n"
        "v_exp_f32 v98, v98\n"
        "v_exp_f32 v99, v99\n"
        "v_mfma_f32_32x32x16_bf16 v[128:143], a[68:71], v[116:119], v[128:143]\n"
        "ds_write_b96 v5, v[148:150]\n"
        "v_cvt_pk_bf16_f32 v112, v96, v97\n"
        "v_exp_f32 v100, v100\n"
        "v_exp_f32 v101, v101\n"
        "v_cvt_pk_bf16_f32 v113, v98, v99\n"
        "v_exp_f32 v102, v102\n"
        "v_exp_f32 v103, v103\n"
        "v_cvt_pk_bf16_f32 v114, v100, v101\n"
        "v_mfma_f32_32x32x16_bf16 v[80:95], a[8:11], v[32:35], v[64:79]\n"
        "ds_write_b96 v6, v[152:154]\n"
        "v_exp_f32 v104, v104\n"
        "v_exp_f32 v105, v105\n"
        "v_cvt_pk_bf16_f32 v115, v102, v103\n"
        "v_exp_f32 v106, v106\n"
        "v_exp_f32 v107, v107\n"
        "v_mfma_f32_32x32x16_bf16 v[128:143], a[72:75], v[112:115], v[128:143]\n"
        "ds_write_b96 v7, v[156:158]\n"
        "v_cvt_pk_bf16_f32 v116, v104, v105\n"
        "v_exp_f32 v108, v108\n"
        "v_exp_f32 v109, v109\n"
        "v_cvt_pk_bf16_f32 v117, v106, v107\n"
        "v_exp_f32 v110, v110\n"
        "v_exp_f32 v111, v111\n"
        "v_cvt_pk_bf16_f32 v118, v108, v109\n"
        "v_exp_f32 v80, v80\n"
        "v_exp_f32 v81, v81\n"
        "v_cvt_pk_bf16_f32 v119, v110, v111\n"
        "v_exp_f32 v82, v82\n"
        "v_exp_f32 v83, v83\n"
        "v_mfma_f32_32x32x16_bf16 v[128:143], a[76:79], v[116:119], v[128:143]\n"
        "s_waitcnt lgkmcnt(0)\n"
        "s_barrier\n"
        "v_cvt_pk_bf16_f32 v112, v80, v81\n"
        "v_exp_f32 v84, v84\n"
        "v_exp_f32 v85, v85\n"
        "v_cvt_pk_bf16_f32 v113, v82, v83\n"
        "v_exp_f32 v86, v86\n"
        "v_exp_f32 v87, v87\n"
        "v_cvt_pk_bf16_f32 v114, v84, v85\n"
        "v_mfma_f32_32x32x16_bf16 v[96:111], a[12:15], v[32:35], v[64:79]\n"
        "ds_read_b128 v[192:195], v8\n"
        "v_exp_f32 v88, v88\n"
        "v_exp_f32 v89, v89\n"
        "v_cvt_pk_bf16_f32 v115, v86, v87\n"
        "v_exp_f32 v90, v90\n"
        "v_exp_f32 v91, v91\n"
        "v_mfma_f32_32x32x16_bf16 v[128:143], a[80:83], v[112:115], v[128:143]\n"
        "ds_read_b128 v[196:199], v8 offset:1024\n"
        "v_cvt_pk_bf16_f32 v116, v88, v89\n"
        "v_exp_f32 v92, v92\n"
        "v_exp_f32 v93, v93\n"
        "v_cvt_pk_bf16_f32 v117, v90, v91\n"
        "v_exp_f32 v94, v94\n"
        "v_exp_f32 v95, v95\n"
        "v_cvt_pk_bf16_f32 v118, v92, v93\n"
        "v_exp_f32 v96, v96\n"
        "v_exp_f32 v97, v97\n"
        "v_cvt_pk_bf16_f32 v119, v94, v95\n"
        "v_exp_f32 v98, v98\n"
        "v_exp_f32 v99, v99\n"
        "v_mfma_f32_32x32x16_bf16 v[128:143], a[84:87], v[116:119], v[128:143]\n"
        "ds_read_b128 v[200:203], v8 offset:2048\n"
        "v_cvt_pk_bf16_f32 v112, v96, v97\n"
        "v_exp_f32 v100, v100\n"
        "v_exp_f32 v101, v101\n"
        "v_cvt_pk_bf16_f32 v113, v98, v99\n"
        "v_exp_f32 v102, v102\n"
        "v_exp_f32 v103, v103\n"
        "v_cvt_pk_bf16_f32 v114, v100, v101\n"
        "v_mfma_f32_32x32x16_bf16 v[80:95], a[16:19], v[32:35], v[64:79]\n"
        "buffer_load_dwordx4 v[36:39], v1, s[16:19], s58 offen offset:3072\n"
        "v_exp_f32 v104, v104\n"
        "v_exp_f32 v105, v105\n"
        "v_cvt_pk_bf16_f32 v115, v102, v103\n"
        "v_exp_f32 v106, v106\n"
        "v_exp_f32 v107, v107\n"
        "v_mfma_f32_32x32x16_bf16 v[128:143], a[88:91], v[112:115], v[128:143]\n"
        "s_waitcnt lgkmcnt(0)\n"
        "v_cvt_pk_bf16_f32 v116, v104, v105\n"
        "v_exp_f32 v108, v108\n"
        "v_exp_f32 v109, v109\n"
        "v_cvt_pk_bf16_f32 v117, v106, v107\n"
        "v_exp_f32 v110, v110\n"
        "v_add_f32 v212, v144, v192\n"
        "v_add_f32 v213, v145, v193\n"
        "v_add_f32 v214, v146, v194\n"
        "v_exp_f32 v111, v111\n"
        "v_cvt_pk_bf16_f32 v118, v108, v109\n"
        "v_exp_f32 v80, v80\n"
        "v_exp_f32 v81, v81\n"
        "v_cvt_pk_bf16_f32 v119, v110, v111\n"
        "v_exp_f32 v82, v82\n"
        "v_exp_f32 v83, v83\n"
        "v_mfma_f32_32x32x16_bf16 v[128:143], a[92:95], v[116:119], v[128:143]\n"
        "s_add_u32 m0, s41, 0x8000\n"
        "s_add_u32 s42, m0, s43\n"
        "buffer_load_dwordx4 v1, s[16:19], s42 offen offset:0 lds\n"
        "v_cvt_pk_bf16_f32 v112, v80, v81\n"
        "v_exp_f32 v84, v84\n"
        "v_exp_f32 v85, v85\n"
        "v_cvt_pk_bf16_f32 v113, v82, v83\n"
        "v_exp_f32 v86, v86\n"
        "v_add_f32 v196, v196, v200\n"
        "v_add_f32 v197, v197, v201\n"
        "v_add_f32 v198, v198, v202\n"
        "v_exp_f32 v87, v87\n"
        "v_cvt_pk_bf16_f32 v114, v84, v85\n"
        "v_mfma_f32_32x32x16_bf16 v[96:111], a[20:23], v[32:35], v[64:79]\n"
        "v_add_f32 v212, v212, v196\n"
        "v_add_f32 v213, v213, v197\n"
        "v_add_f32 v214, v214, v198\n"
        "v_exp_f32 v88, v88\n"
        "v_exp_f32 v89, v89\n"
        "v_cvt_pk_bf16_f32 v115, v86, v87\n"
        "v_exp_f32 v90, v90\n"
        "v_exp_f32 v91, v91\n"
        "v_mfma_f32_32x32x16_bf16 v[128:143], a[96:99], v[112:115], v[128:143]\n"
        "v_rcp_f32 v214, v214\n"
        "v_cvt_pk_bf16_f32 v116, v88, v89\n"
        "v_exp_f32 v92, v92\n"
        "v_exp_f32 v93, v93\n"
        "v_cvt_pk_bf16_f32 v117, v90, v91\n"
        "v_exp_f32 v94, v94\n"
        "v_mul_f32 v212, v212, v214\n"
        "v_mul_f32 v213, v213, v214\n"
        "v_exp_f32 v95, v95\n"
        "v_cvt_pk_bf16_f32 v118, v92, v93\n"
        "v_exp_f32 v96, v96\n"
        "v_exp_f32 v97, v97\n"
        "v_cvt_pk_bf16_f32 v119, v94, v95\n"
        "v_exp_f32 v98, v98\n"
        "v_exp_f32 v99, v99\n"
        "v_mfma_f32_32x32x16_bf16 v[128:143], a[100:103], v[116:119], v[128:143]\n"
        "v_cvt_pk_bf16_f32 v212, v212, v213\n"
        "v_cvt_pk_bf16_f32 v112, v96, v97\n"
        "v_exp_f32 v100, v100\n"
        "v_exp_f32 v101, v101\n"
        "v_cvt_pk_bf16_f32 v113, v98, v99\n"
        "v_exp_f32 v102, v102\n"
        "v_exp_f32 v103, v103\n"
        "v_cvt_pk_bf16_f32 v114, v100, v101\n"
        "v_mfma_f32_32x32x16_bf16 v[80:95], a[24:27], v[32:35], v[64:79]\n"
        "ds_write_b32 v9, v212\n"
        "v_exp_f32 v104, v104\n"
        "v_exp_f32 v105, v105\n"
        "v_cvt_pk_bf16_f32 v115, v102, v103\n"
        "v_exp_f32 v106, v106\n"
        "v_exp_f32 v107, v107\n"
        "v_mfma_f32_32x32x16_bf16 v[128:143], a[104:107], v[112:115], v[128:143]\n"
        "buffer_load_dwordx4 v1, s[16:19], s42 offen offset:1024 lds\n"
        "v_cvt_pk_bf16_f32 v116, v104, v105\n"
        "v_exp_f32 v108, v108\n"
        "v_exp_f32 v109, v109\n"
        "v_cvt_pk_bf16_f32 v117, v106, v107\n"
        "v_exp_f32 v110, v110\n"
        "v_exp_f32 v111, v111\n"
        "v_cvt_pk_bf16_f32 v118, v108, v109\n"
        "v_exp_f32 v80, v80\n"
        "v_exp_f32 v81, v81\n"
        "v_cvt_pk_bf16_f32 v119, v110, v111\n"
        "v_exp_f32 v82, v82\n"
        "v_exp_f32 v83, v83\n"
        "v_mfma_f32_32x32x16_bf16 v[128:143], a[108:111], v[116:119], v[128:143]\n"
        "s_waitcnt lgkmcnt(0)\n"
        "s_barrier\n"
        "s_add_u32 s36, s35, 0x18000\n"
        "v_cvt_pk_bf16_f32 v112, v80, v81\n"
        "v_exp_f32 v84, v84\n"
        "v_exp_f32 v85, v85\n"
        "v_cvt_pk_bf16_f32 v113, v82, v83\n"
        "v_exp_f32 v86, v86\n"
        "v_exp_f32 v87, v87\n"
        "v_cvt_pk_bf16_f32 v114, v84, v85\n"
        "v_mfma_f32_32x32x16_bf16 v[96:111], a[28:31], v[32:35], v[64:79]\n"
        "ds_read_b128 v[204:207], v10\n"
        "v_exp_f32 v88, v88\n"
        "v_exp_f32 v89, v89\n"
        "v_cvt_pk_bf16_f32 v115, v86, v87\n"
        "v_exp_f32 v90, v90\n"
        "v_exp_f32 v91, v91\n"
        "v_mfma_f32_32x32x16_bf16 v[128:143], a[112:115], v[112:115], v[128:143]\n"
        "ds_read_b128 v[208:211], v10 offset:512\n"
        "v_cvt_pk_bf16_f32 v116, v88, v89\n"
        "v_exp_f32 v92, v92\n"
        "v_exp_f32 v93, v93\n"
        "v_cvt_pk_bf16_f32 v117, v90, v91\n"
        "v_exp_f32 v94, v94\n"
        "v_exp_f32 v95, v95\n"
        "v_cvt_pk_bf16_f32 v118, v92, v93\n"
        "v_exp_f32 v96, v96\n"
        "v_exp_f32 v97, v97\n"
        "v_cvt_pk_bf16_f32 v119, v94, v95\n"
        "v_exp_f32 v98, v98\n"
        "v_exp_f32 v99, v99\n"
        "v_mfma_f32_32x32x16_bf16 v[128:143], a[116:119], v[116:119], v[128:143]\n"
        "s_waitcnt vmcnt(12) lgkmcnt(0)\n"
        "v_cvt_pk_bf16_f32 v112, v96, v97\n"
        "v_exp_f32 v100, v100\n"
        "v_exp_f32 v101, v101\n"
        "v_cvt_pk_bf16_f32 v113, v98, v99\n"
        "v_exp_f32 v102, v102\n"
        "v_mfma_f32_16x16x32_bf16 v[160:163], a[192:195], v[204:207], v[160:163]\n"
        "v_exp_f32 v103, v103\n"
        "v_cvt_pk_bf16_f32 v114, v100, v101\n"
        "v_mfma_f32_32x32x16_bf16 v[80:95], a[32:35], v[32:35], v[64:79]\n"
        "v_exp_f32 v104, v104\n"
        "v_exp_f32 v105, v105\n"
        "v_cvt_pk_bf16_f32 v115, v102, v103\n"
        "v_exp_f32 v106, v106\n"
        "v_mfma_f32_16x16x32_bf16 v[164:167], a[196:199], v[204:207], v[164:167]\n"
        "v_exp_f32 v107, v107\n"
        "v_mfma_f32_32x32x16_bf16 v[128:143], a[120:123], v[112:115], v[128:143]\n"
        "v_cvt_pk_bf16_f32 v116, v104, v105\n"
        "v_exp_f32 v108, v108\n"
        "v_exp_f32 v109, v109\n"
        "v_cvt_pk_bf16_f32 v117, v106, v107\n"
        "v_exp_f32 v110, v110\n"
        "v_mfma_f32_16x16x32_bf16 v[168:171], a[200:203], v[204:207], v[168:171]\n"
        "v_exp_f32 v111, v111\n"
        "v_cvt_pk_bf16_f32 v118, v108, v109\n"
        "v_exp_f32 v80, v80\n"
        "v_exp_f32 v81, v81\n"
        "v_cvt_pk_bf16_f32 v119, v110, v111\n"
        "v_exp_f32 v82, v82\n"
        "v_exp_f32 v83, v83\n"
        "v_mfma_f32_32x32x16_bf16 v[128:143], a[124:127], v[116:119], v[128:143]\n"
        "buffer_load_dwordx4 a[192:195], v1, s[16:19], s36 offen offset:0\n"
        "v_cvt_pk_bf16_f32 v112, v80, v81\n"
        "v_exp_f32 v84, v84\n"
        "v_exp_f32 v85, v85\n"
        "v_cvt_pk_bf16_f32 v113, v82, v83\n"
        "v_exp_f32 v86, v86\n"
        "v_mfma_f32_16x16x32_bf16 v[172:175], a[204:207], v[204:207], v[172:175]\n"
        "v_exp_f32 v87, v87\n"
        "v_cvt_pk_bf16_f32 v114, v84, v85\n"
        "v_mfma_f32_32x32x16_bf16 v[96:111], a[36:39], v[32:35], v[64:79]\n"
        "buffer_load_dwordx4 a[196:199], v1, s[16:19], s36 offen offset:1024\n"
        "v_exp_f32 v88, v88\n"
        "v_exp_f32 v89, v89\n"
        "v_cvt_pk_bf16_f32 v115, v86, v87\n"
        "v_exp_f32 v90, v90\n"
        "v_mfma_f32_16x16x32_bf16 v[176:179], a[208:211], v[204:207], v[176:179]\n"
        "v_exp_f32 v91, v91\n"
        "v_mfma_f32_32x32x16_bf16 v[128:143], a[128:131], v[112:115], v[128:143]\n"
        "buffer_load_dwordx4 a[200:203], v1, s[16:19], s36 offen offset:2048\n"
        "v_cvt_pk_bf16_f32 v116, v88, v89\n"
        "v_exp_f32 v92, v92\n"
        "v_exp_f32 v93, v93\n"
        "v_cvt_pk_bf16_f32 v117, v90, v91\n"
        "v_exp_f32 v94, v94\n"
        "v_mfma_f32_16x16x32_bf16 v[180:183], a[212:215], v[204:207], v[180:183]\n"
        "v_exp_f32 v95, v95\n"
        "v_cvt_pk_bf16_f32 v118, v92, v93\n"
        "v_exp_f32 v96, v96\n"
        "v_exp_f32 v97, v97\n"
        "v_cvt_pk_bf16_f32 v119, v94, v95\n"
        "v_exp_f32 v98, v98\n"
        "v_exp_f32 v99, v99\n"
        "v_mfma_f32_32x32x16_bf16 v[128:143], a[132:135], v[116:119], v[128:143]\n"
        "buffer_load_dwordx4 a[204:207], v1, s[16:19], s36 offen offset:3072\n"
        "v_cvt_pk_bf16_f32 v112, v96, v97\n"
        "v_exp_f32 v100, v100\n"
        "v_exp_f32 v101, v101\n"
        "v_cvt_pk_bf16_f32 v113, v98, v99\n"
        "v_exp_f32 v102, v102\n"
        "v_mfma_f32_16x16x32_bf16 v[184:187], a[216:219], v[204:207], v[184:187]\n"
        "v_exp_f32 v103, v103\n"
        "v_cvt_pk_bf16_f32 v114, v100, v101\n"
        "v_mfma_f32_32x32x16_bf16 v[80:95], a[40:43], v[32:35], v[64:79]\n"
        "buffer_load_dwordx4 a[208:211], v24, s[16:19], s36 offen offset:0\n"
        "v_exp_f32 v104, v104\n"
        "v_exp_f32 v105, v105\n"
        "v_cvt_pk_bf16_f32 v115, v102, v103\n"
        "v_exp_f32 v106, v106\n"
        "v_mfma_f32_16x16x32_bf16 v[188:191], a[220:223], v[204:207], v[188:191]\n"
        "v_exp_f32 v107, v107\n"
        "v_mfma_f32_32x32x16_bf16 v[128:143], a[136:139], v[112:115], v[128:143]\n"
        "buffer_load_dwordx4 a[212:215], v24, s[16:19], s36 offen offset:1024\n"
        "v_cvt_pk_bf16_f32 v116, v104, v105\n"
        "v_exp_f32 v108, v108\n"
        "v_exp_f32 v109, v109\n"
        "v_cvt_pk_bf16_f32 v117, v106, v107\n"
        "v_exp_f32 v110, v110\n"
        "v_mfma_f32_16x16x32_bf16 v[160:163], a[224:227], v[208:211], v[160:163]\n"
        "v_exp_f32 v111, v111\n"
        "v_cvt_pk_bf16_f32 v118, v108, v109\n"
        "v_exp_f32 v80, v80\n"
        "v_exp_f32 v81, v81\n"
        "v_cvt_pk_bf16_f32 v119, v110, v111\n"
        "v_exp_f32 v82, v82\n"
        "v_exp_f32 v83, v83\n"
        "v_mfma_f32_32x32x16_bf16 v[128:143], a[140:143], v[116:119], v[128:143]\n"
        "buffer_load_dwordx4 a[216:219], v24, s[16:19], s36 offen offset:2048\n"
        "v_cvt_pk_bf16_f32 v112, v80, v81\n"
        "v_exp_f32 v84, v84\n"
        "v_exp_f32 v85, v85\n"
        "v_cvt_pk_bf16_f32 v113, v82, v83\n"
        "v_exp_f32 v86, v86\n"
        "v_mfma_f32_16x16x32_bf16 v[164:167], a[228:231], v[208:211], v[164:167]\n"
        "v_exp_f32 v87, v87\n"
        "v_cvt_pk_bf16_f32 v114, v84, v85\n"
        "v_mfma_f32_32x32x16_bf16 v[96:111], a[44:47], v[32:35], v[64:79]\n"
        "buffer_load_dwordx4 a[220:223], v24, s[16:19], s36 offen offset:3072\n"
        "v_exp_f32 v88, v88\n"
        "v_exp_f32 v89, v89\n"
        "v_cvt_pk_bf16_f32 v115, v86, v87\n"
        "v_exp_f32 v90, v90\n"
        "v_mfma_f32_16x16x32_bf16 v[168:171], a[232:235], v[208:211], v[168:171]\n"
        "v_exp_f32 v91, v91\n"
        "v_mfma_f32_32x32x16_bf16 v[128:143], a[144:147], v[112:115], v[128:143]\n"
        "buffer_load_dwordx4 a[224:227], v25, s[16:19], s36 offen offset:0\n"
        "v_cvt_pk_bf16_f32 v116, v88, v89\n"
        "v_exp_f32 v92, v92\n"
        "v_exp_f32 v93, v93\n"
        "v_cvt_pk_bf16_f32 v117, v90, v91\n"
        "v_exp_f32 v94, v94\n"
        "v_mfma_f32_16x16x32_bf16 v[172:175], a[236:239], v[208:211], v[172:175]\n"
        "v_exp_f32 v95, v95\n"
        "v_cvt_pk_bf16_f32 v118, v92, v93\n"
        "v_exp_f32 v96, v96\n"
        "v_exp_f32 v97, v97\n"
        "v_cvt_pk_bf16_f32 v119, v94, v95\n"
        "v_exp_f32 v98, v98\n"
        "v_exp_f32 v99, v99\n"
        "v_mfma_f32_32x32x16_bf16 v[128:143], a[148:151], v[116:119], v[128:143]\n"
        "buffer_load_dwordx4 a[228:231], v25, s[16:19], s36 offen offset:1024\n"
        "v_cvt_pk_bf16_f32 v112, v96, v97\n"
        "v_exp_f32 v100, v100\n"
        "v_exp_f32 v101, v101\n"
        "v_cvt_pk_bf16_f32 v113, v98, v99\n"
        "v_exp_f32 v102, v102\n"
        "v_mfma_f32_16x16x32_bf16 v[176:179], a[240:243], v[208:211], v[176:179]\n"
        "v_exp_f32 v103, v103\n"
        "v_cvt_pk_bf16_f32 v114, v100, v101\n"
        "v_mfma_f32_32x32x16_bf16 v[80:95], a[48:51], v[32:35], v[64:79]\n"
        "buffer_load_dwordx4 a[232:235], v25, s[16:19], s36 offen offset:2048\n"
        "v_exp_f32 v104, v104\n"
        "v_exp_f32 v105, v105\n"
        "v_cvt_pk_bf16_f32 v115, v102, v103\n"
        "v_exp_f32 v106, v106\n"
        "v_mfma_f32_16x16x32_bf16 v[180:183], a[244:247], v[208:211], v[180:183]\n"
        "v_exp_f32 v107, v107\n"
        "v_mfma_f32_32x32x16_bf16 v[128:143], a[152:155], v[112:115], v[128:143]\n"
        "buffer_load_dwordx4 a[236:239], v25, s[16:19], s36 offen offset:3072\n"
        "v_cvt_pk_bf16_f32 v116, v104, v105\n"
        "v_exp_f32 v108, v108\n"
        "v_exp_f32 v109, v109\n"
        "v_cvt_pk_bf16_f32 v117, v106, v107\n"
        "v_exp_f32 v110, v110\n"
        "v_mfma_f32_16x16x32_bf16 v[184:187], a[248:251], v[208:211], v[184:187]\n"
        "v_exp_f32 v111, v111\n"
        "v_cvt_pk_bf16_f32 v118, v108, v109\n"
        "v_exp_f32 v80, v80\n"
        "v_exp_f32 v81, v81\n"
        "v_cvt_pk_bf16_f32 v119, v110, v111\n"
        "v_exp_f32 v82, v82\n"
        "v_exp_f32 v83, v83\n"
        "v_mfma_f32_32x32x16_bf16 v[128:143], a[156:159], v[116:119], v[128:143]\n"
        "buffer_load_dwordx4 a[240:243], v26, s[16:19], s36 offen offset:0\n"
        "v_cvt_pk_bf16_f32 v112, v80, v81\n"
        "v_exp_f32 v84, v84\n"
        "v_exp_f32 v85, v85\n"
        "v_cvt_pk_bf16_f32 v113, v82, v83\n"
        "v_exp_f32 v86, v86\n"
        "v_mfma_f32_16x16x32_bf16 v[188:191], a[252:255], v[208:211], v[188:191]\n"
        "v_exp_f32 v87, v87\n"
        "v_cvt_pk_bf16_f32 v114, v84, v85\n"
        "v_mfma_f32_32x32x16_bf16 v[96:111], a[52:55], v[32:35], v[64:79]\n"
        "buffer_load_dwordx4 a[244:247], v26, s[16:19], s36 offen offset:1024\n"
        "v_exp_f32 v88, v88\n"
        "v_exp_f32 v89, v89\n"
        "v_cvt_pk_bf16_f32 v115, v86, v87\n"
        "v_exp_f32 v90, v90\n"
        "v_exp_f32 v91, v91\n"
        "v_mfma_f32_32x32x16_bf16 v[128:143], a[160:163], v[112:115], v[128:143]\n"
        "buffer_load_dwordx4 a[248:251], v26, s[16:19], s36 offen offset:2048\n"
        "v_cvt_pk_bf16_f32 v116, v88, v89\n"
        "v_exp_f32 v92, v92\n"
        "v_exp_f32 v93, v93\n"
        "v_cvt_pk_bf16_f32 v117, v90, v91\n"
        "v_exp_f32 v94, v94\n"
        "v_exp_f32 v95, v95\n"
        "v_cvt_pk_bf16_f32 v118, v92, v93\n"
        "v_exp_f32 v96, v96\n"
        "v_exp_f32 v97, v97\n"
        "v_cvt_pk_bf16_f32 v119, v94, v95\n"
        "v_exp_f32 v98, v98\n"
        "v_exp_f32 v99, v99\n"
        "v_mfma_f32_32x32x16_bf16 v[128:143], a[164:167], v[116:119], v[128:143]\n"
        "buffer_load_dwordx4 a[252:255], v26, s[16:19], s36 offen offset:3072\n"
        "v_cvt_pk_bf16_f32 v112, v96, v97\n"
        "v_exp_f32 v100, v100\n"
        "v_exp_f32 v101, v101\n"
        "v_cvt_pk_bf16_f32 v113, v98, v99\n"
        "v_exp_f32 v102, v102\n"
        "v_exp_f32 v103, v103\n"
        "v_cvt_pk_bf16_f32 v114, v100, v101\n"
        "v_mfma_f32_32x32x16_bf16 v[80:95], a[56:59], v[32:35], v[64:79]\n"
        "buffer_load_dwordx4 v[60:63], v1, s[16:19], s61 offen offset:2048\n"
        "v_exp_f32 v104, v104\n"
        "v_exp_f32 v105, v105\n"
        "v_cvt_pk_bf16_f32 v115, v102, v103\n"
        "v_exp_f32 v106, v106\n"
        "v_exp_f32 v107, v107\n"
        "v_mfma_f32_32x32x16_bf16 v[128:143], a[168:171], v[112:115], v[128:143]\n"
        "buffer_load_dwordx4 v[120:123], v1, s[16:19], s60 offen offset:3072\n"
        "v_cvt_pk_bf16_f32 v116, v104, v105\n"
        "v_exp_f32 v108, v108\n"
        "v_exp_f32 v109, v109\n"
        "v_cvt_pk_bf16_f32 v117, v106, v107\n"
        "v_exp_f32 v110, v110\n"
        "v_exp_f32 v111, v111\n"
        "v_cvt_pk_bf16_f32 v118, v108, v109\n"
        "v_exp_f32 v80, v80\n"
        "v_exp_f32 v81, v81\n"
        "v_cvt_pk_bf16_f32 v119, v110, v111\n"
        "v_exp_f32 v82, v82\n"
        "v_exp_f32 v83, v83\n"
        "v_mfma_f32_32x32x16_bf16 v[128:143], a[172:175], v[116:119], v[128:143]\n"
        "buffer_load_dwordx4 v1, s[16:19], s42 offen offset:2048 lds\n"
        "v_cvt_pk_bf16_f32 v112, v80, v81\n"
        "v_exp_f32 v84, v84\n"
        "v_exp_f32 v85, v85\n"
        "v_cvt_pk_bf16_f32 v113, v82, v83\n"
        "v_exp_f32 v86, v86\n"
        "v_exp_f32 v87, v87\n"
        "v_cvt_pk_bf16_f32 v114, v84, v85\n"
        "v_mfma_f32_32x32x16_bf16 v[96:111], a[60:63], v[32:35], v[64:79]\n"
        "buffer_load_dwordx4 v[124:127], v1, s[16:19], s61 offen offset:3072\n"
        "v_exp_f32 v88, v88\n"
        "v_exp_f32 v89, v89\n"
        "v_cvt_pk_bf16_f32 v115, v86, v87\n"
        "v_exp_f32 v90, v90\n"
        "v_exp_f32 v91, v91\n"
        "v_mfma_f32_32x32x16_bf16 v[128:143], a[176:179], v[112:115], v[128:143]\n"
        "buffer_load_dwordx4 v[228:231], v24, s[16:19], s60 offen offset:0\n"
        "v_cvt_pk_bf16_f32 v116, v88, v89\n"
        "v_exp_f32 v92, v92\n"
        "v_exp_f32 v93, v93\n"
        "v_cvt_pk_bf16_f32 v117, v90, v91\n"
        "v_exp_f32 v94, v94\n"
        "v_exp_f32 v95, v95\n"
        "v_cvt_pk_bf16_f32 v118, v92, v93\n"
        "v_exp_f32 v96, v96\n"
        "v_exp_f32 v97, v97\n"
        "v_cvt_pk_bf16_f32 v119, v94, v95\n"
        "v_exp_f32 v98, v98\n"
        "v_exp_f32 v99, v99\n"
        "v_mfma_f32_32x32x16_bf16 v[128:143], a[180:183], v[116:119], v[128:143]\n"
        "buffer_load_dwordx4 v1, s[16:19], s42 offen offset:3072 lds\n"
        "v_cvt_pk_bf16_f32 v112, v96, v97\n"
        "v_exp_f32 v100, v100\n"
        "v_exp_f32 v101, v101\n"
        "v_cvt_pk_bf16_f32 v113, v98, v99\n"
        "v_exp_f32 v102, v102\n"
        "v_exp_f32 v103, v103\n"
        "v_cvt_pk_bf16_f32 v114, v100, v101\n"
        "s_waitcnt vmcnt(24)\n"
        "v_mfma_f32_32x32x16_bf16 v[80:95], a[0:3], v[36:39], v[64:79]\n"
        "buffer_load_dwordx4 v[232:235], v24, s[16:19], s61 offen offset:0\n"
        "v_exp_f32 v104, v104\n"
        "v_exp_f32 v105, v105\n"
        "v_cvt_pk_bf16_f32 v115, v102, v103\n"
        "v_exp_f32 v106, v106\n"
        "v_exp_f32 v107, v107\n"
        "v_mfma_f32_32x32x16_bf16 v[128:143], a[184:187], v[112:115], v[128:143]\n"
        "v_cvt_pk_bf16_f32 v116, v104, v105\n"
        "v_exp_f32 v108, v108\n"
        "v_exp_f32 v109, v109\n"
        "v_cvt_pk_bf16_f32 v117, v106, v107\n"
        "v_exp_f32 v110, v110\n"
        "v_exp_f32 v111, v111\n"
        "v_cvt_pk_bf16_f32 v118, v108, v109\n"
        "v_exp_f32 v80, v80\n"
        "v_exp_f32 v81, v81\n"
        "v_cvt_pk_bf16_f32 v119, v110, v111\n"
        "v_exp_f32 v82, v82\n"
        "v_exp_f32 v83, v83\n"
        "v_mfma_f32_32x32x16_bf16 v[128:143], a[188:191], v[116:119], v[128:143]\n"
        "v_cvt_pk_bf16_f32 v112, v80, v81\n"
        "v_exp_f32 v84, v84\n"
        "v_exp_f32 v85, v85\n"
        "v_cvt_pk_bf16_f32 v113, v82, v83\n"
        "v_exp_f32 v86, v86\n"
        "v_exp_f32 v87, v87\n"
        "v_cvt_pk_bf16_f32 v114, v84, v85\n"
        "v_mfma_f32_32x32x16_bf16 v[96:111], a[4:7], v[36:39], v[64:79]\n"
        "v_exp_f32 v88, v88\n"
        "v_exp_f32 v89, v89\n"
        "v_cvt_pk_bf16_f32 v115, v86, v87\n"
        "v_exp_f32 v90, v90\n"
        "v_exp_f32 v91, v91\n"
        "v_mfma_f32_32x32x16_bf16 v[144:159], a[64:67], v[112:115], 0\n"
        "v_cvt_pk_bf16_f32 v116, v88, v89\n"
        "v_exp_f32 v92, v92\n"
        "v_exp_f32 v93, v93\n"
        "v_cvt_pk_bf16_f32 v117, v90, v91\n"
        "v_exp_f32 v94, v94\n"
        "v_exp_f32 v95, v95\n"
        "v_cvt_pk_bf16_f32 v118, v92, v93\n"
        "v_exp_f32 v96, v96\n"
        "v_exp_f32 v97, v97\n"
        "v_cvt_pk_bf16_f32 v119, v94, v95\n"
        "v_exp_f32 v98, v98\n"
        "v_exp_f32 v99, v99\n"
        "v_mfma_f32_32x32x16_bf16 v[144:159], a[68:71], v[116:119], v[144:159]\n"
        "ds_write_b96 v5, v[132:134]\n"
        "v_cvt_pk_bf16_f32 v112, v96, v97\n"
        "v_exp_f32 v100, v100\n"
        "v_exp_f32 v101, v101\n"
        "v_cvt_pk_bf16_f32 v113, v98, v99\n"
        "v_exp_f32 v102, v102\n"
        "v_exp_f32 v103, v103\n"
        "v_cvt_pk_bf16_f32 v114, v100, v101\n"
        "v_mfma_f32_32x32x16_bf16 v[80:95], a[8:11], v[36:39], v[64:79]\n"
        "ds_write_b96 v6, v[136:138]\n"
        "v_exp_f32 v104, v104\n"
        "v_exp_f32 v105, v105\n"
        "v_cvt_pk_bf16_f32 v115, v102, v103\n"
        "v_exp_f32 v106, v106\n"
        "v_exp_f32 v107, v107\n"
        "v_mfma_f32_32x32x16_bf16 v[144:159], a[72:75], v[112:115], v[144:159]\n"
        "ds_write_b96 v7, v[140:142]\n"
        "v_cvt_pk_bf16_f32 v116, v104, v105\n"
        "v_exp_f32 v108, v108\n"
        "v_exp_f32 v109, v109\n"
        "v_cvt_pk_bf16_f32 v117, v106, v107\n"
        "v_exp_f32 v110, v110\n"
        "v_exp_f32 v111, v111\n"
        "v_cvt_pk_bf16_f32 v118, v108, v109\n"
        "v_exp_f32 v80, v80\n"
        "v_exp_f32 v81, v81\n"
        "v_cvt_pk_bf16_f32 v119, v110, v111\n"
        "v_exp_f32 v82, v82\n"
        "v_exp_f32 v83, v83\n"
        "v_mfma_f32_32x32x16_bf16 v[144:159], a[76:79], v[116:119], v[144:159]\n"
        "s_waitcnt lgkmcnt(0)\n"
        "s_barrier\n"
        "v_cvt_pk_bf16_f32 v112, v80, v81\n"
        "v_exp_f32 v84, v84\n"
        "v_exp_f32 v85, v85\n"
        "v_cvt_pk_bf16_f32 v113, v82, v83\n"
        "v_exp_f32 v86, v86\n"
        "v_exp_f32 v87, v87\n"
        "v_cvt_pk_bf16_f32 v114, v84, v85\n"
        "v_mfma_f32_32x32x16_bf16 v[96:111], a[12:15], v[36:39], v[64:79]\n"
        "ds_read_b128 v[192:195], v8\n"
        "v_exp_f32 v88, v88\n"
        "v_exp_f32 v89, v89\n"
        "v_cvt_pk_bf16_f32 v115, v86, v87\n"
        "v_exp_f32 v90, v90\n"
        "v_exp_f32 v91, v91\n"
        "v_mfma_f32_32x32x16_bf16 v[144:159], a[80:83], v[112:115], v[144:159]\n"
        "ds_read_b128 v[196:199], v8 offset:1024\n"
        "v_cvt_pk_bf16_f32 v116, v88, v89\n"
        "v_exp_f32 v92, v92\n"
        "v_exp_f32 v93, v93\n"
        "v_cvt_pk_bf16_f32 v117, v90, v91\n"
        "v_exp_f32 v94, v94\n"
        "v_exp_f32 v95, v95\n"
        "v_cvt_pk_bf16_f32 v118, v92, v93\n"
        "v_exp_f32 v96, v96\n"
        "v_exp_f32 v97, v97\n"
        "v_cvt_pk_bf16_f32 v119, v94, v95\n"
        "v_exp_f32 v98, v98\n"
        "v_exp_f32 v99, v99\n"
        "v_mfma_f32_32x32x16_bf16 v[144:159], a[84:87], v[116:119], v[144:159]\n"
        "ds_read_b128 v[200:203], v8 offset:2048\n"
        "v_cvt_pk_bf16_f32 v112, v96, v97\n"
        "v_exp_f32 v100, v100\n"
        "v_exp_f32 v101, v101\n"
        "v_cvt_pk_bf16_f32 v113, v98, v99\n"
        "v_exp_f32 v102, v102\n"
        "v_exp_f32 v103, v103\n"
        "v_cvt_pk_bf16_f32 v114, v100, v101\n"
        "v_mfma_f32_32x32x16_bf16 v[80:95], a[16:19], v[36:39], v[64:79]\n"
        "v_exp_f32 v104, v104\n"
        "v_exp_f32 v105, v105\n"
        "v_cvt_pk_bf16_f32 v115, v102, v103\n"
        "v_exp_f32 v106, v106\n"
        "v_exp_f32 v107, v107\n"
        "v_mfma_f32_32x32x16_bf16 v[144:159], a[88:91], v[112:115], v[144:159]\n"
        "s_waitcnt lgkmcnt(0)\n"
        "v_cvt_pk_bf16_f32 v116, v104, v105\n"
        "v_exp_f32 v108, v108\n"
        "v_exp_f32 v109, v109\n"
        "v_cvt_pk_bf16_f32 v117, v106, v107\n"
        "v_exp_f32 v110, v110\n"
        "v_add_f32 v212, v128, v192\n"
        "v_add_f32 v213, v129, v193\n"
        "v_add_f32 v214, v130, v194\n"
        "v_exp_f32 v111, v111\n"
        "v_cvt_pk_bf16_f32 v118, v108, v109\n"
        "v_exp_f32 v80, v80\n"
        "v_exp_f32 v81, v81\n"
        "v_cvt_pk_bf16_f32 v119, v110, v111\n"
        "v_exp_f32 v82, v82\n"
        "v_exp_f32 v83, v83\n"
        "v_mfma_f32_32x32x16_bf16 v[144:159], a[92:95], v[116:119], v[144:159]\n"
        "v_cvt_pk_bf16_f32 v112, v80, v81\n"
        "v_exp_f32 v84, v84\n"
        "v_exp_f32 v85, v85\n"
        "v_cvt_pk_bf16_f32 v113, v82, v83\n"
        "v_exp_f32 v86, v86\n"
        "v_add_f32 v196, v196, v200\n"
        "v_add_f32 v197, v197, v201\n"
        "v_add_f32 v198, v198, v202\n"
        "v_exp_f32 v87, v87\n"
        "v_cvt_pk_bf16_f32 v114, v84, v85\n"
        "v_mfma_f32_32x32x16_bf16 v[96:111], a[20:23], v[36:39], v[64:79]\n"
        "v_add_f32 v212, v212, v196\n"
        "v_add_f32 v213, v213, v197\n"
        "v_add_f32 v214, v214, v198\n"
        "v_exp_f32 v88, v88\n"
        "v_exp_f32 v89, v89\n"
        "v_cvt_pk_bf16_f32 v115, v86, v87\n"
        "v_exp_f32 v90, v90\n"
        "v_exp_f32 v91, v91\n"
        "v_mfma_f32_32x32x16_bf16 v[144:159], a[96:99], v[112:115], v[144:159]\n"
        "v_rcp_f32 v214, v214\n"
        "v_cvt_pk_bf16_f32 v116, v88, v89\n"
        "v_exp_f32 v92, v92\n"
        "v_exp_f32 v93, v93\n"
        "v_cvt_pk_bf16_f32 v117, v90, v91\n"
        "v_exp_f32 v94, v94\n"
        "v_mul_f32 v212, v212, v214\n"
        "v_mul_f32 v213, v213, v214\n"
        "v_exp_f32 v95, v95\n"
        "v_cvt_pk_bf16_f32 v118, v92, v93\n"
        "v_exp_f32 v96, v96\n"
        "v_exp_f32 v97, v97\n"
        "v_cvt_pk_bf16_f32 v119, v94, v95\n"
        "v_exp_f32 v98, v98\n"
        "v_exp_f32 v99, v99\n"
        "v_mfma_f32_32x32x16_bf16 v[144:159], a[100:103], v[116:119], v[144:159]\n"
        "v_cvt_pk_bf16_f32 v212, v212, v213\n"
        "v_cvt_pk_bf16_f32 v112, v96, v97\n"
        "v_exp_f32 v100, v100\n"
        "v_exp_f32 v101, v101\n"
        "v_cvt_pk_bf16_f32 v113, v98, v99\n"
        "v_exp_f32 v102, v102\n"
        "v_exp_f32 v103, v103\n"
        "v_cvt_pk_bf16_f32 v114, v100, v101\n"
        "v_mfma_f32_32x32x16_bf16 v[80:95], a[24:27], v[36:39], v[64:79]\n"
        "ds_write_b32 v9, v212\n"
        "v_exp_f32 v104, v104\n"
        "v_exp_f32 v105, v105\n"
        "v_cvt_pk_bf16_f32 v115, v102, v103\n"
        "v_exp_f32 v106, v106\n"
        "v_exp_f32 v107, v107\n"
        "v_mfma_f32_32x32x16_bf16 v[144:159], a[104:107], v[112:115], v[144:159]\n"
        "v_cvt_pk_bf16_f32 v116, v104, v105\n"
        "v_exp_f32 v108, v108\n"
        "v_exp_f32 v109, v109\n"
        "v_cvt_pk_bf16_f32 v117, v106, v107\n"
        "v_exp_f32 v110, v110\n"
        "v_exp_f32 v111, v111\n"
        "v_cvt_pk_bf16_f32 v118, v108, v109\n"
        "v_exp_f32 v80, v80\n"
        "v_exp_f32 v81, v81\n"
        "v_cvt_pk_bf16_f32 v119, v110, v111\n"
        "v_exp_f32 v82, v82\n"
        "v_exp_f32 v83, v83\n"
        "v_mfma_f32_32x32x16_bf16 v[144:159], a[108:111], v[116:119], v[144:159]\n"
        "s_waitcnt lgkmcnt(0)\n"
        "s_barrier\n"
        "s_add_u32 s36, s35, 0x1c000\n"
        "v_cvt_pk_bf16_f32 v112, v80, v81\n"
        "v_exp_f32 v84, v84\n"
        "v_exp_f32 v85, v85\n"
        "v_cvt_pk_bf16_f32 v113, v82, v83\n"
        "v_exp_f32 v86, v86\n"
        "v_exp_f32 v87, v87\n"
        "v_cvt_pk_bf16_f32 v114, v84, v85\n"
        "v_mfma_f32_32x32x16_bf16 v[96:111], a[28:31], v[36:39], v[64:79]\n"
        "ds_read_b128 v[204:207], v10\n"
        "v_exp_f32 v88, v88\n"
        "v_exp_f32 v89, v89\n"
        "v_cvt_pk_bf16_f32 v115, v86, v87\n"
        "v_exp_f32 v90, v90\n"
        "v_exp_f32 v91, v91\n"
        "v_mfma_f32_32x32x16_bf16 v[144:159], a[112:115], v[112:115], v[144:159]\n"
        "ds_read_b128 v[208:211], v10 offset:512\n"
        "v_cvt_pk_bf16_f32 v116, v88, v89\n"
        "v_exp_f32 v92, v92\n"
        "v_exp_f32 v93, v93\n"
        "v_cvt_pk_bf16_f32 v117, v90, v91\n"
        "v_exp_f32 v94, v94\n"
        "v_exp_f32 v95, v95\n"
        "v_cvt_pk_bf16_f32 v118, v92, v93\n"
        "v_exp_f32 v96, v96\n"
        "v_exp_f32 v97, v97\n"
        "v_cvt_pk_bf16_f32 v119, v94, v95\n"
        "v_exp_f32 v98, v98\n"
        "v_exp_f32 v99, v99\n"
        "v_mfma_f32_32x32x16_bf16 v[144:159], a[116:119], v[116:119], v[144:159]\n"
        "s_waitcnt vmcnt(7) lgkmcnt(0)\n"
        "v_cvt_pk_bf16_f32 v112, v96, v97\n"
        "v_exp_f32 v100, v100\n"
        "v_exp_f32 v101, v101\n"
        "v_cvt_pk_bf16_f32 v113, v98, v99\n"
        "v_exp_f32 v102, v102\n"
        "v_mfma_f32_16x16x32_bf16 v[160:163], a[192:195], v[204:207], v[160:163]\n"
        "v_exp_f32 v103, v103\n"
        "v_cvt_pk_bf16_f32 v114, v100, v101\n"
        "v_mfma_f32_32x32x16_bf16 v[80:95], a[32:35], v[36:39], v[64:79]\n"
        "v_exp_f32 v104, v104\n"
        "v_exp_f32 v105, v105\n"
        "v_cvt_pk_bf16_f32 v115, v102, v103\n"
        "v_exp_f32 v106, v106\n"
        "v_mfma_f32_16x16x32_bf16 v[164:167], a[196:199], v[204:207], v[164:167]\n"
        "v_exp_f32 v107, v107\n"
        "v_mfma_f32_32x32x16_bf16 v[144:159], a[120:123], v[112:115], v[144:159]\n"
        "v_cvt_pk_bf16_f32 v116, v104, v105\n"
        "v_exp_f32 v108, v108\n"
        "v_exp_f32 v109, v109\n"
        "v_cvt_pk_bf16_f32 v117, v106, v107\n"
        "v_exp_f32 v110, v110\n"
        "v_mfma_f32_16x16x32_bf16 v[168:171], a[200:203], v[204:207], v[168:171]\n"
        "v_exp_f32 v111, v111\n"
        "v_cvt_pk_bf16_f32 v118, v108, v109\n"
        "v_exp_f32 v80, v80\n"
        "v_exp_f32 v81, v81\n"
        "v_cvt_pk_bf16_f32 v119, v110, v111\n"
        "v_exp_f32 v82, v82\n"
        "v_exp_f32 v83, v83\n"
        "v_mfma_f32_32x32x16_bf16 v[144:159], a[124:127], v[116:119], v[144:159]\n"
        "buffer_load_dwordx4 a[192:195], v1, s[16:19], s36 offen offset:0\n"
        "v_cvt_pk_bf16_f32 v112, v80, v81\n"
        "v_exp_f32 v84, v84\n"
        "v_exp_f32 v85, v85\n"
        "v_cvt_pk_bf16_f32 v113, v82, v83\n"
        "v_exp_f32 v86, v86\n"
        "v_mfma_f32_16x16x32_bf16 v[172:175], a[204:207], v[204:207], v[172:175]\n"
        "v_exp_f32 v87, v87\n"
        "v_cvt_pk_bf16_f32 v114, v84, v85\n"
        "v_mfma_f32_32x32x16_bf16 v[96:111], a[36:39], v[36:39], v[64:79]\n"
        "buffer_load_dwordx4 a[196:199], v1, s[16:19], s36 offen offset:1024\n"
        "v_exp_f32 v88, v88\n"
        "v_exp_f32 v89, v89\n"
        "v_cvt_pk_bf16_f32 v115, v86, v87\n"
        "v_exp_f32 v90, v90\n"
        "v_mfma_f32_16x16x32_bf16 v[176:179], a[208:211], v[204:207], v[176:179]\n"
        "v_exp_f32 v91, v91\n"
        "v_mfma_f32_32x32x16_bf16 v[144:159], a[128:131], v[112:115], v[144:159]\n"
        "buffer_load_dwordx4 a[200:203], v1, s[16:19], s36 offen offset:2048\n"
        "v_cvt_pk_bf16_f32 v116, v88, v89\n"
        "v_exp_f32 v92, v92\n"
        "v_exp_f32 v93, v93\n"
        "v_cvt_pk_bf16_f32 v117, v90, v91\n"
        "v_exp_f32 v94, v94\n"
        "v_mfma_f32_16x16x32_bf16 v[180:183], a[212:215], v[204:207], v[180:183]\n"
        "v_exp_f32 v95, v95\n"
        "v_cvt_pk_bf16_f32 v118, v92, v93\n"
        "v_exp_f32 v96, v96\n"
        "v_exp_f32 v97, v97\n"
        "v_cvt_pk_bf16_f32 v119, v94, v95\n"
        "v_exp_f32 v98, v98\n"
        "v_exp_f32 v99, v99\n"
        "v_mfma_f32_32x32x16_bf16 v[144:159], a[132:135], v[116:119], v[144:159]\n"
        "buffer_load_dwordx4 a[204:207], v1, s[16:19], s36 offen offset:3072\n"
        "v_cvt_pk_bf16_f32 v112, v96, v97\n"
        "v_exp_f32 v100, v100\n"
        "v_exp_f32 v101, v101\n"
        "v_cvt_pk_bf16_f32 v113, v98, v99\n"
        "v_exp_f32 v102, v102\n"
        "v_mfma_f32_16x16x32_bf16 v[184:187], a[216:219], v[204:207], v[184:187]\n"
        "v_exp_f32 v103, v103\n"
        "v_cvt_pk_bf16_f32 v114, v100, v101\n"
        "v_mfma_f32_32x32x16_bf16 v[80:95], a[40:43], v[36:39], v[64:79]\n"
        "buffer_load_dwordx4 a[208:211], v24, s[16:19], s36 offen offset:0\n"
        "v_exp_f32 v104, v104\n"
        "v_exp_f32 v105, v105\n"
        "v_cvt_pk_bf16_f32 v115, v102, v103\n"
        "v_exp_f32 v106, v106\n"
        "v_mfma_f32_16x16x32_bf16 v[188:191], a[220:223], v[204:207], v[188:191]\n"
        "v_exp_f32 v107, v107\n"
        "v_mfma_f32_32x32x16_bf16 v[144:159], a[136:139], v[112:115], v[144:159]\n"
        "buffer_load_dwordx4 a[212:215], v24, s[16:19], s36 offen offset:1024\n"
        "v_cvt_pk_bf16_f32 v116, v104, v105\n"
        "v_exp_f32 v108, v108\n"
        "v_exp_f32 v109, v109\n"
        "v_cvt_pk_bf16_f32 v117, v106, v107\n"
        "v_exp_f32 v110, v110\n"
        "v_mfma_f32_16x16x32_bf16 v[160:163], a[224:227], v[208:211], v[160:163]\n"
        "v_exp_f32 v111, v111\n"
        "v_cvt_pk_bf16_f32 v118, v108, v109\n"
        "v_exp_f32 v80, v80\n"
        "v_exp_f32 v81, v81\n"
        "v_cvt_pk_bf16_f32 v119, v110, v111\n"
        "v_exp_f32 v82, v82\n"
        "v_exp_f32 v83, v83\n"
        "v_mfma_f32_32x32x16_bf16 v[144:159], a[140:143], v[116:119], v[144:159]\n"
        "buffer_load_dwordx4 a[216:219], v24, s[16:19], s36 offen offset:2048\n"
        "v_cvt_pk_bf16_f32 v112, v80, v81\n"
        "v_exp_f32 v84, v84\n"
        "v_exp_f32 v85, v85\n"
        "v_cvt_pk_bf16_f32 v113, v82, v83\n"
        "v_exp_f32 v86, v86\n"
        "v_mfma_f32_16x16x32_bf16 v[164:167], a[228:231], v[208:211], v[164:167]\n"
        "v_exp_f32 v87, v87\n"
        "v_cvt_pk_bf16_f32 v114, v84, v85\n"
        "v_mfma_f32_32x32x16_bf16 v[96:111], a[44:47], v[36:39], v[64:79]\n"
        "buffer_load_dwordx4 a[220:223], v24, s[16:19], s36 offen offset:3072\n"
        "v_exp_f32 v88, v88\n"
        "v_exp_f32 v89, v89\n"
        "v_cvt_pk_bf16_f32 v115, v86, v87\n"
        "v_exp_f32 v90, v90\n"
        "v_mfma_f32_16x16x32_bf16 v[168:171], a[232:235], v[208:211], v[168:171]\n"
        "v_exp_f32 v91, v91\n"
        "v_mfma_f32_32x32x16_bf16 v[144:159], a[144:147], v[112:115], v[144:159]\n"
        "buffer_load_dwordx4 a[224:227], v25, s[16:19], s36 offen offset:0\n"
        "v_cvt_pk_bf16_f32 v116, v88, v89\n"
        "v_exp_f32 v92, v92\n"
        "v_exp_f32 v93, v93\n"
        "v_cvt_pk_bf16_f32 v117, v90, v91\n"
        "v_exp_f32 v94, v94\n"
        "v_mfma_f32_16x16x32_bf16 v[172:175], a[236:239], v[208:211], v[172:175]\n"
        "v_exp_f32 v95, v95\n"
        "v_cvt_pk_bf16_f32 v118, v92, v93\n"
        "v_exp_f32 v96, v96\n"
        "v_exp_f32 v97, v97\n"
        "v_cvt_pk_bf16_f32 v119, v94, v95\n"
        "v_exp_f32 v98, v98\n"
        "v_exp_f32 v99, v99\n"
        "v_mfma_f32_32x32x16_bf16 v[144:159], a[148:151], v[116:119], v[144:159]\n"
        "buffer_load_dwordx4 a[228:231], v25, s[16:19], s36 offen offset:1024\n"
        "v_cvt_pk_bf16_f32 v112, v96, v97\n"
        "v_exp_f32 v100, v100\n"
        "v_exp_f32 v101, v101\n"
        "v_cvt_pk_bf16_f32 v113, v98, v99\n"
        "v_exp_f32 v102, v102\n"
        "v_mfma_f32_16x16x32_bf16 v[176:179], a[240:243], v[208:211], v[176:179]\n"
        "v_exp_f32 v103, v103\n"
        "v_cvt_pk_bf16_f32 v114, v100, v101\n"
        "v_mfma_f32_32x32x16_bf16 v[80:95], a[48:51], v[36:39], v[64:79]\n"
        "buffer_load_dwordx4 a[232:235], v25, s[16:19], s36 offen offset:2048\n"
        "v_exp_f32 v104, v104\n"
        "v_exp_f32 v105, v105\n"
        "v_cvt_pk_bf16_f32 v115, v102, v103\n"
        "v_exp_f32 v106, v106\n"
        "v_mfma_f32_16x16x32_bf16 v[180:183], a[244:247], v[208:211], v[180:183]\n"
        "v_exp_f32 v107, v107\n"
        "v_mfma_f32_32x32x16_bf16 v[144:159], a[152:155], v[112:115], v[144:159]\n"
        "buffer_load_dwordx4 a[236:239], v25, s[16:19], s36 offen offset:3072\n"
        "v_cvt_pk_bf16_f32 v116, v104, v105\n"
        "v_exp_f32 v108, v108\n"
        "v_exp_f32 v109, v109\n"
        "v_cvt_pk_bf16_f32 v117, v106, v107\n"
        "v_exp_f32 v110, v110\n"
        "v_mfma_f32_16x16x32_bf16 v[184:187], a[248:251], v[208:211], v[184:187]\n"
        "v_exp_f32 v111, v111\n"
        "v_cvt_pk_bf16_f32 v118, v108, v109\n"
        "v_exp_f32 v80, v80\n"
        "v_exp_f32 v81, v81\n"
        "v_cvt_pk_bf16_f32 v119, v110, v111\n"
        "v_exp_f32 v82, v82\n"
        "v_exp_f32 v83, v83\n"
        "v_mfma_f32_32x32x16_bf16 v[144:159], a[156:159], v[116:119], v[144:159]\n"
        "buffer_load_dwordx4 a[240:243], v26, s[16:19], s36 offen offset:0\n"
        "v_cvt_pk_bf16_f32 v112, v80, v81\n"
        "v_exp_f32 v84, v84\n"
        "v_exp_f32 v85, v85\n"
        "v_cvt_pk_bf16_f32 v113, v82, v83\n"
        "v_exp_f32 v86, v86\n"
        "v_mfma_f32_16x16x32_bf16 v[188:191], a[252:255], v[208:211], v[188:191]\n"
        "v_exp_f32 v87, v87\n"
        "v_cvt_pk_bf16_f32 v114, v84, v85\n"
        "v_mfma_f32_32x32x16_bf16 v[96:111], a[52:55], v[36:39], v[64:79]\n"
        "buffer_load_dwordx4 a[244:247], v26, s[16:19], s36 offen offset:1024\n"
        "v_exp_f32 v88, v88\n"
        "v_exp_f32 v89, v89\n"
        "v_cvt_pk_bf16_f32 v115, v86, v87\n"
        "v_exp_f32 v90, v90\n"
        "v_exp_f32 v91, v91\n"
        "v_mfma_f32_32x32x16_bf16 v[144:159], a[160:163], v[112:115], v[144:159]\n"
        "buffer_load_dwordx4 a[248:251], v26, s[16:19], s36 offen offset:2048\n"
        "v_cvt_pk_bf16_f32 v116, v88, v89\n"
        "v_exp_f32 v92, v92\n"
        "v_exp_f32 v93, v93\n"
        "v_cvt_pk_bf16_f32 v117, v90, v91\n"
        "v_exp_f32 v94, v94\n"
        "v_exp_f32 v95, v95\n"
        "v_cvt_pk_bf16_f32 v118, v92, v93\n"
        "v_exp_f32 v96, v96\n"
        "v_exp_f32 v97, v97\n"
        "v_cvt_pk_bf16_f32 v119, v94, v95\n"
        "v_exp_f32 v98, v98\n"
        "v_exp_f32 v99, v99\n"
        "v_mfma_f32_32x32x16_bf16 v[144:159], a[164:167], v[116:119], v[144:159]\n"
        "buffer_load_dwordx4 a[252:255], v26, s[16:19], s36 offen offset:3072\n"
        "v_cvt_pk_bf16_f32 v112, v96, v97\n"
        "v_exp_f32 v100, v100\n"
        "v_exp_f32 v101, v101\n"
        "v_cvt_pk_bf16_f32 v113, v98, v99\n"
        "v_exp_f32 v102, v102\n"
        "v_exp_f32 v103, v103\n"
        "v_cvt_pk_bf16_f32 v114, v100, v101\n"
        "v_mfma_f32_32x32x16_bf16 v[80:95], a[56:59], v[36:39], v[64:79]\n"
        "buffer_load_dwordx4 v[236:239], v24, s[16:19], s60 offen offset:1024\n"
        "v_exp_f32 v104, v104\n"
        "v_exp_f32 v105, v105\n"
        "v_cvt_pk_bf16_f32 v115, v102, v103\n"
        "v_exp_f32 v106, v106\n"
        "v_exp_f32 v107, v107\n"
        "v_mfma_f32_32x32x16_bf16 v[144:159], a[168:171], v[112:115], v[144:159]\n"
        "buffer_load_dwordx4 v[240:243], v24, s[16:19], s61 offen offset:1024\n"
        "v_cvt_pk_bf16_f32 v116, v104, v105\n"
        "v_exp_f32 v108, v108\n"
        "v_exp_f32 v109, v109\n"
        "v_cvt_pk_bf16_f32 v117, v106, v107\n"
        "v_exp_f32 v110, v110\n"
        "v_exp_f32 v111, v111\n"
        "v_cvt_pk_bf16_f32 v118, v108, v109\n"
        "v_exp_f32 v80, v80\n"
        "v_exp_f32 v81, v81\n"
        "v_cvt_pk_bf16_f32 v119, v110, v111\n"
        "v_exp_f32 v82, v82\n"
        "v_exp_f32 v83, v83\n"
        "v_mfma_f32_32x32x16_bf16 v[144:159], a[172:175], v[116:119], v[144:159]\n"
        "v_cvt_pk_bf16_f32 v112, v80, v81\n"
        "v_exp_f32 v84, v84\n"
        "v_exp_f32 v85, v85\n"
        "v_cvt_pk_bf16_f32 v113, v82, v83\n"
        "v_exp_f32 v86, v86\n"
        "v_exp_f32 v87, v87\n"
        "v_cvt_pk_bf16_f32 v114, v84, v85\n"
        "v_mfma_f32_32x32x16_bf16 v[96:111], a[60:63], v[36:39], v[64:79]\n"
        "buffer_load_dwordx4 v[244:247], v24, s[16:19], s60 offen offset:2048\n"
        "v_exp_f32 v88, v88\n"
        "v_exp_f32 v89, v89\n"
        "v_cvt_pk_bf16_f32 v115, v86, v87\n"
        "v_exp_f32 v90, v90\n"
        "v_exp_f32 v91, v91\n"
        "v_mfma_f32_32x32x16_bf16 v[144:159], a[176:179], v[112:115], v[144:159]\n"
        "buffer_load_dwordx4 v[248:251], v24, s[16:19], s61 offen offset:2048\n"
        "v_cvt_pk_bf16_f32 v116, v88, v89\n"
        "v_exp_f32 v92, v92\n"
        "v_exp_f32 v93, v93\n"
        "v_cvt_pk_bf16_f32 v117, v90, v91\n"
        "v_exp_f32 v94, v94\n"
        "v_exp_f32 v95, v95\n"
        "v_cvt_pk_bf16_f32 v118, v92, v93\n"
        "v_exp_f32 v96, v96\n"
        "v_exp_f32 v97, v97\n"
        "v_cvt_pk_bf16_f32 v119, v94, v95\n"
        "v_exp_f32 v98, v98\n"
        "v_exp_f32 v99, v99\n"
        "v_mfma_f32_32x32x16_bf16 v[144:159], a[180:183], v[116:119], v[144:159]\n"
        "v_cvt_pk_bf16_f32 v112, v96, v97\n"
        "v_exp_f32 v100, v100\n"
        "v_exp_f32 v101, v101\n"
        "v_cvt_pk_bf16_f32 v113, v98, v99\n"
        "v_exp_f32 v102, v102\n"
        "v_exp_f32 v103, v103\n"
        "v_cvt_pk_bf16_f32 v114, v100, v101\n"
        "v_exp_f32 v104, v104\n"
        "v_exp_f32 v105, v105\n"
        "v_cvt_pk_bf16_f32 v115, v102, v103\n"
        "v_exp_f32 v106, v106\n"
        "v_exp_f32 v107, v107\n"
        "v_mfma_f32_32x32x16_bf16 v[144:159], a[184:187], v[112:115], v[144:159]\n"
        "v_cvt_pk_bf16_f32 v116, v104, v105\n"
        "v_exp_f32 v108, v108\n"
        "v_exp_f32 v109, v109\n"
        "v_cvt_pk_bf16_f32 v117, v106, v107\n"
        "v_exp_f32 v110, v110\n"
        "v_exp_f32 v111, v111\n"
        "v_cvt_pk_bf16_f32 v118, v108, v109\n"
        "v_cvt_pk_bf16_f32 v119, v110, v111\n"
        "s_nop 1\n"
        "v_mfma_f32_32x32x16_bf16 v[144:159], a[188:191], v[116:119], v[144:159]\n"
        "buffer_load_dwordx4 a[24:27], v1, s[16:19], s62 offen offset:0\n"
        "buffer_load_dwordx4 a[28:31], v1, s[16:19], s63 offen offset:0\n"
        "s_nop 9\n"
        "ds_write_b96 v5, v[148:150]\n"
        "ds_write_b96 v6, v[152:154]\n"
        "ds_write_b96 v7, v[156:158]\n"
        "buffer_load_dwordx4 a[32:35], v1, s[16:19], s62 offen offset:1024\n"
        "s_waitcnt lgkmcnt(0)\n"
        "s_barrier\n"
        "ds_read_b128 v[192:195], v8 offset:0\n"
        "ds_read_b128 v[196:199], v8 offset:1024\n"
        "ds_read_b128 v[200:203], v8 offset:2048\n"
        "buffer_load_dwordx4 a[36:39], v1, s[16:19], s63 offen offset:1024\n"
        "s_waitcnt lgkmcnt(2)\n"
        "v_add_f32 v212, v144, v192\n"
        "v_add_f32 v213, v145, v193\n"
        "v_add_f32 v214, v146, v194\n"
        "s_waitcnt lgkmcnt(0)\n"
        "v_add_f32 v196, v196, v200\n"
        "v_add_f32 v197, v197, v201\n"
        "v_add_f32 v198, v198, v202\n"
        "v_add_f32 v214, v214, v198\n"
        "v_add_f32 v212, v212, v196\n"
        "v_add_f32 v213, v213, v197\n"
        "v_rcp_f32 v214, v214\n"
        "s_nop 0\n"
        "v_mul_f32 v212, v212, v214\n"
        "v_mul_f32 v213, v213, v214\n"
        "v_cvt_pk_bf16_f32 v212, v212, v213\n"
        "ds_write_b32 v9, v212\n"
        "buffer_load_dwordx4 a[40:43], v1, s[16:19], s62 offen offset:2048\n"
        "s_waitcnt lgkmcnt(0)\n"
        "s_barrier\n"
        "ds_read_b128 v[204:207], v10\n"
        "ds_read_b128 v[208:211], v10 offset:512\n"
        "buffer_load_dwordx4 a[44:47], v1, s[16:19], s63 offen offset:2048\n"
        "s_waitcnt lgkmcnt(0)\n"
        "s_waitcnt vmcnt(10)\n"
        "v_mfma_f32_16x16x32_bf16 v[160:163], a[192:195], v[204:207], v[160:163]\n"
        "v_mfma_f32_16x16x32_bf16 v[164:167], a[196:199], v[204:207], v[164:167]\n"
        "v_mfma_f32_16x16x32_bf16 v[168:171], a[200:203], v[204:207], v[168:171]\n"
        "buffer_load_dwordx4 a[48:51], v1, s[16:19], s64 offen offset:0\n"
        "v_mfma_f32_16x16x32_bf16 v[172:175], a[204:207], v[204:207], v[172:175]\n"
        "v_mfma_f32_16x16x32_bf16 v[176:179], a[208:211], v[204:207], v[176:179]\n"
        "v_mfma_f32_16x16x32_bf16 v[180:183], a[212:215], v[204:207], v[180:183]\n"
        "v_mfma_f32_16x16x32_bf16 v[184:187], a[216:219], v[204:207], v[184:187]\n"
        "buffer_load_dwordx4 a[52:55], v1, s[16:19], s65 offen offset:0\n"
        "v_mfma_f32_16x16x32_bf16 v[188:191], a[220:223], v[204:207], v[188:191]\n"
        "v_mfma_f32_16x16x32_bf16 v[160:163], a[224:227], v[208:211], v[160:163]\n"
        "v_mfma_f32_16x16x32_bf16 v[164:167], a[228:231], v[208:211], v[164:167]\n"
        "v_mfma_f32_16x16x32_bf16 v[168:171], a[232:235], v[208:211], v[168:171]\n"
        "buffer_load_dwordx4 a[56:59], v1, s[16:19], s64 offen offset:1024\n"
        "v_mfma_f32_16x16x32_bf16 v[172:175], a[236:239], v[208:211], v[172:175]\n"
        "v_mfma_f32_16x16x32_bf16 v[176:179], a[240:243], v[208:211], v[176:179]\n"
        "v_mfma_f32_16x16x32_bf16 v[180:183], a[244:247], v[208:211], v[180:183]\n"
        "v_mfma_f32_16x16x32_bf16 v[184:187], a[248:251], v[208:211], v[184:187]\n"
        "buffer_load_dwordx4 a[60:63], v1, s[16:19], s65 offen offset:1024\n"
        "v_mfma_f32_16x16x32_bf16 v[188:191], a[252:255], v[208:211], v[188:191]\n"
        "L_A_epi:\n"
        "buffer_load_dwordx4 a[64:67], v1, s[16:19], s64 offen offset:2048\n"
        "s_mov_b32 s46, 0xff00ff00\n"
        "s_mov_b32 s47, 0xff00ff00\n"
        "s_mov_b64 exec, s[46:47]\n"
        "v_pk_mov_b32 v[160:161], v[176:177], v[176:177] op_sel:[0,1]\n"
        "v_pk_mov_b32 v[162:163], v[178:179], v[178:179] op_sel:[0,1]\n"
        "v_pk_mov_b32 v[164:165], v[180:181], v[180:181] op_sel:[0,1]\n"
        "v_pk_mov_b32 v[166:167], v[182:183], v[182:183] op_sel:[0,1]\n"
        "v_pk_mov_b32 v[168:169], v[184:185], v[184:185] op_sel:[0,1]\n"
        "v_pk_mov_b32 v[170:171], v[186:187], v[186:187] op_sel:[0,1]\n"
        "v_pk_mov_b32 v[172:173], v[188:189], v[188:189] op_sel:[0,1]\n"
        "v_pk_mov_b32 v[174:175], v[190:191], v[190:191] op_sel:[0,1]\n"
        "s_mov_b64 exec, -1\n"
        "v_pk_add_f32 v[100:101], v[160:161], v[162:163]\n"
        "v_pk_add_f32 v[102:103], v[164:165], v[166:167]\n"
        "v_pk_add_f32 v[104:105], v[168:169], v[170:171]\n"
        "v_pk_add_f32 v[106:107], v[172:173], v[174:175]\n"
        "v_pk_add_f32 v[100:101], v[100:101], v[102:103]\n"
        "v_pk_add_f32 v[104:105], v[104:105], v[106:107]\n"
        "v_pk_add_f32 v[100:101], v[100:101], v[104:105]\n"
        "v_add_f32 v98, v100, v101\n"
        "buffer_load_dwordx4 a[68:71], v1, s[16:19], s65 offen offset:2048\n"
        "v_mul_f32 v98, 0x3d800000, v98\n"
        "v_pk_add_f32 v[64:65], v[160:161], v[98:99] op_sel_hi:[1,0] neg_lo:[0,1] neg_hi:[0,1]\n"
        "v_pk_add_f32 v[66:67], v[162:163], v[98:99] op_sel_hi:[1,0] neg_lo:[0,1] neg_hi:[0,1]\n"
        "v_add_f32_dpp v96, v98, v98 row_ror:8 row_mask:0xf bank_mask:0xf\n"
        "v_mov_b32 v99, v96\n"
        "v_pk_add_f32 v[68:69], v[164:165], v[98:99] op_sel_hi:[1,0] neg_lo:[0,1] neg_hi:[0,1]\n"
        "v_pk_add_f32 v[70:71], v[166:167], v[98:99] op_sel_hi:[1,0] neg_lo:[0,1] neg_hi:[0,1]\n"
        "v_permlane16_swap_b32 v96, v99\n"
        "v_pk_add_f32 v[72:73], v[168:169], v[98:99] op_sel_hi:[1,0] neg_lo:[0,1] neg_hi:[0,1]\n"
        "v_add_f32 v96, v96, v99\n"
        "v_mov_b32 v99, v96\n"
        "v_pk_add_f32 v[74:75], v[170:171], v[98:99] op_sel_hi:[1,0] neg_lo:[0,1] neg_hi:[0,1]\n"
        "v_pk_add_f32 v[76:77], v[172:173], v[98:99] op_sel_hi:[1,0] neg_lo:[0,1] neg_hi:[0,1]\n"
        "v_permlane32_swap_b32 v96, v99\n"
        "v_pk_add_f32 v[78:79], v[174:175], v[98:99] op_sel_hi:[1,0] neg_lo:[0,1] neg_hi:[0,1]\n"
        "v_add_f32 v96, v96, v99\n"
        "v_pk_mul_f32 v[100:101], v[64:65], v[64:65]\n"
        "v_mul_f32 v96, 0x3e000000, v96\n"
        "v_pk_mul_f32 v[102:103], v[66:67], v[66:67]\n"
        "v_sub_f32 v99, v98, v96\n"
        "v_pk_fma_f32 v[100:101], v[68:69], v[68:69], v[100:101]\n"
        "v_pk_fma_f32 v[102:103], v[70:71], v[70:71], v[102:103]\n"
        "v_pk_fma_f32 v[100:101], v[72:73], v[72:73], v[100:101]\n"
        "v_pk_fma_f32 v[102:103], v[74:75], v[74:75], v[102:103]\n"
        "v_pk_fma_f32 v[100:101], v[76:77], v[76:77], v[100:101]\n"
        "v_pk_fma_f32 v[102:103], v[78:79], v[78:79], v[102:103]\n"
        "v_pk_add_f32 v[100:101], v[100:101], v[102:103]\n"
        "v_add_f32 v97, v100, v101\n"
        "v_mul_f32 v99, v99, v99\n"
        "s_mov_b32 s38, 0x41800000\n"
        "v_fma_f32 v97, v99, s38, v97\n"
        "v_cvt_pk_bf16_f32 v32, v160, v161\n"
        "v_cvt_pk_bf16_f32 v33, v162, v163\n"
        "v_add_f32_dpp v97, v97, v97 row_ror:8 row_mask:0xf bank_mask:0xf\n"
        "v_mov_b32 v99, v97\n"
        "v_cvt_pk_bf16_f32 v34, v164, v165\n"
        "v_cvt_pk_bf16_f32 v35, v166, v167\n"
        "v_permlane16_swap_b32 v97, v99\n"
        "v_add_f32 v97, v97, v99\n"
        "v_mov_b32 v99, v97\n"
        "v_cvt_pk_bf16_f32 v36, v168, v169\n"
        "v_cvt_pk_bf16_f32 v37, v170, v171\n"
        "v_permlane32_swap_b32 v97, v99\n"
        "v_add_f32 v97, v97, v99\n"
        "s_mov_b64 exec, 0xff\n"
        "ds_write_b64 v23, v[96:97]\n"
        "s_mov_b64 exec, -1\n"
        "v_cvt_pk_bf16_f32 v38, v172, v173\n"
        "v_cvt_pk_bf16_f32 v39, v174, v175\n"
        "buffer_load_dwordx4 a[72:75], v1, s[16:19], s66 offen offset:0\n"
        "s_waitcnt lgkmcnt(0)\n"
        "s_barrier\n"
        "ds_read_b128 v[100:103], v3\n"
        "ds_read_b128 v[104:107], v3 offset:16\n"
        "buffer_load_dwordx4 a[76:79], v1, s[16:19], s67 offen offset:0\n"
        "buffer_load_dwordx4 a[80:83], v1, s[16:19], s66 offen offset:1024\n"
        "buffer_load_dwordx4 a[84:87], v1, s[16:19], s67 offen offset:1024\n"
        "s_lshl_b32 s37, s31, 21\n"
        "s_add_u32 s37, s37, 0xd12000\n"
        "s_waitcnt lgkmcnt(0)\n"
        "v_add_f32 v108, v100, v102\n"
        "v_add_f32 v109, v104, v106\n"
        "v_add_f32 v110, v101, v103\n"
        "v_add_f32 v111, v105, v107\n"
        "v_add_f32 v108, v108, v109\n"
        "v_add_f32 v110, v110, v111\n"
        "v_mul_f32 v108, 0x3e800000, v108\n"
        "v_sub_f32 v100, v100, v108\n"
        "v_sub_f32 v102, v102, v108\n"
        "v_sub_f32 v104, v104, v108\n"
        "v_sub_f32 v106, v106, v108\n"
        "v_mul_f32 v109, v100, v100\n"
        "v_mul_f32 v111, v102, v102\n"
        "v_fmac_f32 v109, v104, v104\n"
        "v_fmac_f32 v111, v106, v106\n"
        "v_add_f32 v109, v109, v111\n"
        "s_mov_b32 s38, 0x43000000\n"
        "v_fma_f32 v110, v109, s38, v110\n"
        "v_mul_f32 v110, 0x3b000000, v110\n"
        "v_cvt_pk_bf16_f32 v110, v110, v108\n"
        "v_and_b32 v108, 0xffff0000, v110\n"
        "v_lshlrev_b32 v110, 16, v110\n"
        "v_add_f32 v110, 0x3727c5ac, v110\n"
        "v_cvt_pk_bf16_f32 v110, v110, v110\n"
        "v_lshlrev_b32 v110, 16, v110\n"
        "v_sqrt_f32 v110, v110\n"
        "s_nop 0\n"
        "v_cvt_pk_bf16_f32 v110, v110, v110\n"
        "v_lshlrev_b32 v110, 16, v110\n"
        "v_rcp_f32 v110, v110\n"
        "v_sub_f32 v108, v98, v108\n"
        "v_mul_f32 v108, v108, v110\n"
        "v_pk_fma_f32 v[64:65], v[64:65], v[110:111], v[108:109] op_sel_hi:[1,0,0]\n"
        "v_pk_fma_f32 v[66:67], v[66:67], v[110:111], v[108:109] op_sel_hi:[1,0,0]\n"
        "v_pk_fma_f32 v[68:69], v[68:69], v[110:111], v[108:109] op_sel_hi:[1,0,0]\n"
        "v_pk_fma_f32 v[70:71], v[70:71], v[110:111], v[108:109] op_sel_hi:[1,0,0]\n"
        "v_pk_fma_f32 v[72:73], v[72:73], v[110:111], v[108:109] op_sel_hi:[1,0,0]\n"
        "v_pk_fma_f32 v[74:75], v[74:75], v[110:111], v[108:109] op_sel_hi:[1,0,0]\n"
        "v_pk_fma_f32 v[76:77], v[76:77], v[110:111], v[108:109] op_sel_hi:[1,0,0]\n"
        "v_pk_fma_f32 v[78:79], v[78:79], v[110:111], v[108:109] op_sel_hi:[1,0,0]\n"
        "v_cvt_pk_bf16_f32 v80, v64, v65\n"
        "v_cvt_pk_bf16_f32 v81, v66, v67\n"
        "v_cvt_pk_bf16_f32 v82, v68, v69\n"
        "v_cvt_pk_bf16_f32 v83, v70, v71\n"
        "v_cvt_pk_bf16_f32 v84, v72, v73\n"
        "v_cvt_pk_bf16_f32 v85, v74, v75\n"
        "v_cvt_pk_bf16_f32 v86, v76, v77\n"
        "v_cvt_pk_bf16_f32 v87, v78, v79\n"
        "v_min_u32 v80, -2, v80\n"
        "v_min_u32 v81, -2, v81\n"
        "v_min_u32 v82, -2, v82\n"
        "v_min_u32 v83, -2, v83\n"
        "v_min_u32 v84, -2, v84\n"
        "v_min_u32 v85, -2, v85\n"
        "v_min_u32 v86, -2, v86\n"
        "v_min_u32 v87, -2, v87\n"
        "buffer_store_dwordx4 v[80:83], v13, s[16:19], s37 offen sc1\n"
        "buffer_store_dwordx4 v[84:87], v13, s[16:19], s37 offen offset:2048 sc1\n"
        "v_mov_b32 v28, v13\n"
        "s_lshl_b32 s36, s54, 2\n"
        "s_add_u32 s36, s36, s28\n"
        "s_lshl_b32 s55, s36, 15\n"
        "s_add_u32 s55, s55, s37\n"
        "s_waitcnt vmcnt(18)\n"
        "v_accvgpr_write_b32 a0, v40\n"
        "v_accvgpr_write_b32 a1, v41\n"
        "v_accvgpr_write_b32 a2, v42\n"
        "v_accvgpr_write_b32 a3, v43\n"
        "v_accvgpr_write_b32 a4, v44\n"
        "v_accvgpr_write_b32 a5, v45\n"
        "v_accvgpr_write_b32 a6, v46\n"
        "v_accvgpr_write_b32 a7, v47\n"
        "v_accvgpr_write_b32 a8, v48\n"
        "v_accvgpr_write_b32 a9, v49\n"
        "v_accvgpr_write_b32 a10, v50\n"
        "v_accvgpr_write_b32 a11, v51\n"
        "v_accvgpr_write_b32 a12, v52\n"
        "v_accvgpr_write_b32 a13, v53\n"
        "v_accvgpr_write_b32 a14, v54\n"
        "v_accvgpr_write_b32 a15, v55\n"
        "v_accvgpr_write_b32 a16, v56\n"
        "v_accvgpr_write_b32 a17, v57\n"
        "v_accvgpr_write_b32 a18, v58\n"
        "v_accvgpr_write_b32 a19, v59\n"
        "v_accvgpr_write_b32 a20, v60\n"
        "v_accvgpr_write_b32 a21, v61\n"
        "v_accvgpr_write_b32 a22, v62\n"
        "v_accvgpr_write_b32 a23, v63\n"
        "v_accvgpr_write_b32 a96, v120\n"
        "v_accvgpr_write_b32 a97, v121\n"
        "v_accvgpr_write_b32 a98, v122\n"
        "v_accvgpr_write_b32 a99, v123\n"
        "v_accvgpr_write_b32 a100, v124\n"
        "v_accvgpr_write_b32 a101, v125\n"
        "v_accvgpr_write_b32 a102, v126\n"
        "v_accvgpr_write_b32 a103, v127\n"
        "v_accvgpr_write_b32 a128, v228\n"
        "v_accvgpr_write_b32 a129, v229\n"
        "v_accvgpr_write_b32 a130, v230\n"
        "v_accvgpr_write_b32 a131, v231\n"
        "v_accvgpr_write_b32 a132, v232\n"
        "v_accvgpr_write_b32 a133, v233\n"
        "v_accvgpr_write_b32 a134, v234\n"
        "v_accvgpr_write_b32 a135, v235\n"
        "v_accvgpr_write_b32 a160, v236\n"
        "v_accvgpr_write_b32 a161, v237\n"
        "v_accvgpr_write_b32 a162, v238\n"
        "v_accvgpr_write_b32 a163, v239\n"
        "v_accvgpr_write_b32 a164, v240\n"
        "v_accvgpr_write_b32 a165, v241\n"
        "v_accvgpr_write_b32 a166, v242\n"
        "v_accvgpr_write_b32 a167, v243\n"
        "v_accvgpr_write_b32 a192, v244\n"
        "v_accvgpr_write_b32 a193, v245\n"
        "v_accvgpr_write_b32 a194, v246\n"
        "v_accvgpr_write_b32 a195, v247\n"
        "v_accvgpr_write_b32 a196, v248\n"
        "v_accvgpr_write_b32 a197, v249\n"
        "v_accvgpr_write_b32 a198, v250\n"
        "v_accvgpr_write_b32 a199, v251\n"
        "v_add_u32 v47, 0x1000, v1\n"
        "v_and_b32 v46, 3, v0\n"
        "v_lshlrev_b32 v46, 7, v46\n"
        "v_bfe_u32 v45, v0, 2, 2\n"
        "v_lshl_add_u32 v46, v45, 13, v46\n"
        "v_add_u32 v2, 0x10000, v1\n"
        "v_add_u32 v3, 0x20000, v1\n"
        "s_add_u32 s57, s56, 4096\n"
        "s_lshl_b32 s34, s54, 8\n"
        "s_add_u32 s34, s34, s53\n"
        "s_add_u32 s33, s34, 0x1080\n"
        "s_add_u32 s34, s34, 4096\n"
        "s_mul_i32 s38, s28, 28\n"
        "s_mul_i32 s58, s51, 112\n"
        "s_add_u32 s58, s58, s38\n"
        "s_lshl_b32 s58, s58, 10\n"
        "s_add_u32 s58, s58, 0x1312000\n"
        "s_xor_b32 s59, s51, 1\n"
        "s_mul_i32 s59, s59, 112\n"
        "s_add_u32 s59, s59, s38\n"
        "s_lshl_b32 s59, s59, 10\n"
        "s_add_u32 s59, s59, 0x1312000\n"
        "s_lshl_b32 s41, s28, 12\n"
        "s_add_u32 s43, s43, s41\n"
        "s_add_u32 s43, s43, 0x28000\n"
        "ds_read_b128 a[224:227], v1 offset:0\n"
        "ds_read_b128 a[228:231], v1 offset:32768\n"
        "ds_read_b128 a[232:235], v2 offset:0\n"
        "ds_read_b128 a[236:239], v1 offset:8192\n"
        "ds_read_b128 a[240:243], v1 offset:40960\n"
        "ds_read_b128 a[244:247], v2 offset:8192\n"
        "s_sleep 6\n"
        "s_add_u32 s36, s55, 0\n"
        "buffer_load_dwordx4 v[128:131], v1, s[16:19], s36 offen offset:0 sc1\n"
        "s_add_u32 s36, s55, 8192\n"
        "buffer_load_dwordx4 v[160:163], v1, s[16:19], s36 offen offset:0 sc1\n"
        "s_add_u32 s36, s55, 16384\n"
        "buffer_load_dwordx4 v[192:195], v1, s[16:19], s36 offen offset:0 sc1\n"
        "s_add_u32 s36, s55, 24576\n"
        "buffer_load_dwordx4 v[224:227], v1, s[16:19], s36 offen offset:0 sc1\n"
        "s_add_u32 s36, s55, 0\n"
        "buffer_load_dwordx4 v[132:135], v1, s[16:19], s36 offen offset:1024 sc1\n"
        "buffer_load_dwordx4 v[136:139], v1, s[16:19], s36 offen offset:2048 sc1\n"
        "buffer_load_dwordx4 v[140:143], v1, s[16:19], s36 offen offset:3072 sc1\n"
        "s_add_u32 s36, s55, 4096\n"
        "buffer_load_dwordx4 v[144:147], v1, s[16:19], s36 offen offset:0 sc1\n"
        "buffer_load_dwordx4 v[148:151], v1, s[16:19], s36 offen offset:1024 sc1\n"
        "buffer_load_dwordx4 v[152:155], v1, s[16:19], s36 offen offset:2048 sc1\n"
        "buffer_load_dwordx4 v[156:159], v1, s[16:19], s36 offen offset:3072 sc1\n"
        "s_add_u32 s36, s55, 8192\n"
        "buffer_load_dwordx4 v[164:167], v1, s[16:19], s36 offen offset:1024 sc1\n"
        "s_waitcnt vmcnt(8)\n"
        "v_max3_u32 v29, v128, v129, v130\n"
        "v_max3_u32 v29, v131, v160, v29\n"
        "v_max3_u32 v29, v161, v162, v29\n"
        "v_max3_u32 v29, v163, v192, v29\n"
        "v_max3_u32 v29, v193, v194, v29\n"
        "v_max3_u32 v29, v195, v224, v29\n"
        "v_max3_u32 v29, v225, v226, v29\n"
        "v_max_u32 v29, v227, v29\n"
        "v_cmp_eq_u32 vcc, -1, v29\n"
        "s_nop 1\n"
        "s_cbranch_vccnz L_x0_rt\n"
        "L_x0_go:\n"
        "s_waitcnt lgkmcnt(5)\n"
        "v_mfma_f32_32x32x16_bf16 v[48:63], a[224:227], v[128:131], 0\n"
        "ds_read_b128 a[248:251], v1 offset:16384\n"
        "s_waitcnt lgkmcnt(5)\n"
        "v_mfma_f32_32x32x16_bf16 v[64:79], a[228:231], v[128:131], 0\n"
        "ds_read_b128 a[252:255], v1 offset:49152\n"
        "buffer_load_dwordx4 v[168:171], v1, s[16:19], s36 offen offset:2048 sc1\n"
        "s_waitcnt lgkmcnt(5)\n"
        "v_mfma_f32_32x32x16_bf16 v[80:95], a[232:235], v[128:131], 0\n"
        "ds_read_b128 a[224:227], v2 offset:16384\n"
        "s_waitcnt lgkmcnt(5)\n"
        "v_mfma_f32_32x32x16_bf16 v[48:63], a[236:239], v[160:163], v[48:63]\n"
        "ds_read_b128 a[228:231], v1 offset:24576\n"
        "buffer_load_dwordx4 v[172:175], v1, s[16:19], s36 offen offset:3072 sc1\n"
        "s_waitcnt lgkmcnt(5)\n"
        "v_mfma_f32_32x32x16_bf16 v[64:79], a[240:243], v[160:163], v[64:79]\n"
        "ds_read_b128 a[232:235], v1 offset:57344\n"
        "s_waitcnt lgkmcnt(5)\n"
        "v_mfma_f32_32x32x16_bf16 v[80:95], a[244:247], v[160:163], v[80:95]\n"
        "ds_read_b128 a[236:239], v2 offset:24576\n"
        "s_add_u32 s36, s55, 12288\n"
        "buffer_load_dwordx4 v[176:179], v1, s[16:19], s36 offen offset:0 sc1\n"
        "s_waitcnt lgkmcnt(5)\n"
        "v_mfma_f32_32x32x16_bf16 v[48:63], a[248:251], v[192:195], v[48:63]\n"
        "s_waitcnt vmcnt(7)\n"
        "v_max3_u32 v29, v132, v133, v134\n"
        "v_max3_u32 v29, v135, v136, v29\n"
        "ds_read_b128 a[240:243], v1 offset:1024\n"
        "s_waitcnt lgkmcnt(5)\n"
        "v_mfma_f32_32x32x16_bf16 v[64:79], a[252:255], v[192:195], v[64:79]\n"
        "v_max3_u32 v29, v137, v138, v29\n"
        "v_max3_u32 v29, v139, v140, v29\n"
        "ds_read_b128 a[244:247], v1 offset:33792\n"
        "buffer_load_dwordx4 v[180:183], v1, s[16:19], s36 offen offset:1024 sc1\n"
        "s_waitcnt lgkmcnt(5)\n"
        "v_mfma_f32_32x32x16_bf16 v[80:95], a[224:227], v[192:195], v[80:95]\n"
        "v_max3_u32 v29, v141, v142, v29\n"
        "v_max3_u32 v29, v143, v144, v29\n"
        "ds_read_b128 a[248:251], v2 offset:1024\n"
        "s_waitcnt lgkmcnt(5)\n"
        "v_mfma_f32_32x32x16_bf16 v[48:63], a[228:231], v[224:227], v[48:63]\n"
        "v_max3_u32 v29, v145, v146, v29\n"
        "v_max_u32 v29, v147, v29\n"
        "ds_read_b128 a[252:255], v1 offset:2048\n"
        "buffer_load_dwordx4 v[184:187], v1, s[16:19], s36 offen offset:2048 sc1\n"
        "s_waitcnt lgkmcnt(5)\n"
        "v_mfma_f32_32x32x16_bf16 v[64:79], a[232:235], v[224:227], v[64:79]\n"
        "v_cmp_eq_u32 vcc, -1, v29\n"
        "s_nop 1\n"
        "s_cbranch_vccnz L_x1_rt\n"
        "L_x1_go:\n"
        "ds_read_b128 a[224:227], v1 offset:34816\n"
        "s_waitcnt lgkmcnt(5)\n"
        "v_mfma_f32_32x32x16_bf16 v[80:95], a[236:239], v[224:227], v[80:95]\n"
        "ds_read_b128 a[228:231], v2 offset:2048\n"
        "buffer_load_dwordx4 v[188:191], v1, s[16:19], s36 offen offset:3072 sc1\n"
        "s_waitcnt lgkmcnt(5)\n"
        "v_mfma_f32_32x32x16_bf16 v[48:63], a[240:243], v[132:135], v[48:63]\n"
        "ds_read_b128 a[232:235], v1 offset:3072\n"
        "s_waitcnt lgkmcnt(5)\n"
        "v_mfma_f32_32x32x16_bf16 v[64:79], a[244:247], v[132:135], v[64:79]\n"
        "ds_read_b128 a[236:239], v1 offset:35840\n"
        "s_add_u32 s36, s55, 16384\n"
        "buffer_load_dwordx4 v[196:199], v1, s[16:19], s36 offen offset:1024 sc1\n"
        "s_waitcnt lgkmcnt(5)\n"
        "v_mfma_f32_32x32x16_bf16 v[80:95], a[248:251], v[132:135], v[80:95]\n"
        "ds_read_b128 a[240:243], v2 offset:3072\n"
        "s_waitcnt lgkmcnt(5)\n"
        "v_mfma_f32_32x32x16_bf16 v[48:63], a[252:255], v[136:139], v[48:63]\n"
        "ds_read_b128 a[244:247], v1 offset:4096\n"
        "buffer_load_dwordx4 v[200:203], v1, s[16:19], s36 offen offset:2048 sc1\n"
        "s_waitcnt lgkmcnt(5)\n"
        "v_mfma_f32_32x32x16_bf16 v[64:79], a[224:227], v[136:139], v[64:79]\n"
        "ds_read_b128 a[248:251], v1 offset:36864\n"
        "s_waitcnt lgkmcnt(5)\n"
        "v_mfma_f32_32x32x16_bf16 v[80:95], a[228:231], v[136:139], v[80:95]\n"
        "ds_read_b128 a[252:255], v2 offset:4096\n"
        "buffer_load_dwordx4 v[204:207], v1, s[16:19], s36 offen offset:3072 sc1\n"
        "s_waitcnt lgkmcnt(5)\n"
        "v_mfma_f32_32x32x16_bf16 v[48:63], a[232:235], v[140:143], v[48:63]\n"
        "s_waitcnt vmcnt(9)\n"
        "v_max3_u32 v29, v148, v149, v150\n"
        "v_max3_u32 v29, v151, v152, v29\n"
        "ds_read_b128 a[224:227], v1 offset:5120\n"
        "s_waitcnt lgkmcnt(5)\n"
        "v_mfma_f32_32x32x16_bf16 v[64:79], a[236:239], v[140:143], v[64:79]\n"
        "v_max3_u32 v29, v153, v154, v29\n"
        "v_max3_u32 v29, v155, v156, v29\n"
        "ds_read_b128 a[228:231], v1 offset:37888\n"
        "s_add_u32 s36, s55, 20480\n"
        "buffer_load_dwordx4 v[208:211], v1, s[16:19], s36 offen offset:0 sc1\n"
        "s_waitcnt lgkmcnt(5)\n"
        "v_mfma_f32_32x32x16_bf16 v[80:95], a[240:243], v[140:143], v[80:95]\n"
        "v_max3_u32 v29, v157, v158, v29\n"
        "v_max3_u32 v29, v159, v164, v29\n"
        "ds_read_b128 a[232:235], v2 offset:5120\n"
        "s_waitcnt lgkmcnt(5)\n"
        "v_mfma_f32_32x32x16_bf16 v[48:63], a[244:247], v[144:147], v[48:63]\n"
        "v_max3_u32 v29, v165, v166, v29\n"
        "v_max_u32 v29, v167, v29\n"
        "ds_read_b128 a[236:239], v1 offset:6144\n"
        "buffer_load_dwordx4 v[212:215], v1, s[16:19], s36 offen offset:1024 sc1\n"
        "s_waitcnt lgkmcnt(5)\n"
        "v_mfma_f32_32x32x16_bf16 v[64:79], a[248:251], v[144:147], v[64:79]\n"
        "v_cmp_eq_u32 vcc, -1, v29\n"
        "s_nop 1\n"
        "s_cbranch_vccnz L_x2_rt\n"
        "L_x2_go:\n"
        "ds_read_b128 a[240:243], v1 offset:38912\n"
        "s_waitcnt lgkmcnt(5)\n"
        "v_mfma_f32_32x32x16_bf16 v[80:95], a[252:255], v[144:147], v[80:95]\n"
        "ds_read_b128 a[244:247], v2 offset:6144\n"
        "buffer_load_dwordx4 v[216:219], v1, s[16:19], s36 offen offset:2048 sc1\n"
        "s_waitcnt lgkmcnt(5)\n"
        "v_mfma_f32_32x32x16_bf16 v[48:63], a[224:227], v[148:151], v[48:63]\n"
        "ds_read_b128 a[248:251], v1 offset:7168\n"
        "s_waitcnt lgkmcnt(5)\n"
        "v_mfma_f32_32x32x16_bf16 v[64:79], a[228:231], v[148:151], v[64:79]\n"
        "ds_read_b128 a[252:255], v1 offset:39936\n"
        "buffer_load_dwordx4 v[220:223], v1, s[16:19], s36 offen offset:3072 sc1\n"
        "s_waitcnt lgkmcnt(5)\n"
        "v_mfma_f32_32x32x16_bf16 v[80:95], a[232:235], v[148:151], v[80:95]\n"
        "ds_read_b128 a[224:227], v2 offset:7168\n"
        "s_waitcnt lgkmcnt(5)\n"
        "v_mfma_f32_32x32x16_bf16 v[48:63], a[236:239], v[152:155], v[48:63]\n"
        "ds_read_b128 a[228:231], v1 offset:9216\n"
        "s_add_u32 s36, s55, 24576\n"
        "buffer_load_dwordx4 v[228:231], v1, s[16:19], s36 offen offset:1024 sc1\n"
        "s_waitcnt lgkmcnt(5)\n"
        "v_mfma_f32_32x32x16_bf16 v[64:79], a[240:243], v[152:155], v[64:79]\n"
        "ds_read_b128 a[232:235], v1 offset:41984\n"
        "s_waitcnt lgkmcnt(5)\n"
        "v_mfma_f32_32x32x16_bf16 v[80:95], a[244:247], v[152:155], v[80:95]\n"
        "ds_read_b128 a[236:239], v2 offset:9216\n"
        "buffer_load_dwordx4 v[232:235], v1, s[16:19], s36 offen offset:2048 sc1\n"
        "s_waitcnt lgkmcnt(5)\n"
        "v_mfma_f32_32x32x16_bf16 v[48:63], a[248:251], v[156:159], v[48:63]\n"
        "s_waitcnt vmcnt(11)\n"
        "v_max3_u32 v29, v168, v169, v170\n"
        "v_max3_u32 v29, v171, v172, v29\n"
        "ds_read_b128 a[240:243], v1 offset:10240\n"
        "s_waitcnt lgkmcnt(5)\n"
        "v_mfma_f32_32x32x16_bf16 v[64:79], a[252:255], v[156:159], v[64:79]\n"
        "v_max3_u32 v29, v173, v174, v29\n"
        "v_max3_u32 v29, v175, v176, v29\n"
        "ds_read_b128 a[244:247], v1 offset:43008\n"
        "buffer_load_dwordx4 v[236:239], v1, s[16:19], s36 offen offset:3072 sc1\n"
        "s_waitcnt lgkmcnt(5)\n"
        "v_mfma_f32_32x32x16_bf16 v[80:95], a[224:227], v[156:159], v[80:95]\n"
        "v_max3_u32 v29, v177, v178, v29\n"
        "v_max3_u32 v29, v179, v180, v29\n"
        "ds_read_b128 a[248:251], v2 offset:10240\n"
        "s_waitcnt lgkmcnt(5)\n"
        "v_mfma_f32_32x32x16_bf16 v[48:63], a[228:231], v[164:167], v[48:63]\n"
        "v_max3_u32 v29, v181, v182, v29\n"
        "v_max_u32 v29, v183, v29\n"
        "ds_read_b128 a[252:255], v1 offset:11264\n"
        "s_add_u32 s36, s55, 28672\n"
        "buffer_load_dwordx4 v[240:243], v1, s[16:19], s36 offen offset:0 sc1\n"
        "s_waitcnt lgkmcnt(5)\n"
        "v_mfma_f32_32x32x16_bf16 v[64:79], a[232:235], v[164:167], v[64:79]\n"
        "v_cmp_eq_u32 vcc, -1, v29\n"
        "s_nop 1\n"
        "s_cbranch_vccnz L_x3_rt\n"
        "L_x3_go:\n"
        "ds_read_b128 a[224:227], v1 offset:44032\n"
        "s_waitcnt lgkmcnt(5)\n"
        "v_mfma_f32_32x32x16_bf16 v[80:95], a[236:239], v[164:167], v[80:95]\n"
        "ds_read_b128 a[228:231], v2 offset:11264\n"
        "buffer_load_dwordx4 v[244:247], v1, s[16:19], s36 offen offset:1024 sc1\n"
        "s_waitcnt lgkmcnt(5)\n"
        "v_mfma_f32_32x32x16_bf16 v[48:63], a[240:243], v[168:171], v[48:63]\n"
        "ds_read_b128 a[232:235], v1 offset:12288\n"
        "s_waitcnt lgkmcnt(5)\n"
        "v_mfma_f32_32x32x16_bf16 v[64:79], a[244:247], v[168:171], v[64:79]\n"
        "ds_read_b128 a[236:239], v1 offset:45056\n"
        "buffer_load_dwordx4 v[248:251], v1, s[16:19], s36 offen offset:2048 sc1\n"
        "s_waitcnt lgkmcnt(5)\n"
        "v_mfma_f32_32x32x16_bf16 v[80:95], a[248:251], v[168:171], v[80:95]\n"
        "ds_read_b128 a[240:243], v2 offset:12288\n"
        "s_waitcnt lgkmcnt(5)\n"
        "v_mfma_f32_32x32x16_bf16 v[48:63], a[252:255], v[172:175], v[48:63]\n"
        "ds_read_b128 a[244:247], v1 offset:13312\n"
        "buffer_load_dwordx4 v[252:255], v1, s[16:19], s36 offen offset:3072 sc1\n"
        "s_waitcnt lgkmcnt(5)\n"
        "v_mfma_f32_32x32x16_bf16 v[64:79], a[224:227], v[172:175], v[64:79]\n"
        "ds_read_b128 a[248:251], v1 offset:46080\n"
        "s_waitcnt lgkmcnt(5)\n"
        "v_mfma_f32_32x32x16_bf16 v[80:95], a[228:231], v[172:175], v[80:95]\n"
        "ds_read_b128 a[252:255], v2 offset:13312\n"
        "s_waitcnt lgkmcnt(5)\n"
        "v_mfma_f32_32x32x16_bf16 v[48:63], a[232:235], v[176:179], v[48:63]\n"
        "s_waitcnt vmcnt(12)\n"
        "v_max3_u32 v29, v184, v185, v186\n"
        "v_max3_u32 v29, v187, v188, v29\n"
        "ds_read_b128 a[224:227], v1 offset:14336\n"
        "s_waitcnt lgkmcnt(5)\n"
        "v_mfma_f32_32x32x16_bf16 v[64:79], a[236:239], v[176:179], v[64:79]\n"
        "v_max3_u32 v29, v189, v190, v29\n"
        "v_max3_u32 v29, v191, v196, v29\n"
        "ds_read_b128 a[228:231], v1 offset:47104\n"
        "s_waitcnt lgkmcnt(5)\n"
        "v_mfma_f32_32x32x16_bf16 v[80:95], a[240:243], v[176:179], v[80:95]\n"
        "v_max3_u32 v29, v197, v198, v29\n"
        "v_max3_u32 v29, v199, v200, v29\n"
        "ds_read_b128 a[232:235], v2 offset:14336\n"
        "s_waitcnt lgkmcnt(5)\n"
        "v_mfma_f32_32x32x16_bf16 v[48:63], a[244:247], v[180:183], v[48:63]\n"
        "v_max3_u32 v29, v201, v202, v29\n"
        "v_max_u32 v29, v203, v29\n"
        "ds_read_b128 a[236:239], v1 offset:15360\n"
        "s_waitcnt lgkmcnt(5)\n"
        "v_mfma_f32_32x32x16_bf16 v[64:79], a[248:251], v[180:183], v[64:79]\n"
        "v_cmp_eq_u32 vcc, -1, v29\n"
        "s_nop 1\n"
        "s_cbranch_vccnz L_x4_rt\n"
        "L_x4_go:\n"
        "ds_read_b128 a[240:243], v1 offset:48128\n"
        "s_waitcnt lgkmcnt(5)\n"
        "v_mfma_f32_32x32x16_bf16 v[80:95], a[252:255], v[180:183], v[80:95]\n"
        "ds_read_b128 a[244:247], v2 offset:15360\n"
        "s_waitcnt lgkmcnt(5)\n"
        "v_mfma_f32_32x32x16_bf16 v[48:63], a[224:227], v[184:187], v[48:63]\n"
        "ds_read_b128 a[248:251], v1 offset:17408\n"
        "s_waitcnt lgkmcnt(5)\n"
        "v_mfma_f32_32x32x16_bf16 v[64:79], a[228:231], v[184:187], v[64:79]\n"
        "ds_read_b128 a[252:255], v1 offset:50176\n"
        "s_waitcnt lgkmcnt(5)\n"
        "v_mfma_f32_32x32x16_bf16 v[80:95], a[232:235], v[184:187], v[80:95]\n"
        "ds_read_b128 a[224:227], v2 offset:17408\n"
        "s_waitcnt lgkmcnt(5)\n"
        "v_mfma_f32_32x32x16_bf16 v[48:63], a[236:239], v[188:191], v[48:63]\n"
        "ds_read_b128 a[228:231], v1 offset:18432\n"
        "s_waitcnt lgkmcnt(5)\n"
        "v_mfma_f32_32x32x16_bf16 v[64:79], a[240:243], v[188:191], v[64:79]\n"
        "ds_read_b128 a[232:235], v1 offset:51200\n"
        "s_waitcnt lgkmcnt(5)\n"
        "v_mfma_f32_32x32x16_bf16 v[80:95], a[244:247], v[188:191], v[80:95]\n"
        "s_barrier\n"
        "ds_read_b128 a[236:239], v2 offset:18432\n"
        "s_waitcnt lgkmcnt(5)\n"
        "v_mfma_f32_32x32x16_bf16 v[48:63], a[248:251], v[196:199], v[48:63]\n"
        "s_waitcnt vmcnt(8)\n"
        "v_max3_u32 v29, v204, v205, v206\n"
        "v_max3_u32 v29, v207, v208, v29\n"
        "s_add_u32 m0, s41, 0x24000\n"
        "s_sub_u32 s42, s43, 0x4000\n"
        "buffer_load_dwordx4 v1, s[16:19], s42 offen offset:0 lds\n"
        "ds_read_b128 a[240:243], v1 offset:19456\n"
        "s_waitcnt lgkmcnt(5)\n"
        "v_mfma_f32_32x32x16_bf16 v[64:79], a[252:255], v[196:199], v[64:79]\n"
        "v_max3_u32 v29, v209, v210, v29\n"
        "v_max3_u32 v29, v211, v212, v29\n"
        "ds_read_b128 a[244:247], v1 offset:52224\n"
        "s_waitcnt lgkmcnt(5)\n"
        "v_mfma_f32_32x32x16_bf16 v[80:95], a[224:227], v[196:199], v[80:95]\n"
        "v_max3_u32 v29, v213, v214, v29\n"
        "v_max3_u32 v29, v215, v216, v29\n"
        "ds_read_b128 a[248:251], v2 offset:19456\n"
        "s_waitcnt lgkmcnt(5)\n"
        "v_mfma_f32_32x32x16_bf16 v[48:63], a[228:231], v[200:203], v[48:63]\n"
        "v_max3_u32 v29, v217, v218, v29\n"
        "v_max_u32 v29, v219, v29\n"
        "buffer_load_dwordx4 v1, s[16:19], s42 offen offset:1024 lds\n"
        "ds_read_b128 a[252:255], v1 offset:20480\n"
        "s_waitcnt lgkmcnt(5)\n"
        "v_mfma_f32_32x32x16_bf16 v[64:79], a[232:235], v[200:203], v[64:79]\n"
        "v_cmp_eq_u32 vcc, -1, v29\n"
        "s_nop 1\n"
        "s_cbranch_vccnz L_x5_rt\n"
        "L_x5_go:\n"
        "ds_read_b128 a[224:227], v1 offset:53248\n"
        "s_waitcnt lgkmcnt(5)\n"
        "v_mfma_f32_32x32x16_bf16 v[80:95], a[236:239], v[200:203], v[80:95]\n"
        "ds_read_b128 a[228:231], v2 offset:20480\n"
        "s_waitcnt lgkmcnt(5)\n"
        "v_mfma_f32_32x32x16_bf16 v[48:63], a[240:243], v[204:207], v[48:63]\n"
        "buffer_load_dwordx4 v1, s[16:19], s42 offen offset:2048 lds\n"
        "ds_read_b128 a[232:235], v1 offset:21504\n"
        "s_waitcnt lgkmcnt(5)\n"
        "v_mfma_f32_32x32x16_bf16 v[64:79], a[244:247], v[204:207], v[64:79]\n"
        "ds_read_b128 a[236:239], v1 offset:54272\n"
        "s_waitcnt lgkmcnt(5)\n"
        "v_mfma_f32_32x32x16_bf16 v[80:95], a[248:251], v[204:207], v[80:95]\n"
        "ds_read_b128 a[240:243], v2 offset:21504\n"
        "s_waitcnt lgkmcnt(5)\n"
        "v_mfma_f32_32x32x16_bf16 v[48:63], a[252:255], v[208:211], v[48:63]\n"
        "buffer_load_dwordx4 v1, s[16:19], s42 offen offset:3072 lds\n"
        "ds_read_b128 a[244:247], v1 offset:22528\n"
        "s_waitcnt lgkmcnt(5)\n"
        "v_mfma_f32_32x32x16_bf16 v[64:79], a[224:227], v[208:211], v[64:79]\n"
        "ds_read_b128 a[248:251], v1 offset:55296\n"
        "s_waitcnt lgkmcnt(5)\n"
        "v_mfma_f32_32x32x16_bf16 v[80:95], a[228:231], v[208:211], v[80:95]\n"
        "ds_read_b128 a[252:255], v2 offset:22528\n"
        "s_waitcnt lgkmcnt(5)\n"
        "v_mfma_f32_32x32x16_bf16 v[48:63], a[232:235], v[212:215], v[48:63]\n"
        "s_waitcnt vmcnt(8)\n"
        "v_max3_u32 v29, v220, v221, v222\n"
        "v_max3_u32 v29, v223, v228, v29\n"
        "s_add_u32 m0, s41, 0x0\n"
        "s_add_u32 s42, s43, 0x0\n"
        "buffer_load_dwordx4 v1, s[16:19], s42 offen offset:0 lds\n"
        "ds_read_b128 a[224:227], v1 offset:23552\n"
        "s_waitcnt lgkmcnt(5)\n"
        "v_mfma_f32_32x32x16_bf16 v[64:79], a[236:239], v[212:215], v[64:79]\n"
        "v_max3_u32 v29, v229, v230, v29\n"
        "v_max3_u32 v29, v231, v232, v29\n"
        "ds_read_b128 a[228:231], v1 offset:56320\n"
        "s_waitcnt lgkmcnt(5)\n"
        "v_mfma_f32_32x32x16_bf16 v[80:95], a[240:243], v[212:215], v[80:95]\n"
        "v_max3_u32 v29, v233, v234, v29\n"
        "v_max3_u32 v29, v235, v236, v29\n"
        "ds_read_b128 a[232:235], v2 offset:23552\n"
        "s_waitcnt lgkmcnt(5)\n"
        "v_mfma_f32_32x32x16_bf16 v[48:63], a[244:247], v[216:219], v[48:63]\n"
        "v_max3_u32 v29, v237, v238, v29\n"
        "v_max_u32 v29, v239, v29\n"
        "buffer_load_dwordx4 v1, s[16:19], s42 offen offset:1024 lds\n"
        "ds_read_b128 a[236:239], v1 offset:25600\n"
        "s_waitcnt lgkmcnt(5)\n"
        "v_mfma_f32_32x32x16_bf16 v[64:79], a[248:251], v[216:219], v[64:79]\n"
        "v_cmp_eq_u32 vcc, -1, v29\n"
        "s_nop 1\n"
        "s_cbranch_vccnz L_x6_rt\n"
        "L_x6_go:\n"
        "ds_read_b128 a[240:243], v1 offset:58368\n"
        "s_waitcnt lgkmcnt(5)\n"
        "v_mfma_f32_32x32x16_bf16 v[80:95], a[252:255], v[216:219], v[80:95]\n"
        "ds_read_b128 a[244:247], v2 offset:25600\n"
        "s_waitcnt lgkmcnt(5)\n"
        "v_mfma_f32_32x32x16_bf16 v[48:63], a[224:227], v[220:223], v[48:63]\n"
        "buffer_load_dwordx4 v1, s[16:19], s42 offen offset:2048 lds\n"
        "ds_read_b128 a[248:251], v1 offset:26624\n"
        "s_waitcnt lgkmcnt(5)\n"
        "v_mfma_f32_32x32x16_bf16 v[64:79], a[228:231], v[220:223], v[64:79]\n"
        "ds_read_b128 a[252:255], v1 offset:59392\n"
        "s_waitcnt lgkmcnt(5)\n"
        "v_mfma_f32_32x32x16_bf16 v[80:95], a[232:235], v[220:223], v[80:95]\n"
        "ds_read_b128 a[224:227], v2 offset:26624\n"
        "s_waitcnt lgkmcnt(5)\n"
        "v_mfma_f32_32x32x16_bf16 v[48:63], a[236:239], v[228:231], v[48:63]\n"
        "buffer_load_dwordx4 v1, s[16:19], s42 offen offset:3072 lds\n"
        "ds_read_b128 a[228:231], v1 offset:27648\n"
        "s_waitcnt lgkmcnt(5)\n"
        "v_mfma_f32_32x32x16_bf16 v[64:79], a[240:243], v[228:231], v[64:79]\n"
        "ds_read_b128 a[232:235], v1 offset:60416\n"
        "s_waitcnt lgkmcnt(5)\n"
        "v_mfma_f32_32x32x16_bf16 v[80:95], a[244:247], v[228:231], v[80:95]\n"
        "ds_read_b128 a[236:239], v2 offset:27648\n"
        "s_mov_b32 s37, 0xb12000\n"
        "buffer_store_dwordx4 v[32:35], v28, s[16:19], s37 offen offset:0 sc1\n"
        "s_waitcnt lgkmcnt(5)\n"
        "v_mfma_f32_32x32x16_bf16 v[48:63], a[248:251], v[232:235], v[48:63]\n"
        "s_waitcnt vmcnt(9)\n"
        "v_max3_u32 v29, v240, v241, v242\n"
        "v_max3_u32 v29, v243, v244, v29\n"
        "s_add_u32 m0, s41, 0x8000\n"
        "s_add_u32 s42, s43, 0x4000\n"
        "buffer_load_dwordx4 v1, s[16:19], s42 offen offset:0 lds\n"
        "ds_read_b128 a[240:243], v1 offset:28672\n"
        "s_waitcnt lgkmcnt(5)\n"
        "v_mfma_f32_32x32x16_bf16 v[64:79], a[252:255], v[232:235], v[64:79]\n"
        "v_max3_u32 v29, v245, v246, v29\n"
        "v_max3_u32 v29, v247, v248, v29\n"
        "ds_read_b128 a[244:247], v1 offset:61440\n"
        "s_waitcnt lgkmcnt(5)\n"
        "v_mfma_f32_32x32x16_bf16 v[80:95], a[224:227], v[232:235], v[80:95]\n"
        "v_max3_u32 v29, v249, v250, v29\n"
        "v_max3_u32 v29, v251, v252, v29\n"
        "ds_read_b128 a[248:251], v2 offset:28672\n"
        "s_waitcnt lgkmcnt(5)\n"
        "v_mfma_f32_32x32x16_bf16 v[48:63], a[228:231], v[236:239], v[48:63]\n"
        "v_max3_u32 v29, v253, v254, v29\n"
        "v_max_u32 v29, v255, v29\n"
        "buffer_load_dwordx4 v1, s[16:19], s42 offen offset:1024 lds\n"
        "ds_read_b128 a[252:255], v1 offset:29696\n"
        "s_waitcnt lgkmcnt(5)\n"
        "v_mfma_f32_32x32x16_bf16 v[64:79], a[232:235], v[236:239], v[64:79]\n"
        "v_cmp_eq_u32 vcc, -1, v29\n"
        "s_nop 1\n"
        "s_cbranch_vccnz L_x7_rt\n"
        "L_x7_go:\n"
        "ds_read_b128 a[224:227], v1 offset:62464\n"
        "s_waitcnt lgkmcnt(5)\n"
        "v_mfma_f32_32x32x16_bf16 v[80:95], a[236:239], v[236:239], v[80:95]\n"
        "ds_read_b128 a[228:231], v2 offset:29696\n"
        "s_waitcnt lgkmcnt(5)\n"
        "v_mfma_f32_32x32x16_bf16 v[48:63], a[240:243], v[240:243], v[48:63]\n"
        "buffer_load_dwordx4 v1, s[16:19], s42 offen offset:2048 lds\n"
        "ds_read_b128 a[232:235], v1 offset:30720\n"
        "s_waitcnt lgkmcnt(5)\n"
        "v_mfma_f32_32x32x16_bf16 v[64:79], a[244:247], v[240:243], v[64:79]\n"
        "ds_read_b128 a[236:239], v1 offset:63488\n"
        "s_mov_b32 s37, 0xb12000\n"
        "buffer_store_dwordx4 v[36:39], v28, s[16:19], s37 offen offset:2048 sc1\n"
        "s_waitcnt lgkmcnt(5)\n"
        "v_mfma_f32_32x32x16_bf16 v[80:95], a[248:251], v[240:243], v[80:95]\n"
        "ds_read_b128 a[240:243], v2 offset:30720\n"
        "s_waitcnt lgkmcnt(5)\n"
        "v_mfma_f32_32x32x16_bf16 v[48:63], a[252:255], v[244:247], v[48:63]\n"
        "buffer_load_dwordx4 v1, s[16:19], s42 offen offset:3072 lds\n"
        "ds_read_b128 a[244:247], v1 offset:31744\n"
        "s_waitcnt lgkmcnt(5)\n"
        "v_mfma_f32_32x32x16_bf16 v[64:79], a[224:227], v[244:247], v[64:79]\n"
        "ds_read_b128 a[248:251], v1 offset:64512\n"
        "s_waitcnt lgkmcnt(5)\n"
        "v_mfma_f32_32x32x16_bf16 v[80:95], a[228:231], v[244:247], v[80:95]\n"
        "ds_read_b128 a[252:255], v2 offset:31744\n"
        "s_waitcnt lgkmcnt(5)\n"
        "v_mfma_f32_32x32x16_bf16 v[48:63], a[232:235], v[248:251], v[48:63]\n"
        "s_add_u32 m0, s41, 0x10000\n"
        "s_add_u32 s42, s43, 0x8000\n"
        "buffer_load_dwordx4 v1, s[16:19], s42 offen offset:0 lds\n"
        "ds_read_b128 a[224:227], v2 offset:32768\n"
        "s_waitcnt lgkmcnt(5)\n"
        "v_mfma_f32_32x32x16_bf16 v[64:79], a[236:239], v[248:251], v[64:79]\n"
        "ds_read_b128 a[228:231], v2 offset:33792\n"
        "s_waitcnt lgkmcnt(5)\n"
        "v_mfma_f32_32x32x16_bf16 v[80:95], a[240:243], v[248:251], v[80:95]\n"
        "ds_read_b128 a[232:235], v2 offset:34816\n"
        "s_waitcnt lgkmcnt(5)\n"
        "v_mfma_f32_32x32x16_bf16 v[48:63], a[244:247], v[252:255], v[48:63]\n"
        "buffer_load_dwordx4 v1, s[16:19], s42 offen offset:1024 lds\n"
        "ds_read_b128 a[236:239], v2 offset:35840\n"
        "s_waitcnt lgkmcnt(5)\n"
        "v_mfma_f32_32x32x16_bf16 v[64:79], a[248:251], v[252:255], v[64:79]\n"
        "ds_read_b128 a[240:243], v2 offset:36864\n"
        "s_waitcnt lgkmcnt(5)\n"
        "v_mfma_f32_32x32x16_bf16 v[80:95], a[252:255], v[252:255], v[80:95]\n"
        "s_waitcnt vmcnt(12)\n"
        "s_barrier\n"
        "ds_read_b128 a[244:247], v2 offset:37888\n"
        "s_waitcnt lgkmcnt(5)\n"
        "v_mfma_f32_32x32x16_bf16 v[96:111], a[224:227], v[128:131], 0\n"
        "buffer_load_dwordx4 v1, s[16:19], s42 offen offset:2048 lds\n"
        "ds_read_b128 a[248:251], v2 offset:38912\n"
        "s_waitcnt lgkmcnt(5)\n"
        "v_mfma_f32_32x32x16_bf16 v[96:111], a[228:231], v[132:135], v[96:111]\n"
        "ds_read_b128 a[252:255], v2 offset:39936\n"
        "s_waitcnt lgkmcnt(5)\n"
        "v_mfma_f32_32x32x16_bf16 v[96:111], a[232:235], v[136:139], v[96:111]\n"
        "ds_read_b128 a[224:227], v2 offset:40960\n"
        "v_mul_f32 v8, 0xbfb8aa3b, v48\n"
        "v_mul_f32 v9, 0xbfb8aa3b, v49\n"
        "v_mul_f32 v10, 0xbfb8aa3b, v50\n"
        "v_mul_f32 v11, 0xbfb8aa3b, v51\n"
        "s_waitcnt lgkmcnt(5)\n"
        "v_mfma_f32_32x32x16_bf16 v[96:111], a[236:239], v[140:143], v[96:111]\n"
        "buffer_load_dwordx4 v1, s[16:19], s42 offen offset:3072 lds\n"
        "ds_read_b128 a[228:231], v2 offset:41984\n"
        "v_exp_f32 v8, v8\n"
        "s_waitcnt lgkmcnt(5)\n"
        "v_mfma_f32_32x32x16_bf16 v[96:111], a[240:243], v[144:147], v[96:111]\n"
        "ds_read_b128 a[232:235], v2 offset:43008\n"
        "v_exp_f32 v9, v9\n"
        "v_exp_f32 v10, v10\n"
        "s_waitcnt lgkmcnt(5)\n"
        "v_mfma_f32_32x32x16_bf16 v[96:111], a[244:247], v[148:151], v[96:111]\n"
        "ds_read_b128 a[236:239], v2 offset:44032\n"
        "v_exp_f32 v11, v11\n"
        "v_mul_f32 v56, v48, v56\n"
        "v_mul_f32 v57, v49, v57\n"
        "s_waitcnt lgkmcnt(5)\n"
        "v_mfma_f32_32x32x16_bf16 v[96:111], a[248:251], v[152:155], v[96:111]\n"
        "s_add_u32 m0, s41, 0x4000\n"
        "s_add_u32 s42, s43, 0xc000\n"
        "buffer_load_dwordx4 v1, s[16:19], s42 offen offset:0 lds\n"
        "ds_read_b128 a[240:243], v2 offset:45056\n"
        "v_mul_f32 v58, v50, v58\n"
        "v_mul_f32 v59, v51, v59\n"
        "s_waitcnt lgkmcnt(5)\n"
        "v_mfma_f32_32x32x16_bf16 v[96:111], a[252:255], v[156:159], v[96:111]\n"
        "ds_read_b128 a[244:247], v2 offset:46080\n"
        "v_add_f32 v8, 1.0, v8\n"
        "v_add_f32 v9, 1.0, v9\n"
        "v_add_f32 v10, 1.0, v10\n"
        "v_add_f32 v11, 1.0, v11\n"
        "s_waitcnt lgkmcnt(5)\n"
        "v_mfma_f32_32x32x16_bf16 v[96:111], a[224:227], v[160:163], v[96:111]\n"
        "ds_read_b128 a[248:251], v2 offset:47104\n"
        "v_rcp_f32 v8, v8\n"
        "v_rcp_f32 v9, v9\n"
        "s_waitcnt lgkmcnt(5)\n"
        "v_mfma_f32_32x32x16_bf16 v[96:111], a[228:231], v[164:167], v[96:111]\n"
        "ds_read_b128 a[252:255], v2 offset:48128\n"
        "v_rcp_f32 v10, v10\n"
        "v_rcp_f32 v11, v11\n"
        "s_waitcnt lgkmcnt(5)\n"
        "v_mfma_f32_32x32x16_bf16 v[96:111], a[232:235], v[168:171], v[96:111]\n"
        "buffer_load_dwordx4 v1, s[16:19], s42 offen offset:1024 lds\n"
        "ds_read_b128 a[224:227], v2 offset:49152\n"
        "v_mul_f32 v56, v56, v8\n"
        "v_mul_f32 v57, v57, v9\n"
        "s_waitcnt lgkmcnt(5)\n"
        "v_mfma_f32_32x32x16_bf16 v[96:111], a[236:239], v[172:175], v[96:111]\n"
        "ds_read_b128 a[228:231], v2 offset:50176\n"
        "v_mul_f32 v58, v58, v10\n"
        "v_mul_f32 v59, v59, v11\n"
        "v_mul_f32 v8, 0xbfb8aa3b, v52\n"
        "v_mul_f32 v9, 0xbfb8aa3b, v53\n"
        "s_waitcnt lgkmcnt(5)\n"
        "v_mfma_f32_32x32x16_bf16 v[96:111], a[240:243], v[176:179], v[96:111]\n"
        "ds_read_b128 a[232:235], v2 offset:51200\n"
        "v_mul_f32 v10, 0xbfb8aa3b, v54\n"
        "v_mul_f32 v11, 0xbfb8aa3b, v55\n"
        "v_exp_f32 v8, v8\n"
        "s_waitcnt lgkmcnt(5)\n"
        "v_mfma_f32_32x32x16_bf16 v[96:111], a[244:247], v[180:183], v[96:111]\n"
        "ds_read_b128 a[236:239], v2 offset:52224\n"
        "v_exp_f32 v9, v9\n"
        "v_exp_f32 v10, v10\n"
        "s_waitcnt lgkmcnt(5)\n"
        "v_mfma_f32_32x32x16_bf16 v[96:111], a[248:251], v[184:187], v[96:111]\n"
        "buffer_load_dwordx4 v1, s[16:19], s42 offen offset:2048 lds\n"
        "ds_read_b128 a[240:243], v2 offset:53248\n"
        "v_exp_f32 v11, v11\n"
        "s_waitcnt lgkmcnt(5)\n"
        "v_mfma_f32_32x32x16_bf16 v[96:111], a[252:255], v[188:191], v[96:111]\n"
        "ds_read_b128 a[244:247], v2 offset:54272\n"
        "v_mul_f32 v60, v52, v60\n"
        "v_mul_f32 v61, v53, v61\n"
        "v_mul_f32 v62, v54, v62\n"
        "v_mul_f32 v63, v55, v63\n"
        "s_waitcnt lgkmcnt(5)\n"
        "v_mfma_f32_32x32x16_bf16 v[96:111], a[224:227], v[192:195], v[96:111]\n"
        "ds_read_b128 a[248:251], v2 offset:55296\n"
        "v_add_f32 v8, 1.0, v8\n"
        "v_add_f32 v9, 1.0, v9\n"
        "v_add_f32 v10, 1.0, v10\n"
        "v_add_f32 v11, 1.0, v11\n"
        "s_waitcnt lgkmcnt(5)\n"
        "v_mfma_f32_32x32x16_bf16 v[96:111], a[228:231], v[196:199], v[96:111]\n"
        "ds_read_b128 a[252:255], v2 offset:56320\n"
        "v_rcp_f32 v8, v8\n"
        "v_rcp_f32 v9, v9\n"
        "s_waitcnt lgkmcnt(5)\n"
        "v_mfma_f32_32x32x16_bf16 v[96:111], a[232:235], v[200:203], v[96:111]\n"
        "buffer_load_dwordx4 v1, s[16:19], s42 offen offset:3072 lds\n"
        "ds_read_b128 a[224:227], v2 offset:57344\n"
        "buffer_load_dwordx4 a[88:91], v1, s[16:19], s66 offen offset:2048\n"
        "s_waitcnt lgkmcnt(5)\n"
        "v_mfma_f32_32x32x16_bf16 v[96:111], a[236:239], v[204:207], v[96:111]\n"
        "ds_read_b128 a[228:231], v2 offset:58368\n"
        "v_rcp_f32 v10, v10\n"
        "v_rcp_f32 v11, v11\n"
        "s_waitcnt lgkmcnt(5)\n"
        "v_mfma_f32_32x32x16_bf16 v[96:111], a[240:243], v[208:211], v[96:111]\n"
        "ds_read_b128 a[232:235], v2 offset:59392\n"
        "v_mul_f32 v60, v60, v8\n"
        "v_mul_f32 v61, v61, v9\n"
        "v_mul_f32 v62, v62, v10\n"
        "v_mul_f32 v63, v63, v11\n"
        "s_waitcnt lgkmcnt(5)\n"
        "v_mfma_f32_32x32x16_bf16 v[96:111], a[244:247], v[212:215], v[96:111]\n"
        "ds_read_b128 a[236:239], v2 offset:60416\n"
        "v_cvt_pk_bf16_f32 v12, v56, v57\n"
        "v_cvt_pk_bf16_f32 v13, v58, v59\n"
        "v_cvt_pk_bf16_f32 v14, v60, v61\n"
        "v_cvt_pk_bf16_f32 v15, v62, v63\n"
        "s_waitcnt lgkmcnt(5)\n"
        "v_mfma_f32_32x32x16_bf16 v[96:111], a[248:251], v[216:219], v[96:111]\n"
        "ds_read_b128 a[240:243], v2 offset:61440\n"
        "buffer_load_dwordx4 a[92:95], v1, s[16:19], s67 offen offset:2048\n"
        "buffer_store_dwordx4 v[12:15], v1, s[16:19], s56 offen offset:0 sc1\n"
        "s_waitcnt lgkmcnt(5)\n"
        "v_mfma_f32_32x32x16_bf16 v[96:111], a[252:255], v[220:223], v[96:111]\n"
        "ds_read_b128 a[244:247], v2 offset:62464\n"
        "v_mul_f32 v20, 0xbfb8aa3b, v64\n"
        "v_mul_f32 v21, 0xbfb8aa3b, v65\n"
        "v_mul_f32 v22, 0xbfb8aa3b, v66\n"
        "v_mul_f32 v23, 0xbfb8aa3b, v67\n"
        "s_waitcnt lgkmcnt(5)\n"
        "v_mfma_f32_32x32x16_bf16 v[96:111], a[224:227], v[224:227], v[96:111]\n"
        "ds_read_b128 a[248:251], v2 offset:63488\n"
        "v_exp_f32 v20, v20\n"
        "v_exp_f32 v21, v21\n"
        "s_waitcnt lgkmcnt(5)\n"
        "v_mfma_f32_32x32x16_bf16 v[96:111], a[228:231], v[228:231], v[96:111]\n"
        "ds_read_b128 a[252:255], v2 offset:64512\n"
        "v_exp_f32 v22, v22\n"
        "v_exp_f32 v23, v23\n"
        "s_waitcnt lgkmcnt(5)\n"
        "v_mfma_f32_32x32x16_bf16 v[96:111], a[232:235], v[232:235], v[96:111]\n"
        "ds_read_b128 a[224:227], v3 offset:0\n"
        "buffer_load_dwordx4 a[104:107], v1, s[16:19], s62 offen offset:3072\n"
        "v_mul_f32 v72, v64, v72\n"
        "v_mul_f32 v73, v65, v73\n"
        "s_waitcnt lgkmcnt(5)\n"
        "v_mfma_f32_32x32x16_bf16 v[96:111], a[236:239], v[236:239], v[96:111]\n"
        "ds_read_b128 a[228:231], v3 offset:1024\n"
        "v_mul_f32 v74, v66, v74\n"
        "v_mul_f32 v75, v67, v75\n"
        "v_add_f32 v20, 1.0, v20\n"
        "v_add_f32 v21, 1.0, v21\n"
        "s_waitcnt lgkmcnt(5)\n"
        "v_mfma_f32_32x32x16_bf16 v[96:111], a[240:243], v[240:243], v[96:111]\n"
        "ds_read_b128 a[232:235], v3 offset:2048\n"
        "v_add_f32 v22, 1.0, v22\n"
        "v_add_f32 v23, 1.0, v23\n"
        "v_rcp_f32 v20, v20\n"
        "s_waitcnt lgkmcnt(5)\n"
        "v_mfma_f32_32x32x16_bf16 v[96:111], a[244:247], v[244:247], v[96:111]\n"
        "ds_read_b128 a[236:239], v3 offset:3072\n"
        "v_rcp_f32 v21, v21\n"
        "v_rcp_f32 v22, v22\n"
        "s_waitcnt lgkmcnt(5)\n"
        "v_mfma_f32_32x32x16_bf16 v[96:111], a[248:251], v[248:251], v[96:111]\n"
        "ds_read_b128 a[240:243], v3 offset:4096\n"
        "buffer_load_dwordx4 a[108:111], v1, s[16:19], s63 offen offset:3072\n"
        "v_rcp_f32 v23, v23\n"
        "s_waitcnt lgkmcnt(5)\n"
        "v_mfma_f32_32x32x16_bf16 v[96:111], a[252:255], v[252:255], v[96:111]\n"
        "ds_read_b128 a[244:247], v3 offset:5120\n"
        "v_mul_f32 v72, v72, v20\n"
        "v_mul_f32 v73, v73, v21\n"
        "v_mul_f32 v74, v74, v22\n"
        "v_mul_f32 v75, v75, v23\n"
        "s_waitcnt lgkmcnt(5)\n"
        "v_mfma_f32_32x32x16_bf16 v[112:127], a[224:227], v[128:131], 0\n"
        "ds_read_b128 a[248:251], v3 offset:6144\n"
        "v_mul_f32 v20, 0xbfb8aa3b, v68\n"
        "v_mul_f32 v21, 0xbfb8aa3b, v69\n"
        "v_mul_f32 v22, 0xbfb8aa3b, v70\n"
        "v_mul_f32 v23, 0xbfb8aa3b, v71\n"
        "s_waitcnt lgkmcnt(5)\n"
        "v_mfma_f32_32x32x16_bf16 v[112:127], a[228:231], v[132:135], v[112:127]\n"
        "ds_read_b128 a[252:255], v3 offset:7168\n"
        "v_exp_f32 v20, v20\n"
        "v_exp_f32 v21, v21\n"
        "s_waitcnt lgkmcnt(5)\n"
        "v_mfma_f32_32x32x16_bf16 v[112:127], a[232:235], v[136:139], v[112:127]\n"
        "ds_read_b128 a[224:227], v3 offset:8192\n"
        "buffer_load_dwordx4 a[112:115], v1, s[16:19], s64 offen offset:3072\n"
        "v_exp_f32 v22, v22\n"
        "s_waitcnt lgkmcnt(5)\n"
        "v_mfma_f32_32x32x16_bf16 v[112:127], a[236:239], v[140:143], v[112:127]\n"
        "ds_read_b128 a[228:231], v3 offset:9216\n"
        "v_exp_f32 v23, v23\n"
        "v_mul_f32 v76, v68, v76\n"
        "v_mul_f32 v77, v69, v77\n"
        "s_waitcnt lgkmcnt(5)\n"
        "v_mfma_f32_32x32x16_bf16 v[112:127], a[240:243], v[144:147], v[112:127]\n"
        "ds_read_b128 a[232:235], v3 offset:10240\n"
        "v_mul_f32 v78, v70, v78\n"
        "v_mul_f32 v79, v71, v79\n"
        "v_add_f32 v20, 1.0, v20\n"
        "v_add_f32 v21, 1.0, v21\n"
        "s_waitcnt lgkmcnt(5)\n"
        "v_mfma_f32_32x32x16_bf16 v[112:127], a[244:247], v[148:151], v[112:127]\n"
        "ds_read_b128 a[236:239], v3 offset:11264\n"
        "v_add_f32 v22, 1.0, v22\n"
        "v_add_f32 v23, 1.0, v23\n"
        "v_rcp_f32 v20, v20\n"
        "s_waitcnt lgkmcnt(5)\n"
        "v_mfma_f32_32x32x16_bf16 v[112:127], a[248:251], v[152:155], v[112:127]\n"
        "ds_read_b128 a[240:243], v3 offset:12288\n"
        "buffer_load_dwordx4 a[116:119], v1, s[16:19], s65 offen offset:3072\n"
        "v_rcp_f32 v21, v21\n"
        "s_waitcnt lgkmcnt(5)\n"
        "v_mfma_f32_32x32x16_bf16 v[112:127], a[252:255], v[156:159], v[112:127]\n"
        "ds_read_b128 a[244:247], v3 offset:13312\n"
        "v_rcp_f32 v22, v22\n"
        "v_rcp_f32 v23, v23\n"
        "s_waitcnt lgkmcnt(5)\n"
        "v_mfma_f32_32x32x16_bf16 v[112:127], a[224:227], v[160:163], v[112:127]\n"
        "ds_read_b128 a[248:251], v3 offset:14336\n"
        "v_mul_f32 v76, v76, v20\n"
        "v_mul_f32 v77, v77, v21\n"
        "v_mul_f32 v78, v78, v22\n"
        "v_mul_f32 v79, v79, v23\n"
        "s_waitcnt lgkmcnt(5)\n"
        "v_mfma_f32_32x32x16_bf16 v[112:127], a[228:231], v[164:167], v[112:127]\n"
        "ds_read_b128 a[252:255], v3 offset:15360\n"
        "v_cvt_pk_bf16_f32 v24, v72, v73\n"
        "v_cvt_pk_bf16_f32 v25, v74, v75\n"
        "v_cvt_pk_bf16_f32 v26, v76, v77\n"
        "v_cvt_pk_bf16_f32 v27, v78, v79\n"
        "s_waitcnt lgkmcnt(5)\n"
        "v_mfma_f32_32x32x16_bf16 v[112:127], a[232:235], v[168:171], v[112:127]\n"
        "ds_read_b128 a[224:227], v3 offset:16384\n"
        "buffer_load_dwordx4 a[120:123], v1, s[16:19], s66 offen offset:3072\n"
        "buffer_store_dwordx4 v[24:27], v1, s[16:19], s56 offen offset:1024 sc1\n"
        "s_waitcnt lgkmcnt(5)\n"
        "v_mfma_f32_32x32x16_bf16 v[112:127], a[236:239], v[172:175], v[112:127]\n"
        "ds_read_b128 a[228:231], v3 offset:17408\n"
        "v_mul_f32 v8, 0xbfb8aa3b, v80\n"
        "v_mul_f32 v9, 0xbfb8aa3b, v81\n"
        "v_mul_f32 v10, 0xbfb8aa3b, v82\n"
        "v_mul_f32 v11, 0xbfb8aa3b, v83\n"
        "s_waitcnt lgkmcnt(5)\n"
        "v_mfma_f32_32x32x16_bf16 v[112:127], a[240:243], v[176:179], v[112:127]\n"
        "ds_read_b128 a[232:235], v3 offset:18432\n"
        "v_exp_f32 v8, v8\n"
        "v_exp_f32 v9, v9\n"
        "s_waitcnt lgkmcnt(5)\n"
        "v_mfma_f32_32x32x16_bf16 v[112:127], a[244:247], v[180:183], v[112:127]\n"
        "ds_read_b128 a[236:239], v3 offset:19456\n"
        "v_exp_f32 v10, v10\n"
        "v_exp_f32 v11, v11\n"
        "s_waitcnt lgkmcnt(5)\n"
        "v_mfma_f32_32x32x16_bf16 v[112:127], a[248:251], v[184:187], v[112:127]\n"
        "ds_read_b128 a[240:243], v3 offset:20480\n"
        "buffer_load_dwordx4 a[124:127], v1, s[16:19], s67 offen offset:3072\n"
        "v_mul_f32 v88, v80, v88\n"
        "v_mul_f32 v89, v81, v89\n"
        "s_waitcnt lgkmcnt(5)\n"
        "v_mfma_f32_32x32x16_bf16 v[112:127], a[252:255], v[188:191], v[112:127]\n"
        "ds_read_b128 a[244:247], v3 offset:21504\n"
        "v_mul_f32 v90, v82, v90\n"
        "v_mul_f32 v91, v83, v91\n"
        "v_add_f32 v8, 1.0, v8\n"
        "v_add_f32 v9, 1.0, v9\n"
        "s_waitcnt lgkmcnt(5)\n"
        "v_mfma_f32_32x32x16_bf16 v[112:127], a[224:227], v[192:195], v[112:127]\n"
        "ds_read_b128 a[248:251], v3 offset:22528\n"
        "v_add_f32 v10, 1.0, v10\n"
        "v_add_f32 v11, 1.0, v11\n"
        "v_rcp_f32 v8, v8\n"
        "s_waitcnt lgkmcnt(5)\n"
        "v_mfma_f32_32x32x16_bf16 v[112:127], a[228:231], v[196:199], v[112:127]\n"
        "ds_read_b128 a[252:255], v3 offset:23552\n"
        "v_rcp_f32 v9, v9\n"
        "v_rcp_f32 v10, v10\n"
        "s_waitcnt lgkmcnt(5)\n"
        "v_mfma_f32_32x32x16_bf16 v[112:127], a[232:235], v[200:203], v[112:127]\n"
        "ds_read_b128 a[224:227], v3 offset:24576\n"
        "buffer_load_dwordx4 a[136:139], v47, s[16:19], s62 offen offset:0\n"
        "v_rcp_f32 v11, v11\n"
        "s_waitcnt lgkmcnt(5)\n"
        "v_mfma_f32_32x32x16_bf16 v[112:127], a[236:239], v[204:207], v[112:127]\n"
        "ds_read_b128 a[228:231], v3 offset:25600\n"
        "v_mul_f32 v88, v88, v8\n"
        "v_mul_f32 v89, v89, v9\n"
        "v_mul_f32 v90, v90, v10\n"
        "v_mul_f32 v91, v91, v11\n"
        "s_waitcnt lgkmcnt(5)\n"
        "v_mfma_f32_32x32x16_bf16 v[112:127], a[240:243], v[208:211], v[112:127]\n"
        "ds_read_b128 a[232:235], v3 offset:26624\n"
        "v_mul_f32 v8, 0xbfb8aa3b, v84\n"
        "v_mul_f32 v9, 0xbfb8aa3b, v85\n"
        "v_mul_f32 v10, 0xbfb8aa3b, v86\n"
        "v_mul_f32 v11, 0xbfb8aa3b, v87\n"
        "s_waitcnt lgkmcnt(5)\n"
        "v_mfma_f32_32x32x16_bf16 v[112:127], a[244:247], v[212:215], v[112:127]\n"
        "ds_read_b128 a[236:239], v3 offset:27648\n"
        "v_exp_f32 v8, v8\n"
        "v_exp_f32 v9, v9\n"
        "s_waitcnt lgkmcnt(5)\n"
        "v_mfma_f32_32x32x16_bf16 v[112:127], a[248:251], v[216:219], v[112:127]\n"
        "ds_read_b128 a[240:243], v3 offset:28672\n"
        "buffer_load_dwordx4 a[140:143], v47, s[16:19], s63 offen offset:0\n"
        "v_exp_f32 v10, v10\n"
        "s_waitcnt lgkmcnt(5)\n"
        "v_mfma_f32_32x32x16_bf16 v[112:127], a[252:255], v[220:223], v[112:127]\n"
        "ds_read_b128 a[244:247], v3 offset:29696\n"
        "v_exp_f32 v11, v11\n"
        "v_mul_f32 v92, v84, v92\n"
        "v_mul_f32 v93, v85, v93\n"
        "s_waitcnt lgkmcnt(5)\n"
        "v_mfma_f32_32x32x16_bf16 v[112:127], a[224:227], v[224:227], v[112:127]\n"
        "ds_read_b128 a[248:251], v3 offset:30720\n"
        "v_mul_f32 v94, v86, v94\n"
        "v_mul_f32 v95, v87, v95\n"
        "v_add_f32 v8, 1.0, v8\n"
        "v_add_f32 v9, 1.0, v9\n"
        "s_waitcnt lgkmcnt(5)\n"
        "v_mfma_f32_32x32x16_bf16 v[112:127], a[228:231], v[228:231], v[112:127]\n"
        "ds_read_b128 a[252:255], v3 offset:31744\n"
        "v_add_f32 v10, 1.0, v10\n"
        "v_add_f32 v11, 1.0, v11\n"
        "v_rcp_f32 v8, v8\n"
        "s_waitcnt lgkmcnt(5)\n"
        "v_mfma_f32_32x32x16_bf16 v[112:127], a[232:235], v[232:235], v[112:127]\n"
        "s_waitcnt vmcnt(12)\n"
        "s_barrier\n"
        "ds_read_b128 a[224:227], v1 offset:0\n"
        "buffer_load_dwordx4 a[144:147], v47, s[16:19], s64 offen offset:0\n"
        "v_rcp_f32 v9, v9\n"
        "s_waitcnt lgkmcnt(5)\n"
        "v_mfma_f32_32x32x16_bf16 v[112:127], a[236:239], v[236:239], v[112:127]\n"
        "ds_read_b128 a[228:231], v1 offset:1024\n"
        "v_rcp_f32 v10, v10\n"
        "v_rcp_f32 v11, v11\n"
        "s_waitcnt lgkmcnt(5)\n"
        "v_mfma_f32_32x32x16_bf16 v[112:127], a[240:243], v[240:243], v[112:127]\n"
        "ds_read_b128 a[232:235], v1 offset:2048\n"
        "v_mul_f32 v92, v92, v8\n"
        "v_mul_f32 v93, v93, v9\n"
        "v_mul_f32 v94, v94, v10\n"
        "v_mul_f32 v95, v95, v11\n"
        "s_waitcnt lgkmcnt(5)\n"
        "v_mfma_f32_32x32x16_bf16 v[112:127], a[244:247], v[244:247], v[112:127]\n"
        "ds_read_b128 a[236:239], v1 offset:3072\n"
        "v_cvt_pk_bf16_f32 v12, v88, v89\n"
        "v_cvt_pk_bf16_f32 v13, v90, v91\n"
        "v_cvt_pk_bf16_f32 v14, v92, v93\n"
        "v_cvt_pk_bf16_f32 v15, v94, v95\n"
        "s_waitcnt lgkmcnt(5)\n"
        "v_mfma_f32_32x32x16_bf16 v[112:127], a[248:251], v[248:251], v[112:127]\n"
        "ds_read_b128 a[240:243], v1 offset:4096\n"
        "buffer_load_dwordx4 a[148:151], v47, s[16:19], s65 offen offset:0\n"
        "buffer_store_dwordx4 v[12:15], v1, s[16:19], s56 offen offset:2048 sc1\n"
        "s_waitcnt lgkmcnt(5)\n"
        "v_mfma_f32_32x32x16_bf16 v[112:127], a[252:255], v[252:255], v[112:127]\n"
        "ds_read_b128 a[244:247], v1 offset:5120\n"
        "v_mul_f32 v20, 0xbfb8aa3b, v96\n"
        "v_mul_f32 v21, 0xbfb8aa3b, v97\n"
        "v_mul_f32 v22, 0xbfb8aa3b, v98\n"
        "v_mul_f32 v23, 0xbfb8aa3b, v99\n"
        "s_waitcnt lgkmcnt(5)\n"
        "v_mfma_f32_32x32x16_bf16 v[48:63], a[224:227], v[128:131], 0\n"
        "ds_read_b128 a[248:251], v1 offset:6144\n"
        "v_exp_f32 v20, v20\n"
        "v_exp_f32 v21, v21\n"
        "s_waitcnt lgkmcnt(5)\n"
        "v_mfma_f32_32x32x16_bf16 v[48:63], a[228:231], v[132:135], v[48:63]\n"
        "ds_read_b128 a[252:255], v1 offset:7168\n"
        "v_exp_f32 v22, v22\n"
        "v_exp_f32 v23, v23\n"
        "s_waitcnt lgkmcnt(5)\n"
        "v_mfma_f32_32x32x16_bf16 v[48:63], a[232:235], v[136:139], v[48:63]\n"
        "ds_read_b128 a[224:227], v1 offset:8192\n"
        "buffer_load_dwordx4 a[152:155], v47, s[16:19], s66 offen offset:0\n"
        "v_mul_f32 v104, v96, v104\n"
        "v_mul_f32 v105, v97, v105\n"
        "s_waitcnt lgkmcnt(5)\n"
        "v_mfma_f32_32x32x16_bf16 v[48:63], a[236:239], v[140:143], v[48:63]\n"
        "ds_read_b128 a[228:231], v1 offset:9216\n"
        "v_mul_f32 v106, v98, v106\n"
        "v_mul_f32 v107, v99, v107\n"
        "v_add_f32 v20, 1.0, v20\n"
        "v_add_f32 v21, 1.0, v21\n"
        "s_waitcnt lgkmcnt(5)\n"
        "v_mfma_f32_32x32x16_bf16 v[48:63], a[240:243], v[144:147], v[48:63]\n"
        "ds_read_b128 a[232:235], v1 offset:10240\n"
        "v_add_f32 v22, 1.0, v22\n"
        "v_add_f32 v23, 1.0, v23\n"
        "v_rcp_f32 v20, v20\n"
        "s_waitcnt lgkmcnt(5)\n"
        "v_mfma_f32_32x32x16_bf16 v[48:63], a[244:247], v[148:151], v[48:63]\n"
        "ds_read_b128 a[236:239], v1 offset:11264\n"
        "v_rcp_f32 v21, v21\n"
        "v_rcp_f32 v22, v22\n"
        "s_waitcnt lgkmcnt(5)\n"
        "v_mfma_f32_32x32x16_bf16 v[48:63], a[248:251], v[152:155], v[48:63]\n"
        "ds_read_b128 a[240:243], v1 offset:12288\n"
        "buffer_load_dwordx4 a[156:159], v47, s[16:19], s67 offen offset:0\n"
        "v_rcp_f32 v23, v23\n"
        "s_waitcnt lgkmcnt(5)\n"
        "v_mfma_f32_32x32x16_bf16 v[48:63], a[252:255], v[156:159], v[48:63]\n"
        "ds_read_b128 a[244:247], v1 offset:13312\n"
        "v_mul_f32 v104, v104, v20\n"
        "v_mul_f32 v105, v105, v21\n"
        "v_mul_f32 v106, v106, v22\n"
        "v_mul_f32 v107, v107, v23\n"
        "s_waitcnt lgkmcnt(5)\n"
        "v_mfma_f32_32x32x16_bf16 v[48:63], a[224:227], v[160:163], v[48:63]\n"
        "ds_read_b128 a[248:251], v1 offset:14336\n"
        "v_mul_f32 v20, 0xbfb8aa3b, v100\n"
        "v_mul_f32 v21, 0xbfb8aa3b, v101\n"
        "v_mul_f32 v22, 0xbfb8aa3b, v102\n"
        "v_mul_f32 v23, 0xbfb8aa3b, v103\n"
        "s_waitcnt lgkmcnt(5)\n"
        "v_mfma_f32_32x32x16_bf16 v[48:63], a[228:231], v[164:167], v[48:63]\n"
        "ds_read_b128 a[252:255], v1 offset:15360\n"
        "v_exp_f32 v20, v20\n"
        "v_exp_f32 v21, v21\n"
        "s_waitcnt lgkmcnt(5)\n"
        "v_mfma_f32_32x32x16_bf16 v[48:63], a[232:235], v[168:171], v[48:63]\n"
        "ds_read_b128 a[224:227], v1 offset:32768\n"
        "buffer_load_dwordx4 a[168:171], v47, s[16:19], s62 offen offset:1024\n"
        "v_exp_f32 v22, v22\n"
        "s_waitcnt lgkmcnt(5)\n"
        "v_mfma_f32_32x32x16_bf16 v[48:63], a[236:239], v[172:175], v[48:63]\n"
        "ds_read_b128 a[228:231], v1 offset:33792\n"
        "v_exp_f32 v23, v23\n"
        "v_mul_f32 v108, v100, v108\n"
        "v_mul_f32 v109, v101, v109\n"
        "s_waitcnt lgkmcnt(5)\n"
        "v_mfma_f32_32x32x16_bf16 v[48:63], a[240:243], v[176:179], v[48:63]\n"
        "ds_read_b128 a[232:235], v1 offset:34816\n"
        "v_mul_f32 v110, v102, v110\n"
        "v_mul_f32 v111, v103, v111\n"
        "v_add_f32 v20, 1.0, v20\n"
        "v_add_f32 v21, 1.0, v21\n"
        "s_waitcnt lgkmcnt(5)\n"
        "v_mfma_f32_32x32x16_bf16 v[48:63], a[244:247], v[180:183], v[48:63]\n"
        "ds_read_b128 a[236:239], v1 offset:35840\n"
        "v_add_f32 v22, 1.0, v22\n"
        "v_add_f32 v23, 1.0, v23\n"
        "v_rcp_f32 v20, v20\n"
        "s_waitcnt lgkmcnt(5)\n"
        "v_mfma_f32_32x32x16_bf16 v[48:63], a[248:251], v[184:187], v[48:63]\n"
        "ds_read_b128 a[240:243], v1 offset:36864\n"
        "buffer_load_dwordx4 a[172:175], v47, s[16:19], s63 offen offset:1024\n"
        "v_rcp_f32 v21, v21\n"
        "s_waitcnt lgkmcnt(5)\n"
        "v_mfma_f32_32x32x16_bf16 v[48:63], a[252:255], v[188:191], v[48:63]\n"
        "ds_read_b128 a[244:247], v1 offset:37888\n"
        "v_rcp_f32 v22, v22\n"
        "v_rcp_f32 v23, v23\n"
        "s_waitcnt lgkmcnt(5)\n"
        "v_mfma_f32_32x32x16_bf16 v[48:63], a[224:227], v[192:195], v[48:63]\n"
        "ds_read_b128 a[248:251], v1 offset:38912\n"
        "v_mul_f32 v108, v108, v20\n"
        "v_mul_f32 v109, v109, v21\n"
        "v_mul_f32 v110, v110, v22\n"
        "v_mul_f32 v111, v111, v23\n"
        "s_waitcnt lgkmcnt(5)\n"
        "v_mfma_f32_32x32x16_bf16 v[48:63], a[228:231], v[196:199], v[48:63]\n"
        "ds_read_b128 a[252:255], v1 offset:39936\n"
        "v_cvt_pk_bf16_f32 v24, v104, v105\n"
        "v_cvt_pk_bf16_f32 v25, v106, v107\n"
        "v_cvt_pk_bf16_f32 v26, v108, v109\n"
        "v_cvt_pk_bf16_f32 v27, v110, v111\n"
        "s_waitcnt lgkmcnt(5)\n"
        "v_mfma_f32_32x32x16_bf16 v[48:63], a[232:235], v[200:203], v[48:63]\n"
        "ds_read_b128 a[224:227], v1 offset:40960\n"
        "buffer_load_dwordx4 a[176:179], v47, s[16:19], s64 offen offset:1024\n"
        "v_min_u32 v24, -2, v24\n"
        "v_min_u32 v25, -2, v25\n"
        "s_waitcnt lgkmcnt(5)\n"
        "v_mfma_f32_32x32x16_bf16 v[48:63], a[236:239], v[204:207], v[48:63]\n"
        "ds_read_b128 a[228:231], v1 offset:41984\n"
        "v_min_u32 v26, -2, v26\n"
        "v_min_u32 v27, -2, v27\n"
        "buffer_store_dwordx4 v[24:27], v1, s[16:19], s56 offen offset:3072 sc1\n"
        "s_waitcnt lgkmcnt(5)\n"
        "v_mfma_f32_32x32x16_bf16 v[48:63], a[240:243], v[208:211], v[48:63]\n"
        "ds_read_b128 a[232:235], v1 offset:43008\n"
        "s_waitcnt vmcnt(6)\n"
        "s_barrier\n"
        "s_cmp_lg_u32 s28, 0\n"
        "s_cbranch_scc1 L_S1_sg\n"
        "v_mov_b32 v31, 1\n"
        "s_mov_b64 exec, 1\n"
        "buffer_atomic_add v31, off, s[16:19], s33\n"
        "s_mov_b64 exec, -1\n"
        "L_S1_sg:\n"
        "s_waitcnt lgkmcnt(5)\n"
        "v_mfma_f32_32x32x16_bf16 v[48:63], a[244:247], v[212:215], v[48:63]\n"
        "ds_read_b128 a[236:239], v1 offset:44032\n"
        "v_mul_f32 v8, 0xbfb8aa3b, v112\n"
        "v_mul_f32 v9, 0xbfb8aa3b, v113\n"
        "v_mul_f32 v10, 0xbfb8aa3b, v114\n"
        "v_mul_f32 v11, 0xbfb8aa3b, v115\n"
        "s_waitcnt lgkmcnt(5)\n"
        "v_mfma_f32_32x32x16_bf16 v[48:63], a[248:251], v[216:219], v[48:63]\n"
        "ds_read_b128 a[240:243], v1 offset:45056\n"
        "buffer_load_dwordx4 a[180:183], v47, s[16:19], s65 offen offset:1024\n"
        "v_exp_f32 v8, v8\n"
        "s_waitcnt lgkmcnt(5)\n"
        "v_mfma_f32_32x32x16_bf16 v[48:63], a[252:255], v[220:223], v[48:63]\n"
        "ds_read_b128 a[244:247], v1 offset:46080\n"
        "v_exp_f32 v9, v9\n"
        "v_exp_f32 v10, v10\n"
        "s_waitcnt lgkmcnt(5)\n"
        "v_mfma_f32_32x32x16_bf16 v[48:63], a[224:227], v[224:227], v[48:63]\n"
        "ds_read_b128 a[248:251], v1 offset:47104\n"
        "v_exp_f32 v11, v11\n"
        "v_mul_f32 v120, v112, v120\n"
        "v_mul_f32 v121, v113, v121\n"
        "s_waitcnt lgkmcnt(5)\n"
        "v_mfma_f32_32x32x16_bf16 v[48:63], a[228:231], v[228:231], v[48:63]\n"
        "ds_read_b128 a[252:255], v1 offset:48128\n"
        "v_mul_f32 v122, v114, v122\n"
        "v_mul_f32 v123, v115, v123\n"
        "v_add_f32 v8, 1.0, v8\n"
        "v_add_f32 v9, 1.0, v9\n"
        "s_waitcnt lgkmcnt(5)\n"
        "v_mfma_f32_32x32x16_bf16 v[48:63], a[232:235], v[232:235], v[48:63]\n"
        "ds_read_b128 a[224:227], v2 offset:0\n"
        "buffer_load_dwordx4 a[184:187], v47, s[16:19], s66 offen offset:1024\n"
        "v_add_f32 v10, 1.0, v10\n"
        "v_add_f32 v11, 1.0, v11\n"
        "s_waitcnt lgkmcnt(5)\n"
        "v_mfma_f32_32x32x16_bf16 v[48:63], a[236:239], v[236:239], v[48:63]\n"
        "ds_read_b128 a[228:231], v2 offset:1024\n"
        "v_rcp_f32 v8, v8\n"
        "v_rcp_f32 v9, v9\n"
        "s_waitcnt lgkmcnt(5)\n"
        "v_mfma_f32_32x32x16_bf16 v[48:63], a[240:243], v[240:243], v[48:63]\n"
        "ds_read_b128 a[232:235], v2 offset:2048\n"
        "v_rcp_f32 v10, v10\n"
        "v_rcp_f32 v11, v11\n"
        "s_waitcnt lgkmcnt(5)\n"
        "v_mfma_f32_32x32x16_bf16 v[48:63], a[244:247], v[244:247], v[48:63]\n"
        "ds_read_b128 a[236:239], v2 offset:3072\n"
        "v_mul_f32 v120, v120, v8\n"
        "v_mul_f32 v121, v121, v9\n"
        "v_mul_f32 v122, v122, v10\n"
        "v_mul_f32 v123, v123, v11\n"
        "s_waitcnt lgkmcnt(5)\n"
        "v_mfma_f32_32x32x16_bf16 v[48:63], a[248:251], v[248:251], v[48:63]\n"
        "ds_read_b128 a[240:243], v2 offset:4096\n"
        "buffer_load_dwordx4 a[188:191], v47, s[16:19], s67 offen offset:1024\n"
        "v_mul_f32 v8, 0xbfb8aa3b, v116\n"
        "v_mul_f32 v9, 0xbfb8aa3b, v117\n"
        "s_waitcnt lgkmcnt(5)\n"
        "v_mfma_f32_32x32x16_bf16 v[48:63], a[252:255], v[252:255], v[48:63]\n"
        "ds_read_b128 a[244:247], v2 offset:5120\n"
        "v_mul_f32 v10, 0xbfb8aa3b, v118\n"
        "v_mul_f32 v11, 0xbfb8aa3b, v119\n"
        "v_exp_f32 v8, v8\n"
        "s_waitcnt lgkmcnt(5)\n"
        "v_mfma_f32_32x32x16_bf16 v[64:79], a[224:227], v[128:131], 0\n"
        "ds_read_b128 a[248:251], v2 offset:6144\n"
        "v_exp_f32 v9, v9\n"
        "v_exp_f32 v10, v10\n"
        "s_waitcnt lgkmcnt(5)\n"
        "v_mfma_f32_32x32x16_bf16 v[64:79], a[228:231], v[132:135], v[64:79]\n"
        "ds_read_b128 a[252:255], v2 offset:7168\n"
        "v_exp_f32 v11, v11\n"
        "v_mul_f32 v124, v116, v124\n"
        "v_mul_f32 v125, v117, v125\n"
        "s_waitcnt lgkmcnt(5)\n"
        "v_mfma_f32_32x32x16_bf16 v[64:79], a[232:235], v[136:139], v[64:79]\n"
        "ds_read_b128 a[224:227], v2 offset:8192\n"
        "buffer_load_dwordx4 a[200:203], v47, s[16:19], s62 offen offset:2048\n"
        "v_mul_f32 v126, v118, v126\n"
        "v_mul_f32 v127, v119, v127\n"
        "s_waitcnt lgkmcnt(5)\n"
        "v_mfma_f32_32x32x16_bf16 v[64:79], a[236:239], v[140:143], v[64:79]\n"
        "ds_read_b128 a[228:231], v2 offset:9216\n"
        "v_add_f32 v8, 1.0, v8\n"
        "v_add_f32 v9, 1.0, v9\n"
        "v_add_f32 v10, 1.0, v10\n"
        "v_add_f32 v11, 1.0, v11\n"
        "s_waitcnt lgkmcnt(5)\n"
        "v_mfma_f32_32x32x16_bf16 v[64:79], a[240:243], v[144:147], v[64:79]\n"
        "ds_read_b128 a[232:235], v2 offset:10240\n"
        "v_rcp_f32 v8, v8\n"
        "v_rcp_f32 v9, v9\n"
        "s_waitcnt lgkmcnt(5)\n"
        "v_mfma_f32_32x32x16_bf16 v[64:79], a[244:247], v[148:151], v[64:79]\n"
        "ds_read_b128 a[236:239], v2 offset:11264\n"
        "v_rcp_f32 v10, v10\n"
        "v_rcp_f32 v11, v11\n"
        "s_waitcnt lgkmcnt(5)\n"
        "v_mfma_f32_32x32x16_bf16 v[64:79], a[248:251], v[152:155], v[64:79]\n"
        "ds_read_b128 a[240:243], v2 offset:12288\n"
        "buffer_load_dwordx4 a[204:207], v47, s[16:19], s63 offen offset:2048\n"
        "v_mul_f32 v124, v124, v8\n"
        "v_mul_f32 v125, v125, v9\n"
        "s_waitcnt lgkmcnt(5)\n"
        "v_mfma_f32_32x32x16_bf16 v[64:79], a[252:255], v[156:159], v[64:79]\n"
        "ds_read_b128 a[244:247], v2 offset:13312\n"
        "v_mul_f32 v126, v126, v10\n"
        "v_mul_f32 v127, v127, v11\n"
        "v_cvt_pk_bf16_f32 v12, v120, v121\n"
        "v_cvt_pk_bf16_f32 v13, v122, v123\n"
        "s_waitcnt lgkmcnt(5)\n"
        "v_mfma_f32_32x32x16_bf16 v[64:79], a[224:227], v[160:163], v[64:79]\n"
        "ds_read_b128 a[248:251], v2 offset:14336\n"
        "v_cvt_pk_bf16_f32 v14, v124, v125\n"
        "v_cvt_pk_bf16_f32 v15, v126, v127\n"
        "v_min_u32 v12, -2, v12\n"
        "v_min_u32 v13, -2, v13\n"
        "s_waitcnt lgkmcnt(5)\n"
        "v_mfma_f32_32x32x16_bf16 v[64:79], a[228:231], v[164:167], v[64:79]\n"
        "ds_read_b128 a[252:255], v2 offset:15360\n"
        "v_min_u32 v14, -2, v14\n"
        "v_min_u32 v15, -2, v15\n"
        "buffer_store_dwordx4 v[12:15], v1, s[16:19], s57 offen offset:0 sc1\n"
        "s_waitcnt lgkmcnt(5)\n"
        "v_mfma_f32_32x32x16_bf16 v[64:79], a[232:235], v[168:171], v[64:79]\n"
        "ds_read_b128 a[224:227], v1 offset:16384\n"
        "buffer_load_dwordx4 a[208:211], v47, s[16:19], s64 offen offset:2048\n"
        "v_mul_f32 v20, 0xbfb8aa3b, v48\n"
        "v_mul_f32 v21, 0xbfb8aa3b, v49\n"
        "s_waitcnt lgkmcnt(5)\n"
        "v_mfma_f32_32x32x16_bf16 v[64:79], a[236:239], v[172:175], v[64:79]\n"
        "ds_read_b128 a[228:231], v1 offset:17408\n"
        "v_mul_f32 v22, 0xbfb8aa3b, v50\n"
        "v_mul_f32 v23, 0xbfb8aa3b, v51\n"
        "v_exp_f32 v20, v20\n"
        "s_waitcnt lgkmcnt(5)\n"
        "v_mfma_f32_32x32x16_bf16 v[64:79], a[240:243], v[176:179], v[64:79]\n"
        "ds_read_b128 a[232:235], v1 offset:18432\n"
        "v_exp_f32 v21, v21\n"
        "v_exp_f32 v22, v22\n"
        "s_waitcnt lgkmcnt(5)\n"
        "v_mfma_f32_32x32x16_bf16 v[64:79], a[244:247], v[180:183], v[64:79]\n"
        "ds_read_b128 a[236:239], v1 offset:19456\n"
        "v_exp_f32 v23, v23\n"
        "v_mul_f32 v56, v48, v56\n"
        "v_mul_f32 v57, v49, v57\n"
        "s_waitcnt lgkmcnt(5)\n"
        "v_mfma_f32_32x32x16_bf16 v[64:79], a[248:251], v[184:187], v[64:79]\n"
        "ds_read_b128 a[240:243], v1 offset:20480\n"
        "buffer_load_dwordx4 a[212:215], v47, s[16:19], s65 offen offset:2048\n"
        "v_mul_f32 v58, v50, v58\n"
        "v_mul_f32 v59, v51, v59\n"
        "s_waitcnt lgkmcnt(5)\n"
        "v_mfma_f32_32x32x16_bf16 v[64:79], a[252:255], v[188:191], v[64:79]\n"
        "ds_read_b128 a[244:247], v1 offset:21504\n"
        "v_add_f32 v20, 1.0, v20\n"
        "v_add_f32 v21, 1.0, v21\n"
        "v_add_f32 v22, 1.0, v22\n"
        "v_add_f32 v23, 1.0, v23\n"
        "s_waitcnt lgkmcnt(5)\n"
        "v_mfma_f32_32x32x16_bf16 v[64:79], a[224:227], v[192:195], v[64:79]\n"
        "ds_read_b128 a[248:251], v1 offset:22528\n"
        "v_rcp_f32 v20, v20\n"
        "v_rcp_f32 v21, v21\n"
        "s_waitcnt lgkmcnt(5)\n"
        "v_mfma_f32_32x32x16_bf16 v[64:79], a[228:231], v[196:199], v[64:79]\n"
        "ds_read_b128 a[252:255], v1 offset:23552\n"
        "v_rcp_f32 v22, v22\n"
        "v_rcp_f32 v23, v23\n"
        "s_waitcnt lgkmcnt(5)\n"
        "v_mfma_f32_32x32x16_bf16 v[64:79], a[232:235], v[200:203], v[64:79]\n"
        "ds_read_b128 a[224:227], v1 offset:24576\n"
        "buffer_load_dwordx4 a[216:219], v47, s[16:19], s66 offen offset:2048\n"
        "v_mul_f32 v56, v56, v20\n"
        "v_mul_f32 v57, v57, v21\n"
        "s_waitcnt lgkmcnt(5)\n"
        "v_mfma_f32_32x32x16_bf16 v[64:79], a[236:239], v[204:207], v[64:79]\n"
        "ds_read_b128 a[228:231], v1 offset:25600\n"
        "v_mul_f32 v58, v58, v22\n"
        "v_mul_f32 v59, v59, v23\n"
        "v_mul_f32 v20, 0xbfb8aa3b, v52\n"
        "v_mul_f32 v21, 0xbfb8aa3b, v53\n"
        "s_waitcnt lgkmcnt(5)\n"
        "v_mfma_f32_32x32x16_bf16 v[64:79], a[240:243], v[208:211], v[64:79]\n"
        "ds_read_b128 a[232:235], v1 offset:26624\n"
        "v_mul_f32 v22, 0xbfb8aa3b, v54\n"
        "v_mul_f32 v23, 0xbfb8aa3b, v55\n"
        "v_exp_f32 v20, v20\n"
        "s_waitcnt lgkmcnt(5)\n"
        "v_mfma_f32_32x32x16_bf16 v[64:79], a[244:247], v[212:215], v[64:79]\n"
        "ds_read_b128 a[236:239], v1 offset:27648\n"
        "v_exp_f32 v21, v21\n"
        "v_exp_f32 v22, v22\n"
        "s_waitcnt lgkmcnt(5)\n"
        "v_mfma_f32_32x32x16_bf16 v[64:79], a[248:251], v[216:219], v[64:79]\n"
        "ds_read_b128 a[240:243], v1 offset:28672\n"
        "buffer_load_dwordx4 a[220:223], v47, s[16:19], s67 offen offset:2048\n"
        "v_exp_f32 v23, v23\n"
        "s_waitcnt lgkmcnt(5)\n"
        "v_mfma_f32_32x32x16_bf16 v[64:79], a[252:255], v[220:223], v[64:79]\n"
        "ds_read_b128 a[244:247], v1 offset:29696\n"
        "v_mul_f32 v60, v52, v60\n"
        "v_mul_f32 v61, v53, v61\n"
        "v_mul_f32 v62, v54, v62\n"
        "v_mul_f32 v63, v55, v63\n"
        "s_waitcnt lgkmcnt(5)\n"
        "v_mfma_f32_32x32x16_bf16 v[64:79], a[224:227], v[224:227], v[64:79]\n"
        "ds_read_b128 a[248:251], v1 offset:30720\n"
        "v_add_f32 v20, 1.0, v20\n"
        "v_add_f32 v21, 1.0, v21\n"
        "v_add_f32 v22, 1.0, v22\n"
        "v_add_f32 v23, 1.0, v23\n"
        "s_waitcnt lgkmcnt(5)\n"
        "v_mfma_f32_32x32x16_bf16 v[64:79], a[228:231], v[228:231], v[64:79]\n"
        "ds_read_b128 a[252:255], v1 offset:31744\n"
        "v_rcp_f32 v20, v20\n"
        "v_rcp_f32 v21, v21\n"
        "s_waitcnt lgkmcnt(5)\n"
        "v_mfma_f32_32x32x16_bf16 v[64:79], a[232:235], v[232:235], v[64:79]\n"
        "v_rcp_f32 v22, v22\n"
        "v_rcp_f32 v23, v23\n"
        "s_waitcnt lgkmcnt(4)\n"
        "v_mfma_f32_32x32x16_bf16 v[64:79], a[236:239], v[236:239], v[64:79]\n"
        "v_mul_f32 v60, v60, v20\n"
        "v_mul_f32 v61, v61, v21\n"
        "v_mul_f32 v62, v62, v22\n"
        "v_mul_f32 v63, v63, v23\n"
        "s_waitcnt lgkmcnt(3)\n"
        "v_mfma_f32_32x32x16_bf16 v[64:79], a[240:243], v[240:243], v[64:79]\n"
        "v_cvt_pk_bf16_f32 v24, v56, v57\n"
        "v_cvt_pk_bf16_f32 v25, v58, v59\n"
        "v_cvt_pk_bf16_f32 v26, v60, v61\n"
        "v_cvt_pk_bf16_f32 v27, v62, v63\n"
        "s_waitcnt lgkmcnt(2)\n"
        "v_mfma_f32_32x32x16_bf16 v[64:79], a[244:247], v[244:247], v[64:79]\n"
        "v_min_u32 v24, -2, v24\n"
        "v_min_u32 v25, -2, v25\n"
        "v_min_u32 v26, -2, v26\n"
        "v_min_u32 v27, -2, v27\n"
        "s_waitcnt lgkmcnt(1)\n"
        "v_mfma_f32_32x32x16_bf16 v[64:79], a[248:251], v[248:251], v[64:79]\n"
        "buffer_store_dwordx4 v[24:27], v1, s[16:19], s57 offen offset:1024 sc1\n"
        "s_waitcnt lgkmcnt(0)\n"
        "v_mfma_f32_32x32x16_bf16 v[64:79], a[252:255], v[252:255], v[64:79]\n"
        "buffer_load_dword v30, off, s[16:19], s33 sc1\n"
        "s_lshr_b32 s49, s30, 3\n"
        "s_mul_i32 s49, s49, 7168\n"
        "s_add_u32 s60, s49, s58\n"
        "s_add_u32 s61, s49, s59\n"
        "s_lshr_b32 s49, s30, 3\n"
        "s_add_u32 s49, s49, 1\n"
        "s_and_b32 s49, s49, 3\n"
        "s_mul_i32 s49, s49, 7168\n"
        "s_add_u32 s62, s49, s58\n"
        "s_add_u32 s63, s49, s59\n"
        "s_lshr_b32 s49, s30, 3\n"
        "s_add_u32 s49, s49, 2\n"
        "s_and_b32 s49, s49, 3\n"
        "s_mul_i32 s49, s49, 7168\n"
        "s_add_u32 s64, s49, s58\n"
        "s_add_u32 s65, s49, s59\n"
        "s_lshr_b32 s49, s30, 3\n"
        "s_add_u32 s49, s49, 3\n"
        "s_and_b32 s49, s49, 3\n"
        "s_mul_i32 s49, s49, 7168\n"
        "s_add_u32 s66, s49, s58\n"
        "s_add_u32 s67, s49, s59\n"
        "v_mul_f32 v8, 0xbfb8aa3b, v64\n"
        "v_mul_f32 v9, 0xbfb8aa3b, v65\n"
        "v_mul_f32 v10, 0xbfb8aa3b, v66\n"
        "v_mul_f32 v11, 0xbfb8aa3b, v67\n"
        "v_exp_f32 v8, v8\n"
        "v_exp_f32 v9, v9\n"
        "v_exp_f32 v10, v10\n"
        "v_exp_f32 v11, v11\n"
        "v_mul_f32 v72, v64, v72\n"
        "v_mul_f32 v73, v65, v73\n"
        "v_mul_f32 v74, v66, v74\n"
        "v_mul_f32 v75, v67, v75\n"
        "v_add_f32 v8, 1.0, v8\n"
        "v_add_f32 v9, 1.0, v9\n"
        "v_add_f32 v10, 1.0, v10\n"
        "v_add_f32 v11, 1.0, v11\n"
        "v_rcp_f32 v8, v8\n"
        "v_rcp_f32 v9, v9\n"
        "v_rcp_f32 v10, v10\n"
        "v_rcp_f32 v11, v11\n"
        "v_mul_f32 v72, v72, v8\n"
        "v_mul_f32 v73, v73, v9\n"
        "v_mul_f32 v74, v74, v10\n"
        "v_mul_f32 v75, v75, v11\n"
        "v_mul_f32 v8, 0xbfb8aa3b, v68\n"
        "v_mul_f32 v9, 0xbfb8aa3b, v69\n"
        "v_mul_f32 v10, 0xbfb8aa3b, v70\n"
        "v_mul_f32 v11, 0xbfb8aa3b, v71\n"
        "v_exp_f32 v8, v8\n"
        "v_exp_f32 v9, v9\n"
        "v_exp_f32 v10, v10\n"
        "v_exp_f32 v11, v11\n"
        "v_mul_f32 v76, v68, v76\n"
        "v_mul_f32 v77, v69, v77\n"
        "v_mul_f32 v78, v70, v78\n"
        "v_mul_f32 v79, v71, v79\n"
        "v_add_f32 v8, 1.0, v8\n"
        "v_add_f32 v9, 1.0, v9\n"
        "v_add_f32 v10, 1.0, v10\n"
        "v_add_f32 v11, 1.0, v11\n"
        "v_rcp_f32 v8, v8\n"
        "v_rcp_f32 v9, v9\n"
        "v_rcp_f32 v10, v10\n"
        "v_rcp_f32 v11, v11\n"
        "v_mul_f32 v76, v76, v8\n"
        "v_mul_f32 v77, v77, v9\n"
        "v_mul_f32 v78, v78, v10\n"
        "v_mul_f32 v79, v79, v11\n"
        "v_cvt_pk_bf16_f32 v12, v72, v73\n"
        "v_cvt_pk_bf16_f32 v13, v74, v75\n"
        "v_cvt_pk_bf16_f32 v14, v76, v77\n"
        "v_cvt_pk_bf16_f32 v15, v78, v79\n"
        "v_min_u32 v12, -2, v12\n"
        "v_min_u32 v13, -2, v13\n"
        "v_min_u32 v14, -2, v14\n"
        "v_min_u32 v15, -2, v15\n"
        "buffer_store_dwordx4 v[12:15], v1, s[16:19], s57 offen offset:2048 sc1\n"
        "s_waitcnt vmcnt(1)\n"
        "s_mov_b32 s46, 0x40000\n"
        "L_P1_chk:\n"
        "v_readfirstlane_b32 s47, v30\n"
        "s_nop 0\n"
        "s_cmp_ge_u32 s47, 16\n"
        "s_cbranch_scc1 L_P1_ok\n"
        "buffer_load_dword v30, off, s[16:19], s33 sc1\n"
        "s_waitcnt vmcnt(0)\n"
        "s_sub_u32 s46, s46, 1\n"
        "s_cmp_lg_u32 s46, 0\n"
        "s_cbranch_scc1 L_P1_chk\n"
        "L_P1_ok:\n"
        "buffer_load_dwordx4 v[128:131], v1, s[16:19], s60 offen offset:0 sc1\n"
        "buffer_load_dwordx4 v[132:135], v1, s[16:19], s61 offen offset:0 sc1\n"
        "buffer_load_dwordx4 v[136:139], v1, s[16:19], s60 offen offset:1024 sc1\n"
        "buffer_load_dwordx4 v[140:143], v1, s[16:19], s61 offen offset:1024 sc1\n"
        "buffer_load_dwordx4 v[144:147], v1, s[16:19], s60 offen offset:2048 sc1\n"
        "buffer_load_dwordx4 v[148:151], v1, s[16:19], s61 offen offset:2048 sc1\n"
        "buffer_load_dwordx4 v[152:155], v1, s[16:19], s62 offen offset:0 sc1\n"
        "buffer_load_dwordx4 v[156:159], v1, s[16:19], s63 offen offset:0 sc1\n"
        "s_barrier\n"
        "s_lshl_b32 s39, s51, 15\n"
        "s_lshl_b32 s41, s40, 11\n"
        "s_add_u32 s39, s39, s41\n"
        "s_add_u32 s39, s39, 0xb12000\n"
        "buffer_load_dwordx4 v[32:35], v1, s[16:19], s39 offen sc1\n"
        "buffer_load_dwordx4 v[36:39], v1, s[16:19], s39 offen offset:1024 sc1\n"
        "s_xor_b32 s38, s28, 1\n"
        "s_lshl_b32 s38, s38, 15\n"
        "s_add_u32 s38, s38, 0\n"
        "v_add_u32 v4, s38, v1\n"
        "s_xor_b32 s38, s28, 2\n"
        "s_lshl_b32 s38, s38, 15\n"
        "s_add_u32 s38, s38, 4096\n"
        "v_add_u32 v5, s38, v1\n"
        "s_xor_b32 s38, s28, 3\n"
        "s_lshl_b32 s38, s38, 15\n"
        "s_add_u32 s38, s38, 8192\n"
        "v_add_u32 v6, s38, v1\n"
        "s_lshl_b32 s38, s28, 15\n"
        "v_add_u32 v7, s38, v1\n"
        "s_mov_b64 s[20:21], s[16:17]\n"
        "s_cmp_eq_u32 s31, 0\n"
        "s_cselect_b32 s22, s18, 0\n"
        "s_and_b32 s39, s30, 7\n"
        "s_lshl_b32 s38, s39, 16\n"
        "s_lshl_b32 s39, s28, 14\n"
        "s_add_u32 s38, s38, s39\n"
        "s_add_u32 s42, s38, 0x152000\n"
        "s_lshl_b32 s43, s28, 15\n"
        "s_add_u32 s43, s43, 0x4000\n"
        "s_waitcnt vmcnt(9)\n"
        "v_mfma_f32_32x32x16_bf16 v[192:207], a[0:3], v[128:131], 0\n"
        "v_mfma_f32_32x32x16_bf16 v[224:239], a[4:7], v[128:131], 0\n"
        "buffer_load_dwordx4 v[160:163], v1, s[16:19], s62 offen offset:1024 sc1\n"
        "s_waitcnt vmcnt(9)\n"
        "v_mfma_f32_32x32x16_bf16 v[208:223], a[0:3], v[132:135], 0\n"
        "v_mfma_f32_32x32x16_bf16 v[240:255], a[4:7], v[132:135], 0\n"
        "buffer_load_dwordx4 v[164:167], v1, s[16:19], s63 offen offset:1024 sc1\n"
        "buffer_load_dwordx4 v[168:171], v1, s[16:19], s62 offen offset:2048 sc1\n"
        "s_waitcnt vmcnt(10)\n"
        "v_mfma_f32_32x32x16_bf16 v[192:207], a[8:11], v[136:139], v[192:207]\n"
        "v_mfma_f32_32x32x16_bf16 v[224:239], a[12:15], v[136:139], v[224:239]\n"
        "buffer_load_dwordx4 v[172:175], v1, s[16:19], s63 offen offset:2048 sc1\n"
        "buffer_load_dwordx4 v[176:179], v1, s[16:19], s64 offen offset:0 sc1\n"
        "s_waitcnt vmcnt(11)\n"
        "v_mfma_f32_32x32x16_bf16 v[208:223], a[8:11], v[140:143], v[208:223]\n"
        "v_mfma_f32_32x32x16_bf16 v[240:255], a[12:15], v[140:143], v[240:255]\n"
        "buffer_load_dwordx4 v[180:183], v1, s[16:19], s65 offen offset:0 sc1\n"
        "buffer_load_dwordx4 v[184:187], v1, s[16:19], s64 offen offset:1024 sc1\n"
        "s_waitcnt vmcnt(12)\n"
        "v_mfma_f32_32x32x16_bf16 v[192:207], a[16:19], v[144:147], v[192:207]\n"
        "v_mfma_f32_32x32x16_bf16 v[224:239], a[20:23], v[144:147], v[224:239]\n"
        "buffer_load_dwordx4 v[188:191], v1, s[16:19], s65 offen offset:1024 sc1\n"
        "buffer_load_dwordx4 v[48:51], v1, s[16:19], s64 offen offset:2048 sc1\n"
        "s_waitcnt vmcnt(13)\n"
        "v_mfma_f32_32x32x16_bf16 v[208:223], a[16:19], v[148:151], v[208:223]\n"
        "v_mfma_f32_32x32x16_bf16 v[240:255], a[20:23], v[148:151], v[240:255]\n"
        "buffer_load_dwordx4 v[52:55], v1, s[16:19], s65 offen offset:2048 sc1\n"
        "buffer_load_dwordx4 v[56:59], v1, s[16:19], s66 offen offset:0 sc1\n"
        "s_waitcnt vmcnt(14)\n"
        "v_mfma_f32_32x32x16_bf16 v[192:207], a[24:27], v[152:155], v[192:207]\n"
        "v_mfma_f32_32x32x16_bf16 v[224:239], a[28:31], v[152:155], v[224:239]\n"
        "buffer_load_dwordx4 v[60:63], v1, s[16:19], s67 offen offset:0 sc1\n"
        "buffer_load_dwordx4 v[64:67], v1, s[16:19], s66 offen offset:1024 sc1\n"
        "s_waitcnt vmcnt(15)\n"
        "v_mfma_f32_32x32x16_bf16 v[208:223], a[24:27], v[156:159], v[208:223]\n"
        "v_mfma_f32_32x32x16_bf16 v[240:255], a[28:31], v[156:159], v[240:255]\n"
        "buffer_load_dwordx4 v[68:71], v1, s[16:19], s67 offen offset:1024 sc1\n"
        "buffer_load_dwordx4 v[72:75], v1, s[16:19], s66 offen offset:2048 sc1\n"
        "s_waitcnt vmcnt(14)\n"
        "v_mfma_f32_32x32x16_bf16 v[192:207], a[32:35], v[160:163], v[192:207]\n"
        "v_mfma_f32_32x32x16_bf16 v[224:239], a[36:39], v[160:163], v[224:239]\n"
        "buffer_load_dwordx4 v[76:79], v1, s[16:19], s67 offen offset:2048 sc1\n"
        "buffer_load_dwordx4 v[80:83], v1, s[16:19], s60 offen offset:3072 sc1\n"
        "s_waitcnt vmcnt(15)\n"
        "v_mfma_f32_32x32x16_bf16 v[208:223], a[32:35], v[164:167], v[208:223]\n"
        "v_mfma_f32_32x32x16_bf16 v[240:255], a[36:39], v[164:167], v[240:255]\n"
        "buffer_load_dwordx4 v[84:87], v1, s[16:19], s61 offen offset:3072 sc1\n"
        "s_waitcnt vmcnt(15)\n"
        "v_mfma_f32_32x32x16_bf16 v[192:207], a[40:43], v[168:171], v[192:207]\n"
        "v_mfma_f32_32x32x16_bf16 v[224:239], a[44:47], v[168:171], v[224:239]\n"
        "buffer_load_dwordx4 v[88:91], v1, s[16:19], s62 offen offset:3072 sc1\n"
        "s_waitcnt vmcnt(15)\n"
        "v_mfma_f32_32x32x16_bf16 v[208:223], a[40:43], v[172:175], v[208:223]\n"
        "v_mfma_f32_32x32x16_bf16 v[240:255], a[44:47], v[172:175], v[240:255]\n"
        "buffer_load_dwordx4 v[92:95], v1, s[16:19], s63 offen offset:3072 sc1\n"
        "s_waitcnt vmcnt(15)\n"
        "v_mfma_f32_32x32x16_bf16 v[192:207], a[48:51], v[176:179], v[192:207]\n"
        "v_mfma_f32_32x32x16_bf16 v[224:239], a[52:55], v[176:179], v[224:239]\n"
        "buffer_load_dwordx4 v[96:99], v1, s[16:19], s64 offen offset:3072 sc1\n"
        "s_waitcnt vmcnt(15)\n"
        "v_mfma_f32_32x32x16_bf16 v[208:223], a[48:51], v[180:183], v[208:223]\n"
        "v_mfma_f32_32x32x16_bf16 v[240:255], a[52:55], v[180:183], v[240:255]\n"
        "buffer_load_dwordx4 v[100:103], v1, s[16:19], s65 offen offset:3072 sc1\n"
        "s_waitcnt vmcnt(15)\n"
        "v_mfma_f32_32x32x16_bf16 v[192:207], a[56:59], v[184:187], v[192:207]\n"
        "v_mfma_f32_32x32x16_bf16 v[224:239], a[60:63], v[184:187], v[224:239]\n"
        "buffer_load_dwordx4 v[104:107], v1, s[16:19], s66 offen offset:3072 sc1\n"
        "s_waitcnt vmcnt(15)\n"
        "v_mfma_f32_32x32x16_bf16 v[208:223], a[56:59], v[188:191], v[208:223]\n"
        "v_mfma_f32_32x32x16_bf16 v[240:255], a[60:63], v[188:191], v[240:255]\n"
        "buffer_load_dwordx4 v[108:111], v1, s[16:19], s67 offen offset:3072 sc1\n"
        "s_waitcnt vmcnt(15)\n"
        "v_mfma_f32_32x32x16_bf16 v[192:207], a[64:67], v[48:51], v[192:207]\n"
        "v_mfma_f32_32x32x16_bf16 v[224:239], a[68:71], v[48:51], v[224:239]\n"
        "buffer_load_dwordx4 v[112:115], v47, s[16:19], s60 offen offset:0 sc1\n"
        "s_waitcnt vmcnt(15)\n"
        "v_mfma_f32_32x32x16_bf16 v[208:223], a[64:67], v[52:55], v[208:223]\n"
        "v_mfma_f32_32x32x16_bf16 v[240:255], a[68:71], v[52:55], v[240:255]\n"
        "buffer_load_dwordx4 v[116:119], v47, s[16:19], s61 offen offset:0 sc1\n"
        "s_waitcnt vmcnt(15)\n"
        "v_mfma_f32_32x32x16_bf16 v[192:207], a[72:75], v[56:59], v[192:207]\n"
        "v_mfma_f32_32x32x16_bf16 v[224:239], a[76:79], v[56:59], v[224:239]\n"
        "buffer_load_dwordx4 v[120:123], v47, s[16:19], s62 offen offset:0 sc1\n"
        "s_waitcnt vmcnt(15)\n"
        "v_mfma_f32_32x32x16_bf16 v[208:223], a[72:75], v[60:63], v[208:223]\n"
        "v_mfma_f32_32x32x16_bf16 v[240:255], a[76:79], v[60:63], v[240:255]\n"
        "buffer_load_dwordx4 v[124:127], v47, s[16:19], s63 offen offset:0 sc1\n"
        "s_waitcnt vmcnt(15)\n"
        "v_mfma_f32_32x32x16_bf16 v[192:207], a[80:83], v[64:67], v[192:207]\n"
        "v_mfma_f32_32x32x16_bf16 v[224:239], a[84:87], v[64:67], v[224:239]\n"
        "buffer_load_dwordx4 v[128:131], v47, s[16:19], s64 offen offset:0 sc1\n"
        "s_waitcnt vmcnt(15)\n"
        "v_mfma_f32_32x32x16_bf16 v[208:223], a[80:83], v[68:71], v[208:223]\n"
        "v_mfma_f32_32x32x16_bf16 v[240:255], a[84:87], v[68:71], v[240:255]\n"
        "buffer_load_dwordx4 v[132:135], v47, s[16:19], s65 offen offset:0 sc1\n"
        "s_waitcnt vmcnt(15)\n"
        "v_mfma_f32_32x32x16_bf16 v[192:207], a[88:91], v[72:75], v[192:207]\n"
        "s_waitcnt vmcnt(10)\n"
        "v_max3_u32 v29, v80, v81, v82\n"
        "v_max3_u32 v29, v83, v84, v29\n"
        "v_max3_u32 v29, v85, v86, v29\n"
        "v_max3_u32 v29, v87, v88, v29\n"
        "v_mfma_f32_32x32x16_bf16 v[224:239], a[92:95], v[72:75], v[224:239]\n"
        "v_max3_u32 v29, v89, v90, v29\n"
        "v_max3_u32 v29, v91, v92, v29\n"
        "v_max3_u32 v29, v93, v94, v29\n"
        "v_max_u32 v29, v95, v29\n"
        "buffer_load_dwordx4 v[136:139], v47, s[16:19], s66 offen offset:0 sc1\n"
        "v_mfma_f32_32x32x16_bf16 v[208:223], a[88:91], v[76:79], v[208:223]\n"
        "v_cmp_eq_u32 vcc, -1, v29\n"
        "s_nop 1\n"
        "s_cbranch_vccnz L_h24_rt\n"
        "L_h24_go:\n"
        "v_mfma_f32_32x32x16_bf16 v[240:255], a[92:95], v[76:79], v[240:255]\n"
        "buffer_load_dwordx4 v[140:143], v47, s[16:19], s67 offen offset:0 sc1\n"
        "v_mfma_f32_32x32x16_bf16 v[192:207], a[96:99], v[80:83], v[192:207]\n"
        "v_mfma_f32_32x32x16_bf16 v[224:239], a[100:103], v[80:83], v[224:239]\n"
        "buffer_load_dwordx4 v[144:147], v47, s[16:19], s60 offen offset:1024 sc1\n"
        "v_mfma_f32_32x32x16_bf16 v[208:223], a[96:99], v[84:87], v[208:223]\n"
        "v_mfma_f32_32x32x16_bf16 v[240:255], a[100:103], v[84:87], v[240:255]\n"
        "buffer_load_dwordx4 v[148:151], v47, s[16:19], s61 offen offset:1024 sc1\n"
        "v_mfma_f32_32x32x16_bf16 v[192:207], a[104:107], v[88:91], v[192:207]\n"
        "s_waitcnt vmcnt(10)\n"
        "v_max3_u32 v29, v96, v97, v98\n"
        "v_max3_u32 v29, v99, v100, v29\n"
        "v_max3_u32 v29, v101, v102, v29\n"
        "v_max3_u32 v29, v103, v104, v29\n"
        "v_mfma_f32_32x32x16_bf16 v[224:239], a[108:111], v[88:91], v[224:239]\n"
        "v_max3_u32 v29, v105, v106, v29\n"
        "v_max3_u32 v29, v107, v108, v29\n"
        "v_max3_u32 v29, v109, v110, v29\n"
        "v_max_u32 v29, v111, v29\n"
        "buffer_load_dwordx4 v[152:155], v47, s[16:19], s62 offen offset:1024 sc1\n"
        "v_mfma_f32_32x32x16_bf16 v[208:223], a[104:107], v[92:95], v[208:223]\n"
        "v_cmp_eq_u32 vcc, -1, v29\n"
        "s_nop 1\n"
        "s_cbranch_vccnz L_h28_rt\n"
        "L_h28_go:\n"
        "v_mfma_f32_32x32x16_bf16 v[240:255], a[108:111], v[92:95], v[240:255]\n"
        "buffer_load_dwordx4 v[156:159], v47, s[16:19], s63 offen offset:1024 sc1\n"
        "v_mfma_f32_32x32x16_bf16 v[192:207], a[112:115], v[96:99], v[192:207]\n"
        "v_mfma_f32_32x32x16_bf16 v[224:239], a[116:119], v[96:99], v[224:239]\n"
        "buffer_load_dwordx4 v[160:163], v47, s[16:19], s64 offen offset:1024 sc1\n"
        "v_mfma_f32_32x32x16_bf16 v[208:223], a[112:115], v[100:103], v[208:223]\n"
        "v_mfma_f32_32x32x16_bf16 v[240:255], a[116:119], v[100:103], v[240:255]\n"
        "buffer_load_dwordx4 v[164:167], v47, s[16:19], s65 offen offset:1024 sc1\n"
        "v_mfma_f32_32x32x16_bf16 v[192:207], a[120:123], v[104:107], v[192:207]\n"
        "s_waitcnt vmcnt(10)\n"
        "v_max3_u32 v29, v112, v113, v114\n"
        "v_max3_u32 v29, v115, v116, v29\n"
        "v_max3_u32 v29, v117, v118, v29\n"
        "v_max3_u32 v29, v119, v120, v29\n"
        "v_mfma_f32_32x32x16_bf16 v[224:239], a[124:127], v[104:107], v[224:239]\n"
        "v_max3_u32 v29, v121, v122, v29\n"
        "v_max3_u32 v29, v123, v124, v29\n"
        "v_max3_u32 v29, v125, v126, v29\n"
        "v_max_u32 v29, v127, v29\n"
        "buffer_load_dwordx4 v[168:171], v47, s[16:19], s66 offen offset:1024 sc1\n"
        "v_mfma_f32_32x32x16_bf16 v[208:223], a[120:123], v[108:111], v[208:223]\n"
        "v_cmp_eq_u32 vcc, -1, v29\n"
        "s_nop 1\n"
        "s_cbranch_vccnz L_h32_rt\n"
        "L_h32_go:\n"
        "v_mfma_f32_32x32x16_bf16 v[240:255], a[124:127], v[108:111], v[240:255]\n"
        "buffer_load_dwordx4 v[172:175], v47, s[16:19], s67 offen offset:1024 sc1\n"
        "v_mfma_f32_32x32x16_bf16 v[192:207], a[128:131], v[112:115], v[192:207]\n"
        "v_mfma_f32_32x32x16_bf16 v[224:239], a[132:135], v[112:115], v[224:239]\n"
        "buffer_load_dwordx4 v[176:179], v47, s[16:19], s60 offen offset:2048 sc1\n"
        "v_mfma_f32_32x32x16_bf16 v[208:223], a[128:131], v[116:119], v[208:223]\n"
        "v_mfma_f32_32x32x16_bf16 v[240:255], a[132:135], v[116:119], v[240:255]\n"
        "buffer_load_dwordx4 v[180:183], v47, s[16:19], s61 offen offset:2048 sc1\n"
        "v_mfma_f32_32x32x16_bf16 v[192:207], a[136:139], v[120:123], v[192:207]\n"
        "s_waitcnt vmcnt(10)\n"
        "v_max3_u32 v29, v128, v129, v130\n"
        "v_max3_u32 v29, v131, v132, v29\n"
        "v_max3_u32 v29, v133, v134, v29\n"
        "v_max3_u32 v29, v135, v136, v29\n"
        "v_mfma_f32_32x32x16_bf16 v[224:239], a[140:143], v[120:123], v[224:239]\n"
        "v_max3_u32 v29, v137, v138, v29\n"
        "v_max3_u32 v29, v139, v140, v29\n"
        "v_max3_u32 v29, v141, v142, v29\n"
        "v_max_u32 v29, v143, v29\n"
        "buffer_load_dwordx4 v[184:187], v47, s[16:19], s62 offen offset:2048 sc1\n"
        "v_mfma_f32_32x32x16_bf16 v[208:223], a[136:139], v[124:127], v[208:223]\n"
        "v_cmp_eq_u32 vcc, -1, v29\n"
        "s_nop 1\n"
        "s_cbranch_vccnz L_h36_rt\n"
        "L_h36_go:\n"
        "v_mfma_f32_32x32x16_bf16 v[240:255], a[140:143], v[124:127], v[240:255]\n"
        "buffer_load_dwordx4 v[188:191], v47, s[16:19], s63 offen offset:2048 sc1\n"
        "v_mfma_f32_32x32x16_bf16 v[192:207], a[144:147], v[128:131], v[192:207]\n"
        "v_mfma_f32_32x32x16_bf16 v[224:239], a[148:151], v[128:131], v[224:239]\n"
        "buffer_load_dwordx4 v[48:51], v47, s[16:19], s64 offen offset:2048 sc1\n"
        "v_mfma_f32_32x32x16_bf16 v[208:223], a[144:147], v[132:135], v[208:223]\n"
        "v_mfma_f32_32x32x16_bf16 v[240:255], a[148:151], v[132:135], v[240:255]\n"
        "buffer_load_dwordx4 v[52:55], v47, s[16:19], s65 offen offset:2048 sc1\n"
        "v_mfma_f32_32x32x16_bf16 v[192:207], a[152:155], v[136:139], v[192:207]\n"
        "s_waitcnt vmcnt(10)\n"
        "v_max3_u32 v29, v144, v145, v146\n"
        "v_max3_u32 v29, v147, v148, v29\n"
        "v_max3_u32 v29, v149, v150, v29\n"
        "v_max3_u32 v29, v151, v152, v29\n"
        "v_mfma_f32_32x32x16_bf16 v[224:239], a[156:159], v[136:139], v[224:239]\n"
        "v_max3_u32 v29, v153, v154, v29\n"
        "v_max3_u32 v29, v155, v156, v29\n"
        "v_max3_u32 v29, v157, v158, v29\n"
        "v_max_u32 v29, v159, v29\n"
        "buffer_load_dwordx4 v[56:59], v47, s[16:19], s66 offen offset:2048 sc1\n"
        "v_mfma_f32_32x32x16_bf16 v[208:223], a[152:155], v[140:143], v[208:223]\n"
        "v_cmp_eq_u32 vcc, -1, v29\n"
        "s_nop 1\n"
        "s_cbranch_vccnz L_h40_rt\n"
        "L_h40_go:\n"
        "v_mfma_f32_32x32x16_bf16 v[240:255], a[156:159], v[140:143], v[240:255]\n"
        "buffer_load_dwordx4 v[60:63], v47, s[16:19], s67 offen offset:2048 sc1\n"
        "v_mfma_f32_32x32x16_bf16 v[192:207], a[160:163], v[144:147], v[192:207]\n"
        "v_mfma_f32_32x32x16_bf16 v[224:239], a[164:167], v[144:147], v[224:239]\n"
        "v_mfma_f32_32x32x16_bf16 v[208:223], a[160:163], v[148:151], v[208:223]\n"
        "v_mfma_f32_32x32x16_bf16 v[240:255], a[164:167], v[148:151], v[240:255]\n"
        "s_add_u32 m0, s43, 0\n"
        "s_nop 0\n"
        "buffer_load_dwordx4 v1, s[20:23], s42 offen offset:0 lds\n"
        "v_mfma_f32_32x32x16_bf16 v[192:207], a[168:171], v[152:155], v[192:207]\n"
        "s_waitcnt vmcnt(9)\n"
        "v_max3_u32 v29, v160, v161, v162\n"
        "v_max3_u32 v29, v163, v164, v29\n"
        "v_max3_u32 v29, v165, v166, v29\n"
        "v_max3_u32 v29, v167, v168, v29\n"
        "v_mfma_f32_32x32x16_bf16 v[224:239], a[172:175], v[152:155], v[224:239]\n"
        "v_max3_u32 v29, v169, v170, v29\n"
        "v_max3_u32 v29, v171, v172, v29\n"
        "v_max3_u32 v29, v173, v174, v29\n"
        "v_max_u32 v29, v175, v29\n"
        "v_mfma_f32_32x32x16_bf16 v[208:223], a[168:171], v[156:159], v[208:223]\n"
        "v_cmp_eq_u32 vcc, -1, v29\n"
        "s_nop 1\n"
        "s_cbranch_vccnz L_h44_rt\n"
        "L_h44_go:\n"
        "v_mfma_f32_32x32x16_bf16 v[240:255], a[172:175], v[156:159], v[240:255]\n"
        "buffer_load_dwordx4 v1, s[20:23], s42 offen offset:1024 lds\n"
        "v_mfma_f32_32x32x16_bf16 v[192:207], a[176:179], v[160:163], v[192:207]\n"
        "v_mfma_f32_32x32x16_bf16 v[224:239], a[180:183], v[160:163], v[224:239]\n"
        "v_mfma_f32_32x32x16_bf16 v[208:223], a[176:179], v[164:167], v[208:223]\n"
        "v_mfma_f32_32x32x16_bf16 v[240:255], a[180:183], v[164:167], v[240:255]\n"
        "buffer_load_dwordx4 v1, s[20:23], s42 offen offset:2048 lds\n"
        "v_mfma_f32_32x32x16_bf16 v[192:207], a[184:187], v[168:171], v[192:207]\n"
        "s_waitcnt vmcnt(7)\n"
        "v_max3_u32 v29, v176, v177, v178\n"
        "v_max3_u32 v29, v179, v180, v29\n"
        "v_max3_u32 v29, v181, v182, v29\n"
        "v_max3_u32 v29, v183, v184, v29\n"
        "v_mfma_f32_32x32x16_bf16 v[224:239], a[188:191], v[168:171], v[224:239]\n"
        "v_max3_u32 v29, v185, v186, v29\n"
        "v_max3_u32 v29, v187, v188, v29\n"
        "v_max3_u32 v29, v189, v190, v29\n"
        "v_max_u32 v29, v191, v29\n"
        "v_mfma_f32_32x32x16_bf16 v[208:223], a[184:187], v[172:175], v[208:223]\n"
        "v_cmp_eq_u32 vcc, -1, v29\n"
        "s_nop 1\n"
        "s_cbranch_vccnz L_h48_rt\n"
        "L_h48_go:\n"
        "v_mfma_f32_32x32x16_bf16 v[240:255], a[188:191], v[172:175], v[240:255]\n"
        "buffer_load_dwordx4 v1, s[20:23], s42 offen offset:3072 lds\n"
        "v_mfma_f32_32x32x16_bf16 v[192:207], a[192:195], v[176:179], v[192:207]\n"
        "v_mfma_f32_32x32x16_bf16 v[224:239], a[196:199], v[176:179], v[224:239]\n"
        "v_mfma_f32_32x32x16_bf16 v[208:223], a[192:195], v[180:183], v[208:223]\n"
        "v_mfma_f32_32x32x16_bf16 v[240:255], a[196:199], v[180:183], v[240:255]\n"
        "s_add_u32 m0, s43, 4096\n"
        "s_add_u32 s42, s42, 4096\n"
        "buffer_load_dwordx4 v1, s[20:23], s42 offen offset:0 lds\n"
        "v_mfma_f32_32x32x16_bf16 v[192:207], a[200:203], v[184:187], v[192:207]\n"
        "s_waitcnt vmcnt(5)\n"
        "v_max3_u32 v29, v48, v49, v50\n"
        "v_max3_u32 v29, v51, v52, v29\n"
        "v_max3_u32 v29, v53, v54, v29\n"
        "v_max3_u32 v29, v55, v56, v29\n"
        "v_mfma_f32_32x32x16_bf16 v[224:239], a[204:207], v[184:187], v[224:239]\n"
        "v_max3_u32 v29, v57, v58, v29\n"
        "v_max3_u32 v29, v59, v60, v29\n"
        "v_max3_u32 v29, v61, v62, v29\n"
        "v_max_u32 v29, v63, v29\n"
        "v_mfma_f32_32x32x16_bf16 v[208:223], a[200:203], v[188:191], v[208:223]\n"
        "v_cmp_eq_u32 vcc, -1, v29\n"
        "s_nop 1\n"
        "s_cbranch_vccnz L_h52_rt\n"
        "L_h52_go:\n"
        "v_mfma_f32_32x32x16_bf16 v[240:255], a[204:207], v[188:191], v[240:255]\n"
        "buffer_load_dwordx4 v1, s[20:23], s42 offen offset:1024 lds\n"
        "v_mfma_f32_32x32x16_bf16 v[192:207], a[208:211], v[48:51], v[192:207]\n"
        "v_mfma_f32_32x32x16_bf16 v[224:239], a[212:215], v[48:51], v[224:239]\n"
        "v_mfma_f32_32x32x16_bf16 v[208:223], a[208:211], v[52:55], v[208:223]\n"
        "v_mfma_f32_32x32x16_bf16 v[240:255], a[212:215], v[52:55], v[240:255]\n"
        "buffer_load_dwordx4 v1, s[20:23], s42 offen offset:2048 lds\n"
        "v_mfma_f32_32x32x16_bf16 v[192:207], a[216:219], v[56:59], v[192:207]\n"
        "v_mfma_f32_32x32x16_bf16 v[224:239], a[220:223], v[56:59], v[224:239]\n"
        "v_mfma_f32_32x32x16_bf16 v[208:223], a[216:219], v[60:63], v[208:223]\n"
        "v_mfma_f32_32x32x16_bf16 v[240:255], a[220:223], v[60:63], v[240:255]\n"
        "buffer_load_dwordx4 v1, s[20:23], s42 offen offset:3072 lds\n"
        "s_nop 9\n"
        "ds_write_b128 v4, v[208:211] offset:0\n"
        "ds_write_b128 v4, v[212:215] offset:1024\n"
        "ds_write_b128 v4, v[216:219] offset:2048\n"
        "ds_write_b128 v4, v[220:223] offset:3072\n"
        "ds_write_b128 v5, v[224:227] offset:0\n"
        "ds_write_b128 v5, v[228:231] offset:1024\n"
        "ds_write_b128 v5, v[232:235] offset:2048\n"
        "ds_write_b128 v5, v[236:239] offset:3072\n"
        "ds_write_b128 v6, v[240:243] offset:0\n"
        "ds_write_b128 v6, v[244:247] offset:1024\n"
        "ds_write_b128 v6, v[248:251] offset:2048\n"
        "ds_write_b128 v6, v[252:255] offset:3072\n"
        "v_lshlrev_b32 v40, 16, v32\n"
        "v_and_b32 v41, 0xffff0000, v32\n"
        "v_lshlrev_b32 v42, 16, v33\n"
        "v_and_b32 v43, 0xffff0000, v33\n"
        "v_lshlrev_b32 v44, 16, v34\n"
        "v_and_b32 v45, 0xffff0000, v34\n"
        "v_lshlrev_b32 v46, 16, v35\n"
        "v_and_b32 v47, 0xffff0000, v35\n"
        "v_lshlrev_b32 v48, 16, v36\n"
        "v_and_b32 v49, 0xffff0000, v36\n"
        "v_lshlrev_b32 v50, 16, v37\n"
        "v_and_b32 v51, 0xffff0000, v37\n"
        "v_lshlrev_b32 v52, 16, v38\n"
        "v_and_b32 v53, 0xffff0000, v38\n"
        "v_lshlrev_b32 v54, 16, v39\n"
        "v_and_b32 v55, 0xffff0000, v39\n"
        "v_pk_add_f32 v[192:193], v[192:193], v[40:41]\n"
        "v_pk_add_f32 v[194:195], v[194:195], v[42:43]\n"
        "v_pk_add_f32 v[196:197], v[196:197], v[44:45]\n"
        "v_pk_add_f32 v[198:199], v[198:199], v[46:47]\n"
        "v_pk_add_f32 v[200:201], v[200:201], v[48:49]\n"
        "v_pk_add_f32 v[202:203], v[202:203], v[50:51]\n"
        "v_pk_add_f32 v[204:205], v[204:205], v[52:53]\n"
        "v_pk_add_f32 v[206:207], v[206:207], v[54:55]\n"
        "s_waitcnt lgkmcnt(0)\n"
        "s_barrier\n"
        "ds_read_b128 v[64:67], v7 offset:0\n"
        "ds_read_b128 v[68:71], v7 offset:1024\n"
        "ds_read_b128 v[72:75], v7 offset:2048\n"
        "ds_read_b128 v[76:79], v7 offset:3072\n"
        "ds_read_b128 v[80:83], v7 offset:4096\n"
        "ds_read_b128 v[84:87], v7 offset:5120\n"
        "ds_read_b128 v[88:91], v7 offset:6144\n"
        "ds_read_b128 v[92:95], v7 offset:7168\n"
        "ds_read_b128 v[96:99], v7 offset:8192\n"
        "ds_read_b128 v[100:103], v7 offset:9216\n"
        "ds_read_b128 v[104:107], v7 offset:10240\n"
        "ds_read_b128 v[108:111], v7 offset:11264\n"
        "s_waitcnt lgkmcnt(8)\n"
        "v_pk_add_f32 v[192:193], v[192:193], v[64:65]\n"
        "v_pk_add_f32 v[194:195], v[194:195], v[66:67]\n"
        "v_pk_add_f32 v[196:197], v[196:197], v[68:69]\n"
        "v_pk_add_f32 v[198:199], v[198:199], v[70:71]\n"
        "v_pk_add_f32 v[200:201], v[200:201], v[72:73]\n"
        "v_pk_add_f32 v[202:203], v[202:203], v[74:75]\n"
        "v_pk_add_f32 v[204:205], v[204:205], v[76:77]\n"
        "v_pk_add_f32 v[206:207], v[206:207], v[78:79]\n"
        "s_waitcnt lgkmcnt(4)\n"
        "v_pk_add_f32 v[192:193], v[192:193], v[80:81]\n"
        "v_pk_add_f32 v[194:195], v[194:195], v[82:83]\n"
        "v_pk_add_f32 v[196:197], v[196:197], v[84:85]\n"
        "v_pk_add_f32 v[198:199], v[198:199], v[86:87]\n"
        "v_pk_add_f32 v[200:201], v[200:201], v[88:89]\n"
        "v_pk_add_f32 v[202:203], v[202:203], v[90:91]\n"
        "v_pk_add_f32 v[204:205], v[204:205], v[92:93]\n"
        "v_pk_add_f32 v[206:207], v[206:207], v[94:95]\n"
        "s_waitcnt lgkmcnt(0)\n"
        "v_pk_add_f32 v[192:193], v[192:193], v[96:97]\n"
        "v_pk_add_f32 v[194:195], v[194:195], v[98:99]\n"
        "v_pk_add_f32 v[196:197], v[196:197], v[100:101]\n"
        "v_pk_add_f32 v[198:199], v[198:199], v[102:103]\n"
        "v_pk_add_f32 v[200:201], v[200:201], v[104:105]\n"
        "v_pk_add_f32 v[202:203], v[202:203], v[106:107]\n"
        "v_pk_add_f32 v[204:205], v[204:205], v[108:109]\n"
        "v_pk_add_f32 v[206:207], v[206:207], v[110:111]\n"
        "v_cvt_pk_bf16_f32 v8, v192, v193\n"
        "v_cvt_pk_bf16_f32 v9, v194, v195\n"
        "v_cvt_pk_bf16_f32 v10, v196, v197\n"
        "v_cvt_pk_bf16_f32 v11, v198, v199\n"
        "v_cvt_pk_bf16_f32 v12, v200, v201\n"
        "v_cvt_pk_bf16_f32 v13, v202, v203\n"
        "v_cvt_pk_bf16_f32 v14, v204, v205\n"
        "v_cvt_pk_bf16_f32 v15, v206, v207\n"
        "v_and_b32 v16, 31, v0\n"
        "s_lshl_b32 s38, s51, 5\n"
        "v_add_u32 v16, s38, v16\n"
        "v_lshrrev_b32 v17, 5, v0\n"
        "s_cmp_eq_u32 s31, 0\n"
        "s_cbranch_scc1 L_C_mid\n"
        "v_lshlrev_b32 v18, 10, v16\n"
        "v_lshl_add_u32 v18, v17, 4, v18\n"
        "s_lshl_b32 s38, s40, 6\n"
        "buffer_store_dwordx4 v[8:11], v18, s[24:27], s38 offen\n"
        "buffer_store_dwordx4 v[12:15], v18, s[24:27], s38 offen offset:32\n"
        "s_branch L_fin\n"
        "L_C_mid:\n"
        "v_min_u32 v8, -2, v8\n"
        "v_min_u32 v9, -2, v9\n"
        "v_min_u32 v10, -2, v10\n"
        "v_min_u32 v11, -2, v11\n"
        "v_min_u32 v12, -2, v12\n"
        "v_min_u32 v13, -2, v13\n"
        "v_min_u32 v14, -2, v14\n"
        "v_min_u32 v15, -2, v15\n"
        "v_lshrrev_b32 v18, 4, v16\n"
        "v_lshlrev_b32 v18, 14, v18\n"
        "v_lshl_add_u32 v18, v17, 8, v18\n"
        "v_and_b32 v19, 15, v16\n"
        "v_lshl_add_u32 v18, v19, 4, v18\n"
        "s_lshl_b32 s38, s40, 10\n"
        "s_add_u32 s38, s38, 0x1112000\n"
        "buffer_store_dwordx4 v[8:11], v18, s[16:19], s38 offen sc1\n"
        "buffer_store_dwordx4 v[12:15], v18, s[16:19], s38 offen offset:512 sc1\n"
        "v_mov_b32 v19, v18\n"
        "s_mov_b32 s33, s38\n"
        "v_pk_add_f32 v[56:57], v[192:193], v[194:195]\n"
        "v_pk_add_f32 v[58:59], v[196:197], v[198:199]\n"
        "v_pk_add_f32 v[60:61], v[200:201], v[202:203]\n"
        "v_pk_add_f32 v[62:63], v[204:205], v[206:207]\n"
        "v_pk_add_f32 v[56:57], v[56:57], v[58:59]\n"
        "v_pk_add_f32 v[60:61], v[60:61], v[62:63]\n"
        "v_pk_add_f32 v[56:57], v[56:57], v[60:61]\n"
        "v_add_f32 v56, v56, v57\n"
        "v_mov_b32 v57, v56\n"
        "s_nop 1\n"
        "v_permlane32_swap_b32 v56, v57\n"
        "v_add_f32 v56, v56, v57\n"
        "v_mul_f32 v56, 0x3d000000, v56\n"
        "v_pk_add_f32 v[192:193], v[192:193], v[56:57] op_sel_hi:[1,0] neg_lo:[0,1] neg_hi:[0,1]\n"
        "v_pk_add_f32 v[194:195], v[194:195], v[56:57] op_sel_hi:[1,0] neg_lo:[0,1] neg_hi:[0,1]\n"
        "v_pk_add_f32 v[196:197], v[196:197], v[56:57] op_sel_hi:[1,0] neg_lo:[0,1] neg_hi:[0,1]\n"
        "v_pk_add_f32 v[198:199], v[198:199], v[56:57] op_sel_hi:[1,0] neg_lo:[0,1] neg_hi:[0,1]\n"
        "v_pk_add_f32 v[200:201], v[200:201], v[56:57] op_sel_hi:[1,0] neg_lo:[0,1] neg_hi:[0,1]\n"
        "v_pk_add_f32 v[202:203], v[202:203], v[56:57] op_sel_hi:[1,0] neg_lo:[0,1] neg_hi:[0,1]\n"
        "v_pk_add_f32 v[204:205], v[204:205], v[56:57] op_sel_hi:[1,0] neg_lo:[0,1] neg_hi:[0,1]\n"
        "v_pk_add_f32 v[206:207], v[206:207], v[56:57] op_sel_hi:[1,0] neg_lo:[0,1] neg_hi:[0,1]\n"
        "v_pk_mul_f32 v[58:59], v[192:193], v[192:193]\n"
        "v_pk_mul_f32 v[60:61], v[194:195], v[194:195]\n"
        "v_pk_fma_f32 v[58:59], v[196:197], v[196:197], v[58:59]\n"
        "v_pk_fma_f32 v[60:61], v[198:199], v[198:199], v[60:61]\n"
        "v_pk_fma_f32 v[58:59], v[200:201], v[200:201], v[58:59]\n"
        "v_pk_fma_f32 v[60:61], v[202:203], v[202:203], v[60:61]\n"
        "v_pk_fma_f32 v[58:59], v[204:205], v[204:205], v[58:59]\n"
        "v_pk_fma_f32 v[60:61], v[206:207], v[206:207], v[60:61]\n"
        "v_pk_add_f32 v[58:59], v[58:59], v[60:61]\n"
        "v_add_f32 v57, v58, v59\n"
        "v_mov_b32 v58, v57\n"
        "s_nop 1\n"
        "v_permlane32_swap_b32 v57, v58\n"
        "v_add_f32 v57, v57, v58\n"
        "v_lshlrev_b32 v18, 3, v0\n"
        "s_lshl_b32 s38, s51, 12\n"
        "s_lshl_b32 s39, s40, 8\n"
        "s_add_u32 s38, s38, s39\n"
        "s_add_u32 s38, s38, 0x92000\n"
        "v_min_u32 v56, -2, v56\n"
        "v_min_u32 v57, -2, v57\n"
        "s_mov_b32 s46, -1\n"
        "s_mov_b32 s47, 0\n"
        "s_mov_b64 exec, s[46:47]\n"
        "buffer_store_dwordx2 v[56:57], v18, s[16:19], s38 offen sc1\n"
        "s_mov_b64 exec, -1\n"
        "v_lshrrev_b32 v17, 4, v0\n"
        "v_lshlrev_b32 v18, 5, v17\n"
        "s_and_b32 s37, s30, 7\n"
        "s_lshl_b32 s38, s37, 8\n"
        "s_add_u32 s38, s38, 0x40000\n"
        "buffer_load_dwordx4 v[96:99], v18, s[16:19], s38 offen offset:0\n"
        "buffer_load_dwordx4 v[100:103], v18, s[16:19], s38 offen offset:16\n"
        "buffer_load_dwordx4 v[104:107], v18, s[16:19], s38 offen offset:128\n"
        "buffer_load_dwordx4 v[108:111], v18, s[16:19], s38 offen offset:144\n"
        "v_lshlrev_b32 v18, 4, v17\n"
        "s_and_b32 s38, s37, 1\n"
        "s_lshl_b32 s38, s38, 6\n"
        "s_add_u32 s38, s38, 0x40800\n"
        "buffer_load_dwordx4 v[112:115], v18, s[16:19], s38 offen\n"
        "s_add_u32 m0, s43, 8192\n"
        "s_add_u32 s42, s42, 4096\n"
        "buffer_load_dwordx4 v1, s[20:23], s42 offen offset:0 lds\n"
        "buffer_load_dwordx4 v1, s[20:23], s42 offen offset:1024 lds\n"
        "buffer_load_dwordx4 v1, s[20:23], s42 offen offset:2048 lds\n"
        "buffer_load_dwordx4 v1, s[20:23], s42 offen offset:3072 lds\n"
        "s_add_u32 m0, s43, 12288\n"
        "s_add_u32 s42, s42, 4096\n"
        "buffer_load_dwordx4 v1, s[20:23], s42 offen offset:0 lds\n"
        "buffer_load_dwordx4 v1, s[20:23], s42 offen offset:1024 lds\n"
        "buffer_load_dwordx4 v1, s[20:23], s42 offen offset:2048 lds\n"
        "buffer_load_dwordx4 v1, s[20:23], s42 offen offset:3072 lds\n"
        "s_and_b32 s35, s30, 1\n"
        "s_lshl_b32 s35, s35, 14\n"
        "s_lshl_b32 s39, s28, 12\n"
        "s_add_u32 m0, s39, 0x20000\n"
        "s_add_u32 s42, s35, s39\n"
        "s_add_u32 s42, s42, 0x4a000\n"
        "buffer_load_dwordx4 v1, s[20:23], s42 offen offset:0 lds\n"
        "buffer_load_dwordx4 v1, s[20:23], s42 offen offset:1024 lds\n"
        "buffer_load_dwordx4 v1, s[20:23], s42 offen offset:2048 lds\n"
        "buffer_load_dwordx4 v1, s[20:23], s42 offen offset:3072 lds\n"
        "v_and_b32 v16, 15, v0\n"
        "s_lshl_b32 s36, s50, 6\n"
        "s_lshl_b32 s37, s28, 4\n"
        "s_add_u32 s36, s36, s37\n"
        "v_add_u32 v16, s36, v16\n"
        "v_lshrrev_b32 v18, 5, v16\n"
        "v_lshlrev_b32 v18, 12, v18\n"
        "v_lshl_add_u32 v18, v17, 10, v18\n"
        "v_and_b32 v19, 31, v16\n"
        "v_lshl_add_u32 v18, v19, 3, v18\n"
        "s_mov_b32 s38, 0x92000\n"
        "s_sleep 2\n"
        "buffer_load_dwordx2 v[8:9], v18, s[16:19], s38 offen offset:0 sc1\n"
        "buffer_load_dwordx2 v[10:11], v18, s[16:19], s38 offen offset:256 sc1\n"
        "buffer_load_dwordx2 v[12:13], v18, s[16:19], s38 offen offset:512 sc1\n"
        "buffer_load_dwordx2 v[14:15], v18, s[16:19], s38 offen offset:768 sc1\n"
        "s_lshl_b32 s35, s36, 10\n"
        "s_add_u32 s35, s35, 0x1112000\n"
        "s_add_u32 s36, s35, 0\n"
        "buffer_load_dwordx4 v[128:131], v1, s[16:19], s36 offen offset:0 sc1\n"
        "buffer_load_dwordx4 v[132:135], v1, s[16:19], s36 offen offset:1024 sc1\n"
        "buffer_load_dwordx4 v[136:139], v1, s[16:19], s36 offen offset:2048 sc1\n"
        "buffer_load_dwordx4 v[140:143], v1, s[16:19], s36 offen offset:3072 sc1\n"
        "s_add_u32 s36, s35, 4096\n"
        "buffer_load_dwordx4 v[144:147], v1, s[16:19], s36 offen offset:0 sc1\n"
        "buffer_load_dwordx4 v[148:151], v1, s[16:19], s36 offen offset:1024 sc1\n"
        "buffer_load_dwordx4 v[152:155], v1, s[16:19], s36 offen offset:2048 sc1\n"
        "buffer_load_dwordx4 v[156:159], v1, s[16:19], s36 offen offset:3072 sc1\n"
        "s_add_u32 s36, s35, 8192\n"
        "buffer_load_dwordx4 v[160:163], v1, s[16:19], s36 offen offset:0 sc1\n"
        "buffer_load_dwordx4 v[164:167], v1, s[16:19], s36 offen offset:1024 sc1\n"
        "buffer_load_dwordx4 v[168:171], v1, s[16:19], s36 offen offset:2048 sc1\n"
        "buffer_load_dwordx4 v[172:175], v1, s[16:19], s36 offen offset:3072 sc1\n"
        "s_add_u32 s36, s35, 12288\n"
        "buffer_load_dwordx4 v[176:179], v1, s[16:19], s36 offen offset:0 sc1\n"
        "buffer_load_dwordx4 v[180:183], v1, s[16:19], s36 offen offset:1024 sc1\n"
        "buffer_load_dwordx4 v[184:187], v1, s[16:19], s36 offen offset:2048 sc1\n"
        "buffer_load_dwordx4 v[188:191], v1, s[16:19], s36 offen offset:3072 sc1\n"
        "s_waitcnt vmcnt(16)\n"
        "v_max3_u32 v29, v8, v9, v10\n"
        "v_max3_u32 v29, v11, v12, v29\n"
        "v_max3_u32 v29, v13, v14, v29\n"
        "v_max_u32 v29, v15, v29\n"
        "v_cmp_eq_u32 vcc, -1, v29\n"
        "s_nop 1\n"
        "s_cbranch_vccnz L_st_rt\n"
        "L_st_go:\n"
        "s_barrier\n"
        "v_add_u32 v2, 0x10000, v1\n"
        "v_add_u32 v3, 0x20000, v1\n"
        "ds_read_b128 a[0:3], v1 offset:16384\n"
        "ds_read_b128 a[4:7], v1 offset:17408\n"
        "ds_read_b128 a[8:11], v1 offset:18432\n"
        "ds_read_b128 a[12:15], v1 offset:19456\n"
        "ds_read_b128 a[16:19], v3 offset:0\n"
        "ds_read_b128 a[20:23], v1 offset:20480\n"
        "ds_read_b128 a[24:27], v1 offset:21504\n"
        "ds_read_b128 a[28:31], v1 offset:22528\n"
        "v_lshrrev_b32 v22, 3, v16\n"
        "v_lshlrev_b32 v18, 6, v22\n"
        "v_lshlrev_b32 v22, 13, v22\n"
        "v_lshrrev_b32 v23, 1, v17\n"
        "v_lshl_add_u32 v22, v23, 7, v22\n"
        "v_and_b32 v23, 7, v16\n"
        "v_lshl_add_u32 v22, v23, 4, v22\n"
        "v_and_b32 v23, 1, v17\n"
        "v_lshl_add_u32 v22, v23, 9, v22\n"
        "s_and_b32 s37, s30, 7\n"
        "s_lshl_b32 s64, s37, 10\n"
        "s_add_u32 s64, s64, 0x912000\n"
        "s_lshl_b32 s66, s37, 3\n"
        "s_add_u32 s66, s66, 0x46000\n"
        "s_lshl_b32 s67, s50, 2\n"
        "s_add_u32 s67, s67, s28\n"
        "s_lshl_b32 s67, s67, 3\n"
        "s_add_u32 s67, s67, 0x41400\n"
        "s_mov_b32 s65, 0x72000\n"
        "s_lshr_b32 s38, s37, 1\n"
        "s_and_b32 s68, s37, 1\n"
        "s_cmp_eq_u32 s38, s28\n"
        "s_cselect_b32 s68, s68, 2\n"
        "v_lshrrev_b32 v19, 5, v16\n"
        "v_lshlrev_b32 v19, 11, v19\n"
        "s_cmp_eq_u32 s68, 1\n"
        "s_cbranch_scc1 L_D_pv\n"
        "v_and_b32 v23, 31, v16\n"
        "v_lshl_add_u32 v19, v23, 4, v19\n"
        "v_lshrrev_b32 v23, 1, v17\n"
        "v_lshl_add_u32 v19, v23, 9, v19\n"
        "v_and_b32 v23, 1, v17\n"
        "v_lshl_add_u32 v19, v23, 3, v19\n"
        "s_branch L_D_pe\n"
        "L_D_pv:\n"
        "v_bfe_u32 v23, v16, 4, 1\n"
        "v_lshl_add_u32 v19, v23, 9, v19\n"
        "v_bfe_u32 v23, v0, 2, 1\n"
        "v_lshl_add_u32 v19, v23, 8, v19\n"
        "v_lshl_add_u32 v19, v17, 6, v19\n"
        "v_bfe_u32 v23, v0, 3, 1\n"
        "v_lshl_add_u32 v19, v23, 3, v19\n"
        "v_and_b32 v23, 3, v0\n"
        "v_lshl_add_u32 v19, v23, 1, v19\n"
        "L_D_pe:\n"
        "v_xor_b32 v23, 0x100, v22\n"
        "v_add_f32 v20, v8, v10\n"
        "v_add_f32 v21, v12, v14\n"
        "v_add_f32 v20, v20, v21\n"
        "v_mov_b32 v21, v20\n"
        "s_nop 1\n"
        "v_permlane16_swap_b32 v20, v21\n"
        "v_add_f32 v20, v20, v21\n"
        "v_mov_b32 v21, v20\n"
        "s_nop 1\n"
        "v_permlane32_swap_b32 v20, v21\n"
        "v_add_f32 v20, v20, v21\n"
        "v_mul_f32 v20, 0x3d800000, v20\n"
        "v_sub_f32 v8, v8, v20\n"
        "v_sub_f32 v10, v10, v20\n"
        "v_sub_f32 v12, v12, v20\n"
        "v_sub_f32 v14, v14, v20\n"
        "v_mul_f32 v21, v8, v8\n"
        "v_fmac_f32 v21, v10, v10\n"
        "v_fmac_f32 v21, v12, v12\n"
        "v_fmac_f32 v21, v14, v14\n"
        "v_add_f32 v9, v9, v11\n"
        "v_add_f32 v13, v13, v15\n"
        "v_add_f32 v9, v9, v13\n"
        "s_mov_b32 s38, 0x42000000\n"
        "v_fma_f32 v21, v21, s38, v9\n"
        "v_mov_b32 v15, v21\n"
        "s_nop 1\n"
        "v_permlane16_swap_b32 v21, v15\n"
        "v_add_f32 v21, v21, v15\n"
        "v_mov_b32 v15, v21\n"
        "s_nop 1\n"
        "v_permlane32_swap_b32 v21, v15\n"
        "v_add_f32 v21, v21, v15\n"
        "v_mul_f32 v21, 0x3b000000, v21\n"
        "v_cvt_pk_bf16_f32 v21, v21, v20\n"
        "v_and_b32 v20, 0xffff0000, v21\n"
        "v_lshlrev_b32 v21, 16, v21\n"
        "v_add_f32 v21, 0x3727c5ac, v21\n"
        "v_cvt_pk_bf16_f32 v21, v21, v21\n"
        "v_lshlrev_b32 v21, 16, v21\n"
        "v_sqrt_f32 v21, v21\n"
        "s_nop 0\n"
        "v_cvt_pk_bf16_f32 v21, v21, v21\n"
        "v_lshlrev_b32 v21, 16, v21\n"
        "v_rcp_f32 v21, v21\n"
        "s_waitcnt vmcnt(12)\n"
        "v_max3_u32 v29, v128, v129, v130\n"
        "v_max3_u32 v29, v131, v132, v29\n"
        "v_max3_u32 v29, v133, v134, v29\n"
        "v_max3_u32 v29, v135, v136, v29\n"
        "v_max3_u32 v29, v137, v138, v29\n"
        "v_max3_u32 v29, v139, v140, v29\n"
        "v_max3_u32 v29, v141, v142, v29\n"
        "v_max_u32 v29, v143, v29\n"
        "v_cmp_eq_u32 vcc, -1, v29\n"
        "s_nop 1\n"
        "s_cbranch_vccnz L_d0_rt\n"
        "L_d0_go:\n"
        "s_waitcnt lgkmcnt(7)\n"
        "v_mfma_f32_16x16x32_bf16 v[64:67], a[0:3], v[128:131], 0\n"
        "ds_read_b128 a[32:35], v1 offset:23552\n"
        "s_waitcnt lgkmcnt(7)\n"
        "v_mfma_f32_16x16x32_bf16 v[68:71], a[4:7], v[128:131], 0\n"
        "ds_read_b128 a[36:39], v3 offset:1024\n"
        "s_waitcnt lgkmcnt(7)\n"
        "v_mfma_f32_16x16x32_bf16 v[72:75], a[8:11], v[128:131], 0\n"
        "ds_read_b128 a[40:43], v1 offset:24576\n"
        "s_waitcnt lgkmcnt(7)\n"
        "v_mfma_f32_16x16x32_bf16 v[76:79], a[12:15], v[128:131], 0\n"
        "ds_read_b128 a[44:47], v1 offset:25600\n"
        "s_waitcnt lgkmcnt(7)\n"
        "v_mfma_f32_16x16x32_bf16 v[80:83], a[16:19], v[128:131], 0\n"
        "ds_read_b128 a[48:51], v1 offset:26624\n"
        "s_waitcnt lgkmcnt(7)\n"
        "v_mfma_f32_16x16x32_bf16 v[64:67], a[20:23], v[132:135], v[64:67]\n"
        "ds_read_b128 a[52:55], v1 offset:27648\n"
        "s_waitcnt lgkmcnt(7)\n"
        "v_mfma_f32_16x16x32_bf16 v[68:71], a[24:27], v[132:135], v[68:71]\n"
        "ds_read_b128 a[56:59], v3 offset:2048\n"
        "s_waitcnt lgkmcnt(7)\n"
        "v_mfma_f32_16x16x32_bf16 v[72:75], a[28:31], v[132:135], v[72:75]\n"
        "ds_read_b128 a[60:63], v1 offset:28672\n"
        "s_waitcnt lgkmcnt(7)\n"
        "v_mfma_f32_16x16x32_bf16 v[76:79], a[32:35], v[132:135], v[76:79]\n"
        "ds_read_b128 a[0:3], v1 offset:29696\n"
        "s_waitcnt lgkmcnt(7)\n"
        "v_mfma_f32_16x16x32_bf16 v[80:83], a[36:39], v[132:135], v[80:83]\n"
        "ds_read_b128 a[4:7], v1 offset:30720\n"
        "s_waitcnt lgkmcnt(7)\n"
        "v_mfma_f32_16x16x32_bf16 v[64:67], a[40:43], v[136:139], v[64:67]\n"
        "ds_read_b128 a[8:11], v1 offset:31744\n"
        "s_waitcnt lgkmcnt(7)\n"
        "v_mfma_f32_16x16x32_bf16 v[68:71], a[44:47], v[136:139], v[68:71]\n"
        "ds_read_b128 a[12:15], v3 offset:3072\n"
        "s_waitcnt lgkmcnt(7)\n"
        "v_mfma_f32_16x16x32_bf16 v[72:75], a[48:51], v[136:139], v[72:75]\n"
        "ds_read_b128 a[16:19], v1 offset:49152\n"
        "s_waitcnt lgkmcnt(7)\n"
        "v_mfma_f32_16x16x32_bf16 v[76:79], a[52:55], v[136:139], v[76:79]\n"
        "ds_read_b128 a[20:23], v1 offset:50176\n"
        "s_waitcnt lgkmcnt(7)\n"
        "v_mfma_f32_16x16x32_bf16 v[80:83], a[56:59], v[136:139], v[80:83]\n"
        "ds_read_b128 a[24:27], v1 offset:51200\n"
        "s_waitcnt lgkmcnt(7)\n"
        "v_mfma_f32_16x16x32_bf16 v[64:67], a[60:63], v[140:143], v[64:67]\n"
        "ds_read_b128 a[28:31], v1 offset:52224\n"
        "s_waitcnt lgkmcnt(7)\n"
        "v_mfma_f32_16x16x32_bf16 v[68:71], a[0:3], v[140:143], v[68:71]\n"
        "ds_read_b128 a[32:35], v3 offset:4096\n"
        "s_waitcnt lgkmcnt(7)\n"
        "v_mfma_f32_16x16x32_bf16 v[72:75], a[4:7], v[140:143], v[72:75]\n"
        "ds_read_b128 a[36:39], v1 offset:53248\n"
        "s_waitcnt lgkmcnt(7)\n"
        "v_mfma_f32_16x16x32_bf16 v[76:79], a[8:11], v[140:143], v[76:79]\n"
        "ds_read_b128 a[40:43], v1 offset:54272\n"
        "s_waitcnt lgkmcnt(7)\n"
        "v_mfma_f32_16x16x32_bf16 v[80:83], a[12:15], v[140:143], v[80:83]\n"
        "ds_read_b128 a[44:47], v1 offset:55296\n"
        "s_waitcnt vmcnt(8)\n"
        "v_max3_u32 v29, v144, v145, v146\n"
        "v_max3_u32 v29, v147, v148, v29\n"
        "v_max3_u32 v29, v149, v150, v29\n"
        "v_max3_u32 v29, v151, v152, v29\n"
        "v_max3_u32 v29, v153, v154, v29\n"
        "v_max3_u32 v29, v155, v156, v29\n"
        "v_max3_u32 v29, v157, v158, v29\n"
        "v_max_u32 v29, v159, v29\n"
        "v_cmp_eq_u32 vcc, -1, v29\n"
        "s_nop 1\n"
        "s_cbranch_vccnz L_d1_rt\n"
        "L_d1_go:\n"
        "s_waitcnt lgkmcnt(7)\n"
        "v_mfma_f32_16x16x32_bf16 v[64:67], a[16:19], v[144:147], v[64:67]\n"
        "ds_read_b128 a[48:51], v1 offset:56320\n"
        "s_waitcnt lgkmcnt(7)\n"
        "v_mfma_f32_16x16x32_bf16 v[68:71], a[20:23], v[144:147], v[68:71]\n"
        "ds_read_b128 a[52:55], v3 offset:5120\n"
        "s_waitcnt lgkmcnt(7)\n"
        "v_mfma_f32_16x16x32_bf16 v[72:75], a[24:27], v[144:147], v[72:75]\n"
        "ds_read_b128 a[56:59], v1 offset:57344\n"
        "s_waitcnt lgkmcnt(7)\n"
        "v_mfma_f32_16x16x32_bf16 v[76:79], a[28:31], v[144:147], v[76:79]\n"
        "ds_read_b128 a[60:63], v1 offset:58368\n"
        "s_waitcnt lgkmcnt(7)\n"
        "v_mfma_f32_16x16x32_bf16 v[80:83], a[32:35], v[144:147], v[80:83]\n"
        "ds_read_b128 a[0:3], v1 offset:59392\n"
        "s_waitcnt lgkmcnt(7)\n"
        "v_mfma_f32_16x16x32_bf16 v[64:67], a[36:39], v[148:151], v[64:67]\n"
        "ds_read_b128 a[4:7], v1 offset:60416\n"
        "s_waitcnt lgkmcnt(7)\n"
        "v_mfma_f32_16x16x32_bf16 v[68:71], a[40:43], v[148:151], v[68:71]\n"
        "ds_read_b128 a[8:11], v3 offset:6144\n"
        "s_waitcnt lgkmcnt(7)\n"
        "v_mfma_f32_16x16x32_bf16 v[72:75], a[44:47], v[148:151], v[72:75]\n"
        "ds_read_b128 a[12:15], v1 offset:61440\n"
        "s_waitcnt lgkmcnt(7)\n"
        "v_mfma_f32_16x16x32_bf16 v[76:79], a[48:51], v[148:151], v[76:79]\n"
        "ds_read_b128 a[16:19], v1 offset:62464\n"
        "s_waitcnt lgkmcnt(7)\n"
        "v_mfma_f32_16x16x32_bf16 v[80:83], a[52:55], v[148:151], v[80:83]\n"
        "ds_read_b128 a[20:23], v1 offset:63488\n"
        "s_waitcnt lgkmcnt(7)\n"
        "v_mfma_f32_16x16x32_bf16 v[64:67], a[56:59], v[152:155], v[64:67]\n"
        "ds_read_b128 a[24:27], v1 offset:64512\n"
        "s_waitcnt lgkmcnt(7)\n"
        "v_mfma_f32_16x16x32_bf16 v[68:71], a[60:63], v[152:155], v[68:71]\n"
        "ds_read_b128 a[28:31], v3 offset:7168\n"
        "s_waitcnt lgkmcnt(7)\n"
        "v_mfma_f32_16x16x32_bf16 v[72:75], a[0:3], v[152:155], v[72:75]\n"
        "ds_read_b128 a[32:35], v2 offset:16384\n"
        "s_waitcnt lgkmcnt(7)\n"
        "v_mfma_f32_16x16x32_bf16 v[76:79], a[4:7], v[152:155], v[76:79]\n"
        "ds_read_b128 a[36:39], v2 offset:17408\n"
        "s_waitcnt lgkmcnt(7)\n"
        "v_mfma_f32_16x16x32_bf16 v[80:83], a[8:11], v[152:155], v[80:83]\n"
        "ds_read_b128 a[40:43], v2 offset:18432\n"
        "s_waitcnt lgkmcnt(7)\n"
        "v_mfma_f32_16x16x32_bf16 v[64:67], a[12:15], v[156:159], v[64:67]\n"
        "ds_read_b128 a[44:47], v2 offset:19456\n"
        "s_waitcnt lgkmcnt(7)\n"
        "v_mfma_f32_16x16x32_bf16 v[68:71], a[16:19], v[156:159], v[68:71]\n"
        "ds_read_b128 a[48:51], v3 offset:8192\n"
        "s_waitcnt lgkmcnt(7)\n"
        "v_mfma_f32_16x16x32_bf16 v[72:75], a[20:23], v[156:159], v[72:75]\n"
        "ds_read_b128 a[52:55], v2 offset:20480\n"
        "s_waitcnt lgkmcnt(7)\n"
        "v_mfma_f32_16x16x32_bf16 v[76:79], a[24:27], v[156:159], v[76:79]\n"
        "ds_read_b128 a[56:59], v2 offset:21504\n"
        "s_waitcnt lgkmcnt(7)\n"
        "v_mfma_f32_16x16x32_bf16 v[80:83], a[28:31], v[156:159], v[80:83]\n"
        "ds_read_b128 a[60:63], v2 offset:22528\n"
        "s_waitcnt vmcnt(4)\n"
        "v_max3_u32 v29, v160, v161, v162\n"
        "v_max3_u32 v29, v163, v164, v29\n"
        "v_max3_u32 v29, v165, v166, v29\n"
        "v_max3_u32 v29, v167, v168, v29\n"
        "v_max3_u32 v29, v169, v170, v29\n"
        "v_max3_u32 v29, v171, v172, v29\n"
        "v_max3_u32 v29, v173, v174, v29\n"
        "v_max_u32 v29, v175, v29\n"
        "v_cmp_eq_u32 vcc, -1, v29\n"
        "s_nop 1\n"
        "s_cbranch_vccnz L_d2_rt\n"
        "L_d2_go:\n"
        "s_waitcnt lgkmcnt(7)\n"
        "v_mfma_f32_16x16x32_bf16 v[64:67], a[32:35], v[160:163], v[64:67]\n"
        "ds_read_b128 a[0:3], v2 offset:23552\n"
        "s_waitcnt lgkmcnt(7)\n"
        "v_mfma_f32_16x16x32_bf16 v[68:71], a[36:39], v[160:163], v[68:71]\n"
        "ds_read_b128 a[4:7], v3 offset:9216\n"
        "s_waitcnt lgkmcnt(7)\n"
        "v_mfma_f32_16x16x32_bf16 v[72:75], a[40:43], v[160:163], v[72:75]\n"
        "ds_read_b128 a[8:11], v2 offset:24576\n"
        "s_waitcnt lgkmcnt(7)\n"
        "v_mfma_f32_16x16x32_bf16 v[76:79], a[44:47], v[160:163], v[76:79]\n"
        "ds_read_b128 a[12:15], v2 offset:25600\n"
        "s_waitcnt lgkmcnt(7)\n"
        "v_mfma_f32_16x16x32_bf16 v[80:83], a[48:51], v[160:163], v[80:83]\n"
        "ds_read_b128 a[16:19], v2 offset:26624\n"
        "s_waitcnt lgkmcnt(7)\n"
        "v_mfma_f32_16x16x32_bf16 v[64:67], a[52:55], v[164:167], v[64:67]\n"
        "ds_read_b128 a[20:23], v2 offset:27648\n"
        "s_waitcnt lgkmcnt(7)\n"
        "v_mfma_f32_16x16x32_bf16 v[68:71], a[56:59], v[164:167], v[68:71]\n"
        "ds_read_b128 a[24:27], v3 offset:10240\n"
        "s_waitcnt lgkmcnt(7)\n"
        "v_mfma_f32_16x16x32_bf16 v[72:75], a[60:63], v[164:167], v[72:75]\n"
        "ds_read_b128 a[28:31], v2 offset:28672\n"
        "s_waitcnt lgkmcnt(7)\n"
        "v_mfma_f32_16x16x32_bf16 v[76:79], a[0:3], v[164:167], v[76:79]\n"
        "ds_read_b128 a[32:35], v2 offset:29696\n"
        "s_waitcnt lgkmcnt(7)\n"
        "v_mfma_f32_16x16x32_bf16 v[80:83], a[4:7], v[164:167], v[80:83]\n"
        "ds_read_b128 a[36:39], v2 offset:30720\n"
        "s_waitcnt lgkmcnt(7)\n"
        "v_mfma_f32_16x16x32_bf16 v[64:67], a[8:11], v[168:171], v[64:67]\n"
        "ds_read_b128 a[40:43], v2 offset:31744\n"
        "s_waitcnt lgkmcnt(7)\n"
        "v_mfma_f32_16x16x32_bf16 v[68:71], a[12:15], v[168:171], v[68:71]\n"
        "ds_read_b128 a[44:47], v3 offset:11264\n"
        "s_waitcnt lgkmcnt(7)\n"
        "v_mfma_f32_16x16x32_bf16 v[72:75], a[16:19], v[168:171], v[72:75]\n"
        "ds_read_b128 a[48:51], v2 offset:49152\n"
        "s_waitcnt lgkmcnt(7)\n"
        "v_mfma_f32_16x16x32_bf16 v[76:79], a[20:23], v[168:171], v[76:79]\n"
        "ds_read_b128 a[52:55], v2 offset:50176\n"
        "s_waitcnt lgkmcnt(7)\n"
        "v_mfma_f32_16x16x32_bf16 v[80:83], a[24:27], v[168:171], v[80:83]\n"
        "ds_read_b128 a[56:59], v2 offset:51200\n"
        "s_waitcnt lgkmcnt(7)\n"
        "v_mfma_f32_16x16x32_bf16 v[64:67], a[28:31], v[172:175], v[64:67]\n"
        "ds_read_b128 a[60:63], v2 offset:52224\n"
        "s_waitcnt lgkmcnt(7)\n"
        "v_mfma_f32_16x16x32_bf16 v[68:71], a[32:35], v[172:175], v[68:71]\n"
        "ds_read_b128 a[0:3], v3 offset:12288\n"
        "s_waitcnt lgkmcnt(7)\n"
        "v_mfma_f32_16x16x32_bf16 v[72:75], a[36:39], v[172:175], v[72:75]\n"
        "ds_read_b128 a[4:7], v2 offset:53248\n"
        "s_waitcnt lgkmcnt(7)\n"
        "v_mfma_f32_16x16x32_bf16 v[76:79], a[40:43], v[172:175], v[76:79]\n"
        "ds_read_b128 a[8:11], v2 offset:54272\n"
        "s_waitcnt lgkmcnt(7)\n"
        "v_mfma_f32_16x16x32_bf16 v[80:83], a[44:47], v[172:175], v[80:83]\n"
        "ds_read_b128 a[12:15], v2 offset:55296\n"
        "s_waitcnt vmcnt(0)\n"
        "v_max3_u32 v29, v176, v177, v178\n"
        "v_max3_u32 v29, v179, v180, v29\n"
        "v_max3_u32 v29, v181, v182, v29\n"
        "v_max3_u32 v29, v183, v184, v29\n"
        "v_max3_u32 v29, v185, v186, v29\n"
        "v_max3_u32 v29, v187, v188, v29\n"
        "v_max3_u32 v29, v189, v190, v29\n"
        "v_max_u32 v29, v191, v29\n"
        "v_cmp_eq_u32 vcc, -1, v29\n"
        "s_nop 1\n"
        "s_cbranch_vccnz L_d3_rt\n"
        "L_d3_go:\n"
        "s_waitcnt lgkmcnt(7)\n"
        "v_mfma_f32_16x16x32_bf16 v[64:67], a[48:51], v[176:179], v[64:67]\n"
        "ds_read_b128 a[16:19], v2 offset:56320\n"
        "s_waitcnt lgkmcnt(7)\n"
        "v_mfma_f32_16x16x32_bf16 v[68:71], a[52:55], v[176:179], v[68:71]\n"
        "ds_read_b128 a[20:23], v3 offset:13312\n"
        "s_waitcnt lgkmcnt(7)\n"
        "v_mfma_f32_16x16x32_bf16 v[72:75], a[56:59], v[176:179], v[72:75]\n"
        "ds_read_b128 a[24:27], v2 offset:57344\n"
        "s_waitcnt lgkmcnt(7)\n"
        "v_mfma_f32_16x16x32_bf16 v[76:79], a[60:63], v[176:179], v[76:79]\n"
        "ds_read_b128 a[28:31], v2 offset:58368\n"
        "s_waitcnt lgkmcnt(7)\n"
        "v_mfma_f32_16x16x32_bf16 v[80:83], a[0:3], v[176:179], v[80:83]\n"
        "ds_read_b128 a[32:35], v2 offset:59392\n"
        "s_waitcnt lgkmcnt(7)\n"
        "v_mfma_f32_16x16x32_bf16 v[64:67], a[4:7], v[180:183], v[64:67]\n"
        "ds_read_b128 a[36:39], v2 offset:60416\n"
        "s_waitcnt lgkmcnt(7)\n"
        "v_mfma_f32_16x16x32_bf16 v[68:71], a[8:11], v[180:183], v[68:71]\n"
        "ds_read_b128 a[40:43], v3 offset:14336\n"
        "s_waitcnt lgkmcnt(7)\n"
        "v_mfma_f32_16x16x32_bf16 v[72:75], a[12:15], v[180:183], v[72:75]\n"
        "ds_read_b128 a[44:47], v2 offset:61440\n"
        "s_waitcnt lgkmcnt(7)\n"
        "v_mfma_f32_16x16x32_bf16 v[76:79], a[16:19], v[180:183], v[76:79]\n"
        "ds_read_b128 a[48:51], v2 offset:62464\n"
        "s_waitcnt lgkmcnt(7)\n"
        "v_mfma_f32_16x16x32_bf16 v[80:83], a[20:23], v[180:183], v[80:83]\n"
        "ds_read_b128 a[52:55], v2 offset:63488\n"
        "s_waitcnt lgkmcnt(7)\n"
        "v_mfma_f32_16x16x32_bf16 v[64:67], a[24:27], v[184:187], v[64:67]\n"
        "ds_read_b128 a[56:59], v2 offset:64512\n"
        "s_waitcnt lgkmcnt(7)\n"
        "v_mfma_f32_16x16x32_bf16 v[68:71], a[28:31], v[184:187], v[68:71]\n"
        "ds_read_b128 a[60:63], v3 offset:15360\n"
        "s_waitcnt lgkmcnt(7)\n"
        "v_mfma_f32_16x16x32_bf16 v[72:75], a[32:35], v[184:187], v[72:75]\n"
        "s_waitcnt lgkmcnt(6)\n"
        "v_mfma_f32_16x16x32_bf16 v[76:79], a[36:39], v[184:187], v[76:79]\n"
        "s_waitcnt lgkmcnt(5)\n"
        "v_mfma_f32_16x16x32_bf16 v[80:83], a[40:43], v[184:187], v[80:83]\n"
        "s_waitcnt lgkmcnt(4)\n"
        "v_mfma_f32_16x16x32_bf16 v[64:67], a[44:47], v[188:191], v[64:67]\n"
        "s_waitcnt lgkmcnt(3)\n"
        "v_mfma_f32_16x16x32_bf16 v[68:71], a[48:51], v[188:191], v[68:71]\n"
        "s_waitcnt lgkmcnt(2)\n"
        "v_mfma_f32_16x16x32_bf16 v[72:75], a[52:55], v[188:191], v[72:75]\n"
        "s_waitcnt lgkmcnt(1)\n"
        "v_mfma_f32_16x16x32_bf16 v[76:79], a[56:59], v[188:191], v[76:79]\n"
        "s_waitcnt lgkmcnt(0)\n"
        "v_mfma_f32_16x16x32_bf16 v[80:83], a[60:63], v[188:191], v[80:83]\n"
        "v_mul_f32 v24, 0x3eb8aa3b, v21\n"
        "v_mul_f32 v28, v20, v21\n"
        "v_mul_f32 v26, v20, v24\n"
        "v_mov_b32 v30, v21\n"
        "v_pk_mul_f32 v[64:65], v[64:65], v[24:25] op_sel_hi:[1,0]\n"
        "v_pk_mul_f32 v[66:67], v[66:67], v[24:25] op_sel_hi:[1,0]\n"
        "v_pk_mul_f32 v[68:69], v[68:69], v[24:25] op_sel_hi:[1,0]\n"
        "v_pk_mul_f32 v[70:71], v[70:71], v[24:25] op_sel_hi:[1,0]\n"
        "v_pk_mul_f32 v[72:73], v[72:73], v[24:25] op_sel_hi:[1,0]\n"
        "v_pk_mul_f32 v[74:75], v[74:75], v[24:25] op_sel_hi:[1,0]\n"
        "v_pk_mul_f32 v[76:77], v[76:77], v[24:25] op_sel_hi:[1,0]\n"
        "v_pk_mul_f32 v[78:79], v[78:79], v[24:25] op_sel_hi:[1,0]\n"
        "v_pk_mul_f32 v[80:81], v[80:81], v[30:31] op_sel_hi:[1,0]\n"
        "v_pk_mul_f32 v[82:83], v[82:83], v[30:31] op_sel_hi:[1,0]\n"
        "v_pk_fma_f32 v[64:65], v[96:97], v[26:27], v[64:65] op_sel_hi:[1,0,1] neg_lo:[0,1,0] neg_hi:[0,1,0]\n"
        "v_pk_fma_f32 v[66:67], v[98:99], v[26:27], v[66:67] op_sel_hi:[1,0,1] neg_lo:[0,1,0] neg_hi:[0,1,0]\n"
        "v_pk_fma_f32 v[68:69], v[100:101], v[26:27], v[68:69] op_sel_hi:[1,0,1] neg_lo:[0,1,0] neg_hi:[0,1,0]\n"
        "v_pk_fma_f32 v[70:71], v[102:103], v[26:27], v[70:71] op_sel_hi:[1,0,1] neg_lo:[0,1,0] neg_hi:[0,1,0]\n"
        "v_pk_fma_f32 v[72:73], v[104:105], v[26:27], v[72:73] op_sel_hi:[1,0,1] neg_lo:[0,1,0] neg_hi:[0,1,0]\n"
        "v_pk_fma_f32 v[74:75], v[106:107], v[26:27], v[74:75] op_sel_hi:[1,0,1] neg_lo:[0,1,0] neg_hi:[0,1,0]\n"
        "v_pk_fma_f32 v[76:77], v[108:109], v[26:27], v[76:77] op_sel_hi:[1,0,1] neg_lo:[0,1,0] neg_hi:[0,1,0]\n"
        "v_pk_fma_f32 v[78:79], v[110:111], v[26:27], v[78:79] op_sel_hi:[1,0,1] neg_lo:[0,1,0] neg_hi:[0,1,0]\n"
        "v_pk_fma_f32 v[80:81], v[112:113], v[28:29], v[80:81] op_sel_hi:[1,0,1] neg_lo:[0,1,0] neg_hi:[0,1,0]\n"
        "v_pk_fma_f32 v[82:83], v[114:115], v[28:29], v[82:83] op_sel_hi:[1,0,1] neg_lo:[0,1,0] neg_hi:[0,1,0]\n"
        "v_cvt_pk_bf16_f32 v84, v64, v65\n"
        "v_cvt_pk_bf16_f32 v85, v66, v67\n"
        "v_cvt_pk_bf16_f32 v86, v68, v69\n"
        "v_cvt_pk_bf16_f32 v87, v70, v71\n"
        "v_cvt_pk_bf16_f32 v88, v72, v73\n"
        "v_cvt_pk_bf16_f32 v89, v74, v75\n"
        "v_cvt_pk_bf16_f32 v90, v76, v77\n"
        "v_cvt_pk_bf16_f32 v91, v78, v79\n"
        "v_cvt_pk_bf16_f32 v24, v80, v81\n"
        "v_cvt_pk_bf16_f32 v25, v82, v83\n"
        "buffer_store_dwordx4 v[84:87], v22, s[16:19], s64 offen sc1\n"
        "buffer_store_dwordx4 v[88:91], v23, s[16:19], s64 offen sc1\n"
        "s_cmp_lg_u32 s68, 0\n"
        "s_cbranch_scc1 L_D_nk\n"
        "buffer_store_dwordx2 v[24:25], v19, s[16:19], s65 offen\n"
        "L_D_nk:\n"
        "s_cmp_lg_u32 s68, 1\n"
        "s_cbranch_scc1 L_D_nv\n"
        "buffer_store_short v24, v19, s[16:19], s65 offen offset:1024\n"
        "buffer_store_short_d16_hi v24, v19, s[16:19], s65 offen offset:1040\n"
        "buffer_store_short v25, v19, s[16:19], s65 offen offset:1056\n"
        "buffer_store_short_d16_hi v25, v19, s[16:19], s65 offen offset:1072\n"
        "L_D_nv:\n"
        "v_dot2_f32_bf16 v64, v84, v84, 0\n"
        "v_dot2_f32_bf16 v68, v88, v88, 0\n"
        "v_dot2_f32_bf16 v66, v24, v24, 0\n"
        "v_dot2_f32_bf16 v64, v85, v85, v64\n"
        "v_dot2_f32_bf16 v68, v89, v89, v68\n"
        "v_dot2_f32_bf16 v66, v25, v25, v66\n"
        "v_dot2_f32_bf16 v64, v86, v86, v64\n"
        "v_dot2_f32_bf16 v68, v90, v90, v68\n"
        "v_dot2_f32_bf16 v64, v87, v87, v64\n"
        "v_dot2_f32_bf16 v68, v91, v91, v68\n"
        "v_mov_b32 v67, v66\n"
        "s_nop 0\n"
        "v_mov_b32 v65, v64\n"
        "v_mov_b32 v69, v68\n"
        "v_permlane16_swap_b32 v66, v67\n"
        "v_permlane16_swap_b32 v64, v65\n"
        "v_permlane16_swap_b32 v68, v69\n"
        "v_add_f32 v66, v66, v67\n"
        "v_add_f32 v64, v64, v65\n"
        "v_add_f32 v68, v68, v69\n"
        "v_mov_b32 v67, v66\n"
        "v_max_f32 v64, v64, v68\n"
        "v_mov_b32 v65, v64\n"
        "v_permlane32_swap_b32 v66, v67\n"
        "s_nop 0\n"
        "v_permlane32_swap_b32 v64, v65\n"
        "v_add_f32 v66, v66, v67\n"
        "v_max_f32 v64, v64, v65\n"
        "s_nop 0\n"
        "v_max_f32_dpp v66, v66, v66 quad_perm:[1,0,3,2] row_mask:0xf bank_mask:0xf\n"
        "v_max_f32_dpp v64, v64, v64 quad_perm:[1,0,3,2] row_mask:0xf bank_mask:0xf\n"
        "s_nop 0\n"
        "v_max_f32_dpp v66, v66, v66 quad_perm:[2,3,0,1] row_mask:0xf bank_mask:0xf\n"
        "v_max_f32_dpp v64, v64, v64 quad_perm:[2,3,0,1] row_mask:0xf bank_mask:0xf\n"
        "s_nop 0\n"
        "v_max_f32_dpp v66, v66, v66 row_half_mirror row_mask:0xf bank_mask:0xf\n"
        "v_max_f32_dpp v64, v64, v64 row_half_mirror row_mask:0xf bank_mask:0xf\n"
        "s_nop 0\n"
        "v_max_f32_dpp v66, v66, v66 row_mirror row_mask:0xf bank_mask:0xf\n"
        "v_mov_b32 v65, v64\n"
        "v_mov_b32 v67, v66\n"
        "s_mov_b64 exec, 0x101\n"
        "buffer_store_dwordx2 v[64:65], v18, s[16:19], s66 offen\n"
        "s_cmp_lg_u32 s68, 0\n"
        "s_cbranch_scc1 L_D_nm\n"
        "s_mov_b64 exec, 1\n"
        "buffer_store_dwordx2 v[66:67], off, s[16:19], s67\n"
        "L_D_nm:\n"
        "s_mov_b64 exec, -1\n"
        "L_fin:\n"
        "s_endpgm\n"
        "L_x0_rt:\n"
        "s_mov_b32 s46, 0x40000\n"
        "L_x0_rl:\n"
        "s_nop 7\n"
        "s_nop 7\n"
        "s_waitcnt vmcnt(0)\n"
        "L_xp:\n"
        "s_mov_b64 exec, 0xffff\n"
        "buffer_load_dword v45, v46, s[16:19], s55 offen offset:2048 sc1\n"
        "s_waitcnt vmcnt(0)\n"
        "v_cmp_eq_u32 vcc, -1, v45\n"
        "s_mov_b64 exec, -1\n"
        "s_nop 0\n"
        "s_cbranch_vccz L_xq\n"
        "s_sub_u32 s46, s46, 1\n"
        "s_cmp_gt_i32 s46, 1\n"
        "s_cbranch_scc1 L_xp\n"
        "L_xq:\n"
        "s_add_u32 s47, s55, 0\n"
        "buffer_load_dwordx4 v[128:131], v1, s[16:19], s47 offen offset:0 sc1\n"
        "s_add_u32 s47, s55, 8192\n"
        "buffer_load_dwordx4 v[160:163], v1, s[16:19], s47 offen offset:0 sc1\n"
        "s_add_u32 s47, s55, 16384\n"
        "buffer_load_dwordx4 v[192:195], v1, s[16:19], s47 offen offset:0 sc1\n"
        "s_add_u32 s47, s55, 24576\n"
        "buffer_load_dwordx4 v[224:227], v1, s[16:19], s47 offen offset:0 sc1\n"
        "s_add_u32 s47, s55, 0\n"
        "buffer_load_dwordx4 v[132:135], v1, s[16:19], s47 offen offset:1024 sc1\n"
        "s_add_u32 s47, s55, 0\n"
        "buffer_load_dwordx4 v[136:139], v1, s[16:19], s47 offen offset:2048 sc1\n"
        "s_add_u32 s47, s55, 0\n"
        "buffer_load_dwordx4 v[140:143], v1, s[16:19], s47 offen offset:3072 sc1\n"
        "s_add_u32 s47, s55, 4096\n"
        "buffer_load_dwordx4 v[144:147], v1, s[16:19], s47 offen offset:0 sc1\n"
        "s_add_u32 s47, s55, 4096\n"
        "buffer_load_dwordx4 v[148:151], v1, s[16:19], s47 offen offset:1024 sc1\n"
        "s_add_u32 s47, s55, 4096\n"
        "buffer_load_dwordx4 v[152:155], v1, s[16:19], s47 offen offset:2048 sc1\n"
        "s_add_u32 s47, s55, 4096\n"
        "buffer_load_dwordx4 v[156:159], v1, s[16:19], s47 offen offset:3072 sc1\n"
        "s_add_u32 s47, s55, 8192\n"
        "buffer_load_dwordx4 v[164:167], v1, s[16:19], s47 offen offset:1024 sc1\n"
        "s_waitcnt vmcnt(8)\n"
        "v_max3_u32 v29, v128, v129, v130\n"
        "v_max3_u32 v29, v131, v160, v29\n"
        "v_max3_u32 v29, v161, v162, v29\n"
        "v_max3_u32 v29, v163, v192, v29\n"
        "v_max3_u32 v29, v193, v194, v29\n"
        "v_max3_u32 v29, v195, v224, v29\n"
        "v_max3_u32 v29, v225, v226, v29\n"
        "v_max_u32 v29, v227, v29\n"
        "v_cmp_eq_u32 vcc, -1, v29\n"
        "s_nop 1\n"
        "s_cbranch_vccz L_x0_go\n"
        "s_sub_u32 s46, s46, 1\n"
        "s_cmp_gt_i32 s46, 0\n"
        "s_cbranch_scc1 L_x0_rl\n"
        "s_branch L_x0_go\n"
        "L_x1_rt:\n"
        "s_mov_b32 s46, 0x40000\n"
        "L_x1_rl:\n"
        "s_nop 7\n"
        "s_nop 7\n"
        "s_add_u32 s47, s55, 0\n"
        "buffer_load_dwordx4 v[132:135], v1, s[16:19], s47 offen offset:1024 sc1\n"
        "s_add_u32 s47, s55, 0\n"
        "buffer_load_dwordx4 v[136:139], v1, s[16:19], s47 offen offset:2048 sc1\n"
        "s_add_u32 s47, s55, 0\n"
        "buffer_load_dwordx4 v[140:143], v1, s[16:19], s47 offen offset:3072 sc1\n"
        "s_add_u32 s47, s55, 4096\n"
        "buffer_load_dwordx4 v[144:147], v1, s[16:19], s47 offen offset:0 sc1\n"
        "s_waitcnt vmcnt(0)\n"
        "v_max3_u32 v29, v132, v133, v134\n"
        "v_max3_u32 v29, v135, v136, v29\n"
        "v_max3_u32 v29, v137, v138, v29\n"
        "v_max3_u32 v29, v139, v140, v29\n"
        "v_max3_u32 v29, v141, v142, v29\n"
        "v_max3_u32 v29, v143, v144, v29\n"
        "v_max3_u32 v29, v145, v146, v29\n"
        "v_max_u32 v29, v147, v29\n"
        "v_cmp_eq_u32 vcc, -1, v29\n"
        "s_nop 1\n"
        "s_cbranch_vccz L_x1_go\n"
        "s_sub_u32 s46, s46, 1\n"
        "s_cmp_gt_i32 s46, 0\n"
        "s_cbranch_scc1 L_x1_rl\n"
        "s_branch L_x1_go\n"
        "L_x2_rt:\n"
        "s_mov_b32 s46, 0x40000\n"
        "L_x2_rl:\n"
        "s_nop 7\n"
        "s_nop 7\n"
        "s_add_u32 s47, s55, 4096\n"
        "buffer_load_dwordx4 v[148:151], v1, s[16:19], s47 offen offset:1024 sc1\n"
        "s_add_u32 s47, s55, 4096\n"
        "buffer_load_dwordx4 v[152:155], v1, s[16:19], s47 offen offset:2048 sc1\n"
        "s_add_u32 s47, s55, 4096\n"
        "buffer_load_dwordx4 v[156:159], v1, s[16:19], s47 offen offset:3072 sc1\n"
        "s_add_u32 s47, s55, 8192\n"
        "buffer_load_dwordx4 v[164:167], v1, s[16:19], s47 offen offset:1024 sc1\n"
        "s_waitcnt vmcnt(0)\n"
        "v_max3_u32 v29, v148, v149, v150\n"
        "v_max3_u32 v29, v151, v152, v29\n"
        "v_max3_u32 v29, v153, v154, v29\n"
        "v_max3_u32 v29, v155, v156, v29\n"
        "v_max3_u32 v29, v157, v158, v29\n"
        "v_max3_u32 v29, v159, v164, v29\n"
        "v_max3_u32 v29, v165, v166, v29\n"
        "v_max_u32 v29, v167, v29\n"
        "v_cmp_eq_u32 vcc, -1, v29\n"
        "s_nop 1\n"
        "s_cbranch_vccz L_x2_go\n"
        "s_sub_u32 s46, s46, 1\n"
        "s_cmp_gt_i32 s46, 0\n"
        "s_cbranch_scc1 L_x2_rl\n"
        "s_branch L_x2_go\n"
        "L_x3_rt:\n"
        "s_mov_b32 s46, 0x40000\n"
        "L_x3_rl:\n"
        "s_nop 7\n"
        "s_nop 7\n"
        "s_add_u32 s47, s55, 8192\n"
        "buffer_load_dwordx4 v[168:171], v1, s[16:19], s47 offen offset:2048 sc1\n"
        "s_add_u32 s47, s55, 8192\n"
        "buffer_load_dwordx4 v[172:175], v1, s[16:19], s47 offen offset:3072 sc1\n"
        "s_add_u32 s47, s55, 12288\n"
        "buffer_load_dwordx4 v[176:179], v1, s[16:19], s47 offen offset:0 sc1\n"
        "s_add_u32 s47, s55, 12288\n"
        "buffer_load_dwordx4 v[180:183], v1, s[16:19], s47 offen offset:1024 sc1\n"
        "s_waitcnt vmcnt(0)\n"
        "v_max3_u32 v29, v168, v169, v170\n"
        "v_max3_u32 v29, v171, v172, v29\n"
        "v_max3_u32 v29, v173, v174, v29\n"
        "v_max3_u32 v29, v175, v176, v29\n"
        "v_max3_u32 v29, v177, v178, v29\n"
        "v_max3_u32 v29, v179, v180, v29\n"
        "v_max3_u32 v29, v181, v182, v29\n"
        "v_max_u32 v29, v183, v29\n"
        "v_cmp_eq_u32 vcc, -1, v29\n"
        "s_nop 1\n"
        "s_cbranch_vccz L_x3_go\n"
        "s_sub_u32 s46, s46, 1\n"
        "s_cmp_gt_i32 s46, 0\n"
        "s_cbranch_scc1 L_x3_rl\n"
        "s_branch L_x3_go\n"
        "L_x4_rt:\n"
        "s_mov_b32 s46, 0x40000\n"
        "L_x4_rl:\n"
        "s_nop 7\n"
        "s_nop 7\n"
        "s_add_u32 s47, s55, 12288\n"
        "buffer_load_dwordx4 v[184:187], v1, s[16:19], s47 offen offset:2048 sc1\n"
        "s_add_u32 s47, s55, 12288\n"
        "buffer_load_dwordx4 v[188:191], v1, s[16:19], s47 offen offset:3072 sc1\n"
        "s_add_u32 s47, s55, 16384\n"
        "buffer_load_dwordx4 v[196:199], v1, s[16:19], s47 offen offset:1024 sc1\n"
        "s_add_u32 s47, s55, 16384\n"
        "buffer_load_dwordx4 v[200:203], v1, s[16:19], s47 offen offset:2048 sc1\n"
        "s_waitcnt vmcnt(0)\n"
        "v_max3_u32 v29, v184, v185, v186\n"
        "v_max3_u32 v29, v187, v188, v29\n"
        "v_max3_u32 v29, v189, v190, v29\n"
        "v_max3_u32 v29, v191, v196, v29\n"
        "v_max3_u32 v29, v197, v198, v29\n"
        "v_max3_u32 v29, v199, v200, v29\n"
        "v_max3_u32 v29, v201, v202, v29\n"
        "v_max_u32 v29, v203, v29\n"
        "v_cmp_eq_u32 vcc, -1, v29\n"
        "s_nop 1\n"
        "s_cbranch_vccz L_x4_go\n"
        "s_sub_u32 s46, s46, 1\n"
        "s_cmp_gt_i32 s46, 0\n"
        "s_cbranch_scc1 L_x4_rl\n"
        "s_branch L_x4_go\n"
        "L_x5_rt:\n"
        "s_mov_b32 s46, 0x40000\n"
        "L_x5_rl:\n"
        "s_nop 7\n"
        "s_nop 7\n"
        "s_add_u32 s47, s55, 16384\n"
        "buffer_load_dwordx4 v[204:207], v1, s[16:19], s47 offen offset:3072 sc1\n"
        "s_add_u32 s47, s55, 20480\n"
        "buffer_load_dwordx4 v[208:211], v1, s[16:19], s47 offen offset:0 sc1\n"
        "s_add_u32 s47, s55, 20480\n"
        "buffer_load_dwordx4 v[212:215], v1, s[16:19], s47 offen offset:1024 sc1\n"
        "s_add_u32 s47, s55, 20480\n"
        "buffer_load_dwordx4 v[216:219], v1, s[16:19], s47 offen offset:2048 sc1\n"
        "s_waitcnt vmcnt(0)\n"
        "v_max3_u32 v29, v204, v205, v206\n"
        "v_max3_u32 v29, v207, v208, v29\n"
        "v_max3_u32 v29, v209, v210, v29\n"
        "v_max3_u32 v29, v211, v212, v29\n"
        "v_max3_u32 v29, v213, v214, v29\n"
        "v_max3_u32 v29, v215, v216, v29\n"
        "v_max3_u32 v29, v217, v218, v29\n"
        "v_max_u32 v29, v219, v29\n"
        "v_cmp_eq_u32 vcc, -1, v29\n"
        "s_nop 1\n"
        "s_cbranch_vccz L_x5_go\n"
        "s_sub_u32 s46, s46, 1\n"
        "s_cmp_gt_i32 s46, 0\n"
        "s_cbranch_scc1 L_x5_rl\n"
        "s_branch L_x5_go\n"
        "L_x6_rt:\n"
        "s_mov_b32 s46, 0x40000\n"
        "L_x6_rl:\n"
        "s_nop 7\n"
        "s_nop 7\n"
        "s_add_u32 s47, s55, 20480\n"
        "buffer_load_dwordx4 v[220:223], v1, s[16:19], s47 offen offset:3072 sc1\n"
        "s_add_u32 s47, s55, 24576\n"
        "buffer_load_dwordx4 v[228:231], v1, s[16:19], s47 offen offset:1024 sc1\n"
        "s_add_u32 s47, s55, 24576\n"
        "buffer_load_dwordx4 v[232:235], v1, s[16:19], s47 offen offset:2048 sc1\n"
        "s_add_u32 s47, s55, 24576\n"
        "buffer_load_dwordx4 v[236:239], v1, s[16:19], s47 offen offset:3072 sc1\n"
        "s_waitcnt vmcnt(0)\n"
        "v_max3_u32 v29, v220, v221, v222\n"
        "v_max3_u32 v29, v223, v228, v29\n"
        "v_max3_u32 v29, v229, v230, v29\n"
        "v_max3_u32 v29, v231, v232, v29\n"
        "v_max3_u32 v29, v233, v234, v29\n"
        "v_max3_u32 v29, v235, v236, v29\n"
        "v_max3_u32 v29, v237, v238, v29\n"
        "v_max_u32 v29, v239, v29\n"
        "v_cmp_eq_u32 vcc, -1, v29\n"
        "s_nop 1\n"
        "s_cbranch_vccz L_x6_go\n"
        "s_sub_u32 s46, s46, 1\n"
        "s_cmp_gt_i32 s46, 0\n"
        "s_cbranch_scc1 L_x6_rl\n"
        "s_branch L_x6_go\n"
        "L_x7_rt:\n"
        "s_mov_b32 s46, 0x40000\n"
        "L_x7_rl:\n"
        "s_nop 7\n"
        "s_nop 7\n"
        "s_add_u32 s47, s55, 28672\n"
        "buffer_load_dwordx4 v[240:243], v1, s[16:19], s47 offen offset:0 sc1\n"
        "s_add_u32 s47, s55, 28672\n"
        "buffer_load_dwordx4 v[244:247], v1, s[16:19], s47 offen offset:1024 sc1\n"
        "s_add_u32 s47, s55, 28672\n"
        "buffer_load_dwordx4 v[248:251], v1, s[16:19], s47 offen offset:2048 sc1\n"
        "s_add_u32 s47, s55, 28672\n"
        "buffer_load_dwordx4 v[252:255], v1, s[16:19], s47 offen offset:3072 sc1\n"
        "s_waitcnt vmcnt(0)\n"
        "v_max3_u32 v29, v240, v241, v242\n"
        "v_max3_u32 v29, v243, v244, v29\n"
        "v_max3_u32 v29, v245, v246, v29\n"
        "v_max3_u32 v29, v247, v248, v29\n"
        "v_max3_u32 v29, v249, v250, v29\n"
        "v_max3_u32 v29, v251, v252, v29\n"
        "v_max3_u32 v29, v253, v254, v29\n"
        "v_max_u32 v29, v255, v29\n"
        "v_cmp_eq_u32 vcc, -1, v29\n"
        "s_nop 1\n"
        "s_cbranch_vccz L_x7_go\n"
        "s_sub_u32 s46, s46, 1\n"
        "s_cmp_gt_i32 s46, 0\n"
        "s_cbranch_scc1 L_x7_rl\n"
        "s_branch L_x7_go\n"
        "L_h24_rt:\n"
        "s_mov_b32 s46, 0x40000\n"
        "L_h24_rl:\n"
        "s_nop 7\n"
        "s_nop 7\n"
        "buffer_load_dwordx4 v[80:83], v1, s[16:19], s60 offen offset:3072 sc1\n"
        "buffer_load_dwordx4 v[84:87], v1, s[16:19], s61 offen offset:3072 sc1\n"
        "buffer_load_dwordx4 v[88:91], v1, s[16:19], s62 offen offset:3072 sc1\n"
        "buffer_load_dwordx4 v[92:95], v1, s[16:19], s63 offen offset:3072 sc1\n"
        "s_waitcnt vmcnt(0)\n"
        "v_max3_u32 v29, v80, v81, v82\n"
        "v_max3_u32 v29, v83, v84, v29\n"
        "v_max3_u32 v29, v85, v86, v29\n"
        "v_max3_u32 v29, v87, v88, v29\n"
        "v_max3_u32 v29, v89, v90, v29\n"
        "v_max3_u32 v29, v91, v92, v29\n"
        "v_max3_u32 v29, v93, v94, v29\n"
        "v_max_u32 v29, v95, v29\n"
        "v_cmp_eq_u32 vcc, -1, v29\n"
        "s_nop 1\n"
        "s_cbranch_vccz L_h24_go\n"
        "s_sub_u32 s46, s46, 1\n"
        "s_cmp_gt_i32 s46, 0\n"
        "s_cbranch_scc1 L_h24_rl\n"
        "s_branch L_h24_go\n"
        "L_h28_rt:\n"
        "s_mov_b32 s46, 0x40000\n"
        "L_h28_rl:\n"
        "s_nop 7\n"
        "s_nop 7\n"
        "buffer_load_dwordx4 v[96:99], v1, s[16:19], s64 offen offset:3072 sc1\n"
        "buffer_load_dwordx4 v[100:103], v1, s[16:19], s65 offen offset:3072 sc1\n"
        "buffer_load_dwordx4 v[104:107], v1, s[16:19], s66 offen offset:3072 sc1\n"
        "buffer_load_dwordx4 v[108:111], v1, s[16:19], s67 offen offset:3072 sc1\n"
        "s_waitcnt vmcnt(0)\n"
        "v_max3_u32 v29, v96, v97, v98\n"
        "v_max3_u32 v29, v99, v100, v29\n"
        "v_max3_u32 v29, v101, v102, v29\n"
        "v_max3_u32 v29, v103, v104, v29\n"
        "v_max3_u32 v29, v105, v106, v29\n"
        "v_max3_u32 v29, v107, v108, v29\n"
        "v_max3_u32 v29, v109, v110, v29\n"
        "v_max_u32 v29, v111, v29\n"
        "v_cmp_eq_u32 vcc, -1, v29\n"
        "s_nop 1\n"
        "s_cbranch_vccz L_h28_go\n"
        "s_sub_u32 s46, s46, 1\n"
        "s_cmp_gt_i32 s46, 0\n"
        "s_cbranch_scc1 L_h28_rl\n"
        "s_branch L_h28_go\n"
        "L_h32_rt:\n"
        "s_mov_b32 s46, 0x40000\n"
        "L_h32_rl:\n"
        "s_nop 7\n"
        "s_nop 7\n"
        "buffer_load_dwordx4 v[112:115], v47, s[16:19], s60 offen offset:0 sc1\n"
        "buffer_load_dwordx4 v[116:119], v47, s[16:19], s61 offen offset:0 sc1\n"
        "buffer_load_dwordx4 v[120:123], v47, s[16:19], s62 offen offset:0 sc1\n"
        "buffer_load_dwordx4 v[124:127], v47, s[16:19], s63 offen offset:0 sc1\n"
        "s_waitcnt vmcnt(0)\n"
        "v_max3_u32 v29, v112, v113, v114\n"
        "v_max3_u32 v29, v115, v116, v29\n"
        "v_max3_u32 v29, v117, v118, v29\n"
        "v_max3_u32 v29, v119, v120, v29\n"
        "v_max3_u32 v29, v121, v122, v29\n"
        "v_max3_u32 v29, v123, v124, v29\n"
        "v_max3_u32 v29, v125, v126, v29\n"
        "v_max_u32 v29, v127, v29\n"
        "v_cmp_eq_u32 vcc, -1, v29\n"
        "s_nop 1\n"
        "s_cbranch_vccz L_h32_go\n"
        "s_sub_u32 s46, s46, 1\n"
        "s_cmp_gt_i32 s46, 0\n"
        "s_cbranch_scc1 L_h32_rl\n"
        "s_branch L_h32_go\n"
        "L_h36_rt:\n"
        "s_mov_b32 s46, 0x40000\n"
        "L_h36_rl:\n"
        "s_nop 7\n"
        "s_nop 7\n"
        "buffer_load_dwordx4 v[128:131], v47, s[16:19], s64 offen offset:0 sc1\n"
        "buffer_load_dwordx4 v[132:135], v47, s[16:19], s65 offen offset:0 sc1\n"
        "buffer_load_dwordx4 v[136:139], v47, s[16:19], s66 offen offset:0 sc1\n"
        "buffer_load_dwordx4 v[140:143], v47, s[16:19], s67 offen offset:0 sc1\n"
        "s_waitcnt vmcnt(0)\n"
        "v_max3_u32 v29, v128, v129, v130\n"
        "v_max3_u32 v29, v131, v132, v29\n"
        "v_max3_u32 v29, v133, v134, v29\n"
        "v_max3_u32 v29, v135, v136, v29\n"
        "v_max3_u32 v29, v137, v138, v29\n"
        "v_max3_u32 v29, v139, v140, v29\n"
        "v_max3_u32 v29, v141, v142, v29\n"
        "v_max_u32 v29, v143, v29\n"
        "v_cmp_eq_u32 vcc, -1, v29\n"
        "s_nop 1\n"
        "s_cbranch_vccz L_h36_go\n"
        "s_sub_u32 s46, s46, 1\n"
        "s_cmp_gt_i32 s46, 0\n"
        "s_cbranch_scc1 L_h36_rl\n"
        "s_branch L_h36_go\n"
        "L_h40_rt:\n"
        "s_mov_b32 s46, 0x40000\n"
        "L_h40_rl:\n"
        "s_nop 7\n"
        "s_nop 7\n"
        "buffer_load_dwordx4 v[144:147], v47, s[16:19], s60 offen offset:1024 sc1\n"
        "buffer_load_dwordx4 v[148:151], v47, s[16:19], s61 offen offset:1024 sc1\n"
        "buffer_load_dwordx4 v[152:155], v47, s[16:19], s62 offen offset:1024 sc1\n"
        "buffer_load_dwordx4 v[156:159], v47, s[16:19], s63 offen offset:1024 sc1\n"
        "s_waitcnt vmcnt(0)\n"
        "v_max3_u32 v29, v144, v145, v146\n"
        "v_max3_u32 v29, v147, v148, v29\n"
        "v_max3_u32 v29, v149, v150, v29\n"
        "v_max3_u32 v29, v151, v152, v29\n"
        "v_max3_u32 v29, v153, v154, v29\n"
        "v_max3_u32 v29, v155, v156, v29\n"
        "v_max3_u32 v29, v157, v158, v29\n"
        "v_max_u32 v29, v159, v29\n"
        "v_cmp_eq_u32 vcc, -1, v29\n"
        "s_nop 1\n"
        "s_cbranch_vccz L_h40_go\n"
        "s_sub_u32 s46, s46, 1\n"
        "s_cmp_gt_i32 s46, 0\n"
        "s_cbranch_scc1 L_h40_rl\n"
        "s_branch L_h40_go\n"
        "L_h44_rt:\n"
        "s_mov_b32 s46, 0x40000\n"
        "L_h44_rl:\n"
        "s_nop 7\n"
        "s_nop 7\n"
        "buffer_load_dwordx4 v[160:163], v47, s[16:19], s64 offen offset:1024 sc1\n"
        "buffer_load_dwordx4 v[164:167], v47, s[16:19], s65 offen offset:1024 sc1\n"
        "buffer_load_dwordx4 v[168:171], v47, s[16:19], s66 offen offset:1024 sc1\n"
        "buffer_load_dwordx4 v[172:175], v47, s[16:19], s67 offen offset:1024 sc1\n"
        "s_waitcnt vmcnt(0)\n"
        "v_max3_u32 v29, v160, v161, v162\n"
        "v_max3_u32 v29, v163, v164, v29\n"
        "v_max3_u32 v29, v165, v166, v29\n"
        "v_max3_u32 v29, v167, v168, v29\n"
        "v_max3_u32 v29, v169, v170, v29\n"
        "v_max3_u32 v29, v171, v172, v29\n"
        "v_max3_u32 v29, v173, v174, v29\n"
        "v_max_u32 v29, v175, v29\n"
        "v_cmp_eq_u32 vcc, -1, v29\n"
        "s_nop 1\n"
        "s_cbranch_vccz L_h44_go\n"
        "s_sub_u32 s46, s46, 1\n"
        "s_cmp_gt_i32 s46, 0\n"
        "s_cbranch_scc1 L_h44_rl\n"
        "s_branch L_h44_go\n"
        "L_h48_rt:\n"
        "s_mov_b32 s46, 0x40000\n"
        "L_h48_rl:\n"
        "s_nop 7\n"
        "s_nop 7\n"
        "buffer_load_dwordx4 v[176:179], v47, s[16:19], s60 offen offset:2048 sc1\n"
        "buffer_load_dwordx4 v[180:183], v47, s[16:19], s61 offen offset:2048 sc1\n"
        "buffer_load_dwordx4 v[184:187], v47, s[16:19], s62 offen offset:2048 sc1\n"
        "buffer_load_dwordx4 v[188:191], v47, s[16:19], s63 offen offset:2048 sc1\n"
        "s_waitcnt vmcnt(0)\n"
        "v_max3_u32 v29, v176, v177, v178\n"
        "v_max3_u32 v29, v179, v180, v29\n"
        "v_max3_u32 v29, v181, v182, v29\n"
        "v_max3_u32 v29, v183, v184, v29\n"
        "v_max3_u32 v29, v185, v186, v29\n"
        "v_max3_u32 v29, v187, v188, v29\n"
        "v_max3_u32 v29, v189, v190, v29\n"
        "v_max_u32 v29, v191, v29\n"
        "v_cmp_eq_u32 vcc, -1, v29\n"
        "s_nop 1\n"
        "s_cbranch_vccz L_h48_go\n"
        "s_sub_u32 s46, s46, 1\n"
        "s_cmp_gt_i32 s46, 0\n"
        "s_cbranch_scc1 L_h48_rl\n"
        "s_branch L_h48_go\n"
        "L_h52_rt:\n"
        "s_mov_b32 s46, 0x40000\n"
        "L_h52_rl:\n"
        "s_nop 7\n"
        "s_nop 7\n"
        "buffer_load_dwordx4 v[48:51], v47, s[16:19], s64 offen offset:2048 sc1\n"
        "buffer_load_dwordx4 v[52:55], v47, s[16:19], s65 offen offset:2048 sc1\n"
        "buffer_load_dwordx4 v[56:59], v47, s[16:19], s66 offen offset:2048 sc1\n"
        "buffer_load_dwordx4 v[60:63], v47, s[16:19], s67 offen offset:2048 sc1\n"
        "s_waitcnt vmcnt(0)\n"
        "v_max3_u32 v29, v48, v49, v50\n"
        "v_max3_u32 v29, v51, v52, v29\n"
        "v_max3_u32 v29, v53, v54, v29\n"
        "v_max3_u32 v29, v55, v56, v29\n"
        "v_max3_u32 v29, v57, v58, v29\n"
        "v_max3_u32 v29, v59, v60, v29\n"
        "v_max3_u32 v29, v61, v62, v29\n"
        "v_max_u32 v29, v63, v29\n"
        "v_cmp_eq_u32 vcc, -1, v29\n"
        "s_nop 1\n"
        "s_cbranch_vccz L_h52_go\n"
        "s_sub_u32 s46, s46, 1\n"
        "s_cmp_gt_i32 s46, 0\n"
        "s_cbranch_scc1 L_h52_rl\n"
        "s_branch L_h52_go\n"
        "L_st_rt:\n"
        "s_mov_b32 s46, 0x40000\n"
        "L_st_rl:\n"
        "s_nop 7\n"
        "s_nop 7\n"
        "buffer_load_dwordx2 v[8:9], v18, s[16:19], s38 offen offset:0 sc1\n"
        "buffer_load_dwordx2 v[10:11], v18, s[16:19], s38 offen offset:256 sc1\n"
        "buffer_load_dwordx2 v[12:13], v18, s[16:19], s38 offen offset:512 sc1\n"
        "buffer_load_dwordx2 v[14:15], v18, s[16:19], s38 offen offset:768 sc1\n"
        "s_waitcnt vmcnt(0)\n"
        "v_max3_u32 v29, v8, v9, v10\n"
        "v_max3_u32 v29, v11, v12, v29\n"
        "v_max3_u32 v29, v13, v14, v29\n"
        "v_max_u32 v29, v15, v29\n"
        "v_cmp_eq_u32 vcc, -1, v29\n"
        "s_nop 1\n"
        "s_cbranch_vccz L_st_af\n"
        "s_sub_u32 s46, s46, 1\n"
        "s_cmp_gt_i32 s46, 0\n"
        "s_cbranch_scc1 L_st_rl\n"
        "L_st_af:\n"
        "s_add_u32 s47, s35, 0\n"
        "buffer_load_dwordx4 v[128:131], v1, s[16:19], s47 offen offset:0 sc1\n"
        "buffer_load_dwordx4 v[132:135], v1, s[16:19], s47 offen offset:1024 sc1\n"
        "buffer_load_dwordx4 v[136:139], v1, s[16:19], s47 offen offset:2048 sc1\n"
        "buffer_load_dwordx4 v[140:143], v1, s[16:19], s47 offen offset:3072 sc1\n"
        "s_add_u32 s47, s35, 4096\n"
        "buffer_load_dwordx4 v[144:147], v1, s[16:19], s47 offen offset:0 sc1\n"
        "buffer_load_dwordx4 v[148:151], v1, s[16:19], s47 offen offset:1024 sc1\n"
        "buffer_load_dwordx4 v[152:155], v1, s[16:19], s47 offen offset:2048 sc1\n"
        "buffer_load_dwordx4 v[156:159], v1, s[16:19], s47 offen offset:3072 sc1\n"
        "s_add_u32 s47, s35, 8192\n"
        "buffer_load_dwordx4 v[160:163], v1, s[16:19], s47 offen offset:0 sc1\n"
        "buffer_load_dwordx4 v[164:167], v1, s[16:19], s47 offen offset:1024 sc1\n"
        "buffer_load_dwordx4 v[168:171], v1, s[16:19], s47 offen offset:2048 sc1\n"
        "buffer_load_dwordx4 v[172:175], v1, s[16:19], s47 offen offset:3072 sc1\n"
        "s_add_u32 s47, s35, 12288\n"
        "buffer_load_dwordx4 v[176:179], v1, s[16:19], s47 offen offset:0 sc1\n"
        "buffer_load_dwordx4 v[180:183], v1, s[16:19], s47 offen offset:1024 sc1\n"
        "buffer_load_dwordx4 v[184:187], v1, s[16:19], s47 offen offset:2048 sc1\n"
        "buffer_load_dwordx4 v[188:191], v1, s[16:19], s47 offen offset:3072 sc1\n"
        "s_branch L_st_go\n"
        "L_d0_rt:\n"
        "s_mov_b32 s46, 0x40000\n"
        "L_d0_rl:\n"
        "s_nop 7\n"
        "s_nop 7\n"
        "s_add_u32 s47, s35, 0\n"
        "buffer_load_dwordx4 v[128:131], v1, s[16:19], s47 offen offset:0 sc1\n"
        "buffer_load_dwordx4 v[132:135], v1, s[16:19], s47 offen offset:1024 sc1\n"
        "buffer_load_dwordx4 v[136:139], v1, s[16:19], s47 offen offset:2048 sc1\n"
        "buffer_load_dwordx4 v[140:143], v1, s[16:19], s47 offen offset:3072 sc1\n"
        "s_waitcnt vmcnt(0)\n"
        "v_max3_u32 v29, v128, v129, v130\n"
        "v_max3_u32 v29, v131, v132, v29\n"
        "v_max3_u32 v29, v133, v134, v29\n"
        "v_max3_u32 v29, v135, v136, v29\n"
        "v_max3_u32 v29, v137, v138, v29\n"
        "v_max3_u32 v29, v139, v140, v29\n"
        "v_max3_u32 v29, v141, v142, v29\n"
        "v_max_u32 v29, v143, v29\n"
        "v_cmp_eq_u32 vcc, -1, v29\n"
        "s_nop 1\n"
        "s_cbranch_vccz L_d0_go\n"
        "s_sub_u32 s46, s46, 1\n"
        "s_cmp_gt_i32 s46, 0\n"
        "s_cbranch_scc1 L_d0_rl\n"
        "s_branch L_d0_go\n"
        "L_d1_rt:\n"
        "s_mov_b32 s46, 0x40000\n"
        "L_d1_rl:\n"
        "s_nop 7\n"
        "s_nop 7\n"
        "s_add_u32 s47, s35, 4096\n"
        "buffer_load_dwordx4 v[144:147], v1, s[16:19], s47 offen offset:0 sc1\n"
        "buffer_load_dwordx4 v[148:151], v1, s[16:19], s47 offen offset:1024 sc1\n"
        "buffer_load_dwordx4 v[152:155], v1, s[16:19], s47 offen offset:2048 sc1\n"
        "buffer_load_dwordx4 v[156:159], v1, s[16:19], s47 offen offset:3072 sc1\n"
        "s_waitcnt vmcnt(0)\n"
        "v_max3_u32 v29, v144, v145, v146\n"
        "v_max3_u32 v29, v147, v148, v29\n"
        "v_max3_u32 v29, v149, v150, v29\n"
        "v_max3_u32 v29, v151, v152, v29\n"
        "v_max3_u32 v29, v153, v154, v29\n"
        "v_max3_u32 v29, v155, v156, v29\n"
        "v_max3_u32 v29, v157, v158, v29\n"
        "v_max_u32 v29, v159, v29\n"
        "v_cmp_eq_u32 vcc, -1, v29\n"
        "s_nop 1\n"
        "s_cbranch_vccz L_d1_go\n"
        "s_sub_u32 s46, s46, 1\n"
        "s_cmp_gt_i32 s46, 0\n"
        "s_cbranch_scc1 L_d1_rl\n"
        "s_branch L_d1_go\n"
        "L_d2_rt:\n"
        "s_mov_b32 s46, 0x40000\n"
        "L_d2_rl:\n"
        "s_nop 7\n"
        "s_nop 7\n"
        "s_add_u32 s47, s35, 8192\n"
        "buffer_load_dwordx4 v[160:163], v1, s[16:19], s47 offen offset:0 sc1\n"
        "buffer_load_dwordx4 v[164:167], v1, s[16:19], s47 offen offset:1024 sc1\n"
        "buffer_load_dwordx4 v[168:171], v1, s[16:19], s47 offen offset:2048 sc1\n"
        "buffer_load_dwordx4 v[172:175], v1, s[16:19], s47 offen offset:3072 sc1\n"
        "s_waitcnt vmcnt(0)\n"
        "v_max3_u32 v29, v160, v161, v162\n"
        "v_max3_u32 v29, v163, v164, v29\n"
        "v_max3_u32 v29, v165, v166, v29\n"
        "v_max3_u32 v29, v167, v168, v29\n"
        "v_max3_u32 v29, v169, v170, v29\n"
        "v_max3_u32 v29, v171, v172, v29\n"
        "v_max3_u32 v29, v173, v174, v29\n"
        "v_max_u32 v29, v175, v29\n"
        "v_cmp_eq_u32 vcc, -1, v29\n"
        "s_nop 1\n"
        "s_cbranch_vccz L_d2_go\n"
        "s_sub_u32 s46, s46, 1\n"
        "s_cmp_gt_i32 s46, 0\n"
        "s_cbranch_scc1 L_d2_rl\n"
        "s_branch L_d2_go\n"
        "L_d3_rt:\n"
        "s_mov_b32 s46, 0x40000\n"
        "L_d3_rl:\n"
        "s_nop 7\n"
        "s_nop 7\n"
        "s_add_u32 s47, s35, 12288\n"
        "buffer_load_dwordx4 v[176:179], v1, s[16:19], s47 offen offset:0 sc1\n"
        "buffer_load_dwordx4 v[180:183], v1, s[16:19], s47 offen offset:1024 sc1\n"
        "buffer_load_dwordx4 v[184:187], v1, s[16:19], s47 offen offset:2048 sc1\n"
        "buffer_load_dwordx4 v[188:191], v1, s[16:19], s47 offen offset:3072 sc1\n"
        "s_waitcnt vmcnt(0)\n"
        "v_max3_u32 v29, v176, v177, v178\n"
        "v_max3_u32 v29, v179, v180, v29\n"
        "v_max3_u32 v29, v181, v182, v29\n"
        "v_max3_u32 v29, v183, v184, v29\n"
        "v_max3_u32 v29, v185, v186, v29\n"
        "v_max3_u32 v29, v187, v188, v29\n"
        "v_max3_u32 v29, v189, v190, v29\n"
        "v_max_u32 v29, v191, v29\n"
        "v_cmp_eq_u32 vcc, -1, v29\n"
        "s_nop 1\n"
        "s_cbranch_vccz L_d3_go\n"
        "s_sub_u32 s46, s46, 1\n"
        "s_cmp_gt_i32 s46, 0\n"
        "s_cbranch_scc1 L_d3_rl\n"
        "s_branch L_d3_go\n"
        "L_hot_end:\n"
        "L_slow:\n"
        "buffer_load_dwordx4 a[20:23], v1, s[16:19], s33 offen offset:2048\n"
        "s_mov_b64 exec, s[46:47]\n"
        "buffer_load_dwordx4 a[104:107], v4, s[16:19], s33 offen offset:2048\n"
        "buffer_load_dwordx4 a[108:111], v4, s[16:19], s33 offen offset:2560\n"
        "s_mov_b64 exec, -1\n"
        "s_add_u32 s33, s33, 4096\n"
        "buffer_load_dwordx4 a[24:27], v1, s[16:19], s33 offen offset:0\n"
        "s_mov_b64 exec, s[46:47]\n"
        "buffer_load_dwordx4 a[112:115], v4, s[16:19], s33 offen offset:0\n"
        "buffer_load_dwordx4 a[116:119], v4, s[16:19], s33 offen offset:512\n"
        "s_mov_b64 exec, -1\n"
        "buffer_load_dwordx4 a[28:31], v1, s[16:19], s33 offen offset:2048\n"
        "s_mov_b64 exec, s[46:47]\n"
        "buffer_load_dwordx4 a[120:123], v4, s[16:19], s33 offen offset:2048\n"
        "buffer_load_dwordx4 a[124:127], v4, s[16:19], s33 offen offset:2560\n"
        "s_mov_b64 exec, -1\n"
        "s_add_u32 s33, s33, 4096\n"
        "buffer_load_dwordx4 a[32:35], v1, s[16:19], s33 offen offset:0\n"
        "s_mov_b64 exec, s[46:47]\n"
        "buffer_load_dwordx4 a[128:131], v4, s[16:19], s33 offen offset:0\n"
        "buffer_load_dwordx4 a[132:135], v4, s[16:19], s33 offen offset:512\n"
        "s_mov_b64 exec, -1\n"
        "buffer_load_dwordx4 a[36:39], v1, s[16:19], s33 offen offset:2048\n"
        "s_mov_b64 exec, s[46:47]\n"
        "buffer_load_dwordx4 a[136:139], v4, s[16:19], s33 offen offset:2048\n"
        "buffer_load_dwordx4 a[140:143], v4, s[16:19], s33 offen offset:2560\n"
        "s_mov_b64 exec, -1\n"
        "s_add_u32 s33, s33, 4096\n"
        "buffer_load_dwordx4 a[40:43], v1, s[16:19], s33 offen offset:0\n"
        "s_mov_b64 exec, s[46:47]\n"
        "buffer_load_dwordx4 a[144:147], v4, s[16:19], s33 offen offset:0\n"
        "buffer_load_dwordx4 a[148:151], v4, s[16:19], s33 offen offset:512\n"
        "s_mov_b64 exec, -1\n"
        "buffer_load_dwordx4 a[44:47], v1, s[16:19], s33 offen offset:2048\n"
        "s_mov_b64 exec, s[46:47]\n"
        "buffer_load_dwordx4 a[152:155], v4, s[16:19], s33 offen offset:2048\n"
        "buffer_load_dwordx4 a[156:159], v4, s[16:19], s33 offen offset:2560\n"
        "s_mov_b64 exec, -1\n"
        "s_add_u32 s33, s33, 4096\n"
        "buffer_load_dwordx4 a[48:51], v1, s[16:19], s33 offen offset:0\n"
        "s_mov_b64 exec, s[46:47]\n"
        "buffer_load_dwordx4 a[160:163], v4, s[16:19], s33 offen offset:0\n"
        "buffer_load_dwordx4 a[164:167], v4, s[16:19], s33 offen offset:512\n"
        "s_mov_b64 exec, -1\n"
        "buffer_load_dwordx4 a[52:55], v1, s[16:19], s33 offen offset:2048\n"
        "s_mov_b64 exec, s[46:47]\n"
        "buffer_load_dwordx4 a[168:171], v4, s[16:19], s33 offen offset:2048\n"
        "buffer_load_dwordx4 a[172:175], v4, s[16:19], s33 offen offset:2560\n"
        "s_mov_b64 exec, -1\n"
        "s_add_u32 s33, s33, 4096\n"
        "buffer_load_dwordx4 a[56:59], v1, s[16:19], s33 offen offset:0\n"
        "s_mov_b64 exec, s[46:47]\n"
        "buffer_load_dwordx4 a[176:179], v4, s[16:19], s33 offen offset:0\n"
        "buffer_load_dwordx4 a[180:183], v4, s[16:19], s33 offen offset:512\n"
        "s_mov_b64 exec, -1\n"
        "buffer_load_dwordx4 a[60:63], v1, s[16:19], s33 offen offset:2048\n"
        "s_mov_b64 exec, s[46:47]\n"
        "buffer_load_dwordx4 a[184:187], v4, s[16:19], s33 offen offset:2048\n"
        "buffer_load_dwordx4 a[188:191], v4, s[16:19], s33 offen offset:2560\n"
        "s_mov_b64 exec, -1\n"
        "s_mul_i32 s41, s28, 0x9000\n"
        "s_add_u32 m0, s41, 0x0\n"
        "s_add_u32 s42, m0, s43\n"
        "buffer_load_dwordx4 v1, s[16:19], s42 offen offset:0 lds\n"
        "buffer_load_dwordx4 v1, s[16:19], s42 offen offset:1024 lds\n"
        "buffer_load_dwordx4 v1, s[16:19], s42 offen offset:2048 lds\n"
        "buffer_load_dwordx4 v1, s[16:19], s42 offen offset:3072 lds\n"
        "s_add_u32 m0, s41, 0x1000\n"
        "s_add_u32 s42, m0, s43\n"
        "buffer_load_dwordx4 v1, s[16:19], s42 offen offset:0 lds\n"
        "buffer_load_dwordx4 v1, s[16:19], s42 offen offset:1024 lds\n"
        "buffer_load_dwordx4 v1, s[16:19], s42 offen offset:2048 lds\n"
        "buffer_load_dwordx4 v1, s[16:19], s42 offen offset:3072 lds\n"
        "s_add_u32 m0, s41, 0x2000\n"
        "s_add_u32 s42, m0, s43\n"
        "buffer_load_dwordx4 v1, s[16:19], s42 offen offset:0 lds\n"
        "buffer_load_dwordx4 v1, s[16:19], s42 offen offset:1024 lds\n"
        "buffer_load_dwordx4 v1, s[16:19], s42 offen offset:2048 lds\n"
        "buffer_load_dwordx4 v1, s[16:19], s42 offen offset:3072 lds\n"
        "s_add_u32 m0, s41, 0x3000\n"
        "s_add_u32 s42, m0, s43\n"
        "buffer_load_dwordx4 v1, s[16:19], s42 offen offset:0 lds\n"
        "buffer_load_dwordx4 v1, s[16:19], s42 offen offset:1024 lds\n"
        "buffer_load_dwordx4 v1, s[16:19], s42 offen offset:2048 lds\n"
        "buffer_load_dwordx4 v1, s[16:19], s42 offen offset:3072 lds\n"
        "s_add_u32 m0, s41, 0x4000\n"
        "s_add_u32 s42, m0, s43\n"
        "buffer_load_dwordx4 v1, s[16:19], s42 offen offset:0 lds\n"
        "buffer_load_dwordx4 v1, s[16:19], s42 offen offset:1024 lds\n"
        "buffer_load_dwordx4 v1, s[16:19], s42 offen offset:2048 lds\n"
        "buffer_load_dwordx4 v1, s[16:19], s42 offen offset:3072 lds\n"
        "s_add_u32 m0, s41, 0x5000\n"
        "s_add_u32 s42, m0, s43\n"
        "buffer_load_dwordx4 v1, s[16:19], s42 offen offset:0 lds\n"
        "buffer_load_dwordx4 v1, s[16:19], s42 offen offset:1024 lds\n"
        "buffer_load_dwordx4 v1, s[16:19], s42 offen offset:2048 lds\n"
        "buffer_load_dwordx4 v1, s[16:19], s42 offen offset:3072 lds\n"
        "s_add_u32 m0, s41, 0x6000\n"
        "s_add_u32 s42, m0, s43\n"
        "buffer_load_dwordx4 v1, s[16:19], s42 offen offset:0 lds\n"
        "buffer_load_dwordx4 v1, s[16:19], s42 offen offset:1024 lds\n"
        "buffer_load_dwordx4 v1, s[16:19], s42 offen offset:2048 lds\n"
        "buffer_load_dwordx4 v1, s[16:19], s42 offen offset:3072 lds\n"
        "s_add_u32 m0, s41, 0x7000\n"
        "s_add_u32 s42, m0, s43\n"
        "buffer_load_dwordx4 v1, s[16:19], s42 offen offset:0 lds\n"
        "buffer_load_dwordx4 v1, s[16:19], s42 offen offset:1024 lds\n"
        "buffer_load_dwordx4 v1, s[16:19], s42 offen offset:2048 lds\n"
        "buffer_load_dwordx4 v1, s[16:19], s42 offen offset:3072 lds\n"
        "s_add_u32 m0, s41, 0x8000\n"
        "s_add_u32 s42, m0, s43\n"
        "buffer_load_dwordx4 v1, s[16:19], s42 offen offset:0 lds\n"
        "buffer_load_dwordx4 v1, s[16:19], s42 offen offset:1024 lds\n"
        "buffer_load_dwordx4 v1, s[16:19], s42 offen offset:2048 lds\n"
        "buffer_load_dwordx4 v1, s[16:19], s42 offen offset:3072 lds\n"
        "s_waitcnt vmcnt(0)\n"
        "v_lshlrev_b32 v160, 16, v220\n"
        "v_and_b32 v161, 0xffff0000, v220\n"
        "v_lshlrev_b32 v176, 16, v220\n"
        "v_and_b32 v177, 0xffff0000, v220\n"
        "v_lshlrev_b32 v162, 16, v221\n"
        "v_and_b32 v163, 0xffff0000, v221\n"
        "v_lshlrev_b32 v178, 16, v221\n"
        "v_and_b32 v179, 0xffff0000, v221\n"
        "v_lshlrev_b32 v164, 16, v222\n"
        "v_and_b32 v165, 0xffff0000, v222\n"
        "v_lshlrev_b32 v180, 16, v222\n"
        "v_and_b32 v181, 0xffff0000, v222\n"
        "v_lshlrev_b32 v166, 16, v223\n"
        "v_and_b32 v167, 0xffff0000, v223\n"
        "v_lshlrev_b32 v182, 16, v223\n"
        "v_and_b32 v183, 0xffff0000, v223\n"
        "v_lshlrev_b32 v168, 16, v224\n"
        "v_and_b32 v169, 0xffff0000, v224\n"
        "v_lshlrev_b32 v184, 16, v224\n"
        "v_and_b32 v185, 0xffff0000, v224\n"
        "v_lshlrev_b32 v170, 16, v225\n"
        "v_and_b32 v171, 0xffff0000, v225\n"
        "v_lshlrev_b32 v186, 16, v225\n"
        "v_and_b32 v187, 0xffff0000, v225\n"
        "v_lshlrev_b32 v172, 16, v226\n"
        "v_and_b32 v173, 0xffff0000, v226\n"
        "v_lshlrev_b32 v188, 16, v226\n"
        "v_and_b32 v189, 0xffff0000, v226\n"
        "v_lshlrev_b32 v174, 16, v227\n"
        "v_and_b32 v175, 0xffff0000, v227\n"
        "v_lshlrev_b32 v190, 16, v227\n"
        "v_and_b32 v191, 0xffff0000, v227\n"
        "v_mov_b32 v16, -1\n"
        "v_mov_b32 v17, -1\n"
        "v_mov_b32 v18, -1\n"
        "v_mov_b32 v19, -1\n"
        "buffer_store_dwordx4 v[16:19], v1, s[16:19], s56 offen offset:3072 sc1\n"
        "buffer_store_dwordx4 v[16:19], v24, s[16:19], s56 offen offset:0 sc1\n"
        "buffer_store_dwordx4 v[16:19], v24, s[16:19], s56 offen offset:1024 sc1\n"
        "buffer_store_dwordx4 v[16:19], v24, s[16:19], s56 offen offset:2048 sc1\n"
        "s_cmp_eq_u32 s31, 0\n"
        "s_cselect_b32 s22, s18, 0\n"
        "s_mov_b64 s[20:21], s[16:17]\n"
        "s_lshl_b32 s38, s51, 15\n"
        "s_lshl_b32 s39, s40, 10\n"
        "s_add_u32 s38, s38, s39\n"
        "s_add_u32 s38, s38, 0x1112000\n"
        "buffer_store_dwordx4 v[16:19], v1, s[20:23], s38 offen sc1\n"
        "s_add_u32 s38, s38, 16384\n"
        "buffer_store_dwordx4 v[16:19], v1, s[20:23], s38 offen sc1\n"
        "s_lshl_b32 s38, s51, 12\n"
        "s_lshl_b32 s39, s40, 8\n"
        "s_add_u32 s38, s38, s39\n"
        "s_add_u32 s38, s38, 0x92000\n"
        "s_mov_b64 exec, 0xffff\n"
        "buffer_store_dwordx4 v[16:19], v1, s[20:23], s38 offen sc1\n"
        "s_mov_b64 exec, -1\n"
        "s_lshl_b32 s38, s52, 2\n"
        "s_add_u32 s38, s38, s28\n"
        "s_lshl_b32 s38, s38, 11\n"
        "s_add_u32 s38, s38, 0xf12000\n"
        "buffer_store_dwordx4 v[16:19], v1, s[20:23], s38 offen offset:0 sc1\n"
        "buffer_store_dwordx4 v[16:19], v1, s[20:23], s38 offen offset:1024 sc1\n"
        "s_mov_b32 s57, 0\n"
        "L_slow_q:\n"
        "s_waitcnt vmcnt(0)\n"
        "s_lshl_b32 s36, s57, 10\n"
        "s_add_u32 s36, s36, s59\n"
        "buffer_load_dwordx4 v[32:35], v1, s[16:19], s36 offen\n"
        "s_lshl_b32 s36, s57, 14\n"
        "s_add_u32 s36, s36, s35\n"
        "buffer_load_dwordx4 a[192:195], v1, s[16:19], s36 offen offset:0\n"
        "buffer_load_dwordx4 a[196:199], v1, s[16:19], s36 offen offset:1024\n"
        "buffer_load_dwordx4 a[200:203], v1, s[16:19], s36 offen offset:2048\n"
        "buffer_load_dwordx4 a[204:207], v1, s[16:19], s36 offen offset:3072\n"
        "buffer_load_dwordx4 a[208:211], v24, s[16:19], s36 offen offset:0\n"
        "buffer_load_dwordx4 a[212:215], v24, s[16:19], s36 offen offset:1024\n"
        "buffer_load_dwordx4 a[216:219], v24, s[16:19], s36 offen offset:2048\n"
        "buffer_load_dwordx4 a[220:223], v24, s[16:19], s36 offen offset:3072\n"
        "buffer_load_dwordx4 a[224:227], v25, s[16:19], s36 offen offset:0\n"
        "buffer_load_dwordx4 a[228:231], v25, s[16:19], s36 offen offset:1024\n"
        "buffer_load_dwordx4 a[232:235], v25, s[16:19], s36 offen offset:2048\n"
        "buffer_load_dwordx4 a[236:239], v25, s[16:19], s36 offen offset:3072\n"
        "buffer_load_dwordx4 a[240:243], v26, s[16:19], s36 offen offset:0\n"
        "buffer_load_dwordx4 a[244:247], v26, s[16:19], s36 offen offset:1024\n"
        "buffer_load_dwordx4 a[248:251], v26, s[16:19], s36 offen offset:2048\n"
        "buffer_load_dwordx4 a[252:255], v26, s[16:19], s36 offen offset:3072\n"
        "v_mov_b32 v216, 0xff800000\n"
        "v_mov_b32 v128, 0\n"
        "v_mov_b32 v129, 0\n"
        "v_mov_b32 v130, 0\n"
        "v_mov_b32 v131, 0\n"
        "v_mov_b32 v132, 0\n"
        "v_mov_b32 v133, 0\n"
        "v_mov_b32 v134, 0\n"
        "v_mov_b32 v135, 0\n"
        "v_mov_b32 v136, 0\n"
        "v_mov_b32 v137, 0\n"
        "v_mov_b32 v138, 0\n"
        "v_mov_b32 v139, 0\n"
        "v_mov_b32 v140, 0\n"
        "v_mov_b32 v141, 0\n"
        "v_mov_b32 v142, 0\n"
        "v_mov_b32 v143, 0\n"
        "s_waitcnt vmcnt(16)\n"
        "v_mfma_f32_32x32x16_bf16 v[80:95], a[0:3], v[32:35], 0\n"
        "s_nop 11\n"
        "v_max3_f32 v217, v80, v81, v216\n"
        "v_max3_f32 v217, v82, v83, v217\n"
        "v_max3_f32 v217, v84, v85, v217\n"
        "v_max3_f32 v217, v86, v87, v217\n"
        "v_max3_f32 v217, v88, v89, v217\n"
        "v_max3_f32 v217, v90, v91, v217\n"
        "v_max3_f32 v217, v92, v93, v217\n"
        "v_max3_f32 v217, v94, v95, v217\n"
        "v_mov_b32 v219, v217\n"
        "s_nop 1\n"
        "v_permlane32_swap_b32 v217, v219\n"
        "v_max_f32 v217, v217, v219\n"
        "v_sub_f32 v218, v216, v217\n"
        "v_exp_f32 v218, v218\n"
        "v_mov_b32 v216, v217\n"
        "v_sub_f32 v80, v80, v217\n"
        "v_sub_f32 v81, v81, v217\n"
        "v_sub_f32 v82, v82, v217\n"
        "v_sub_f32 v83, v83, v217\n"
        "v_sub_f32 v84, v84, v217\n"
        "v_sub_f32 v85, v85, v217\n"
        "v_sub_f32 v86, v86, v217\n"
        "v_sub_f32 v87, v87, v217\n"
        "v_sub_f32 v88, v88, v217\n"
        "v_sub_f32 v89, v89, v217\n"
        "v_sub_f32 v90, v90, v217\n"
        "v_sub_f32 v91, v91, v217\n"
        "v_sub_f32 v92, v92, v217\n"
        "v_sub_f32 v93, v93, v217\n"
        "v_sub_f32 v94, v94, v217\n"
        "v_sub_f32 v95, v95, v217\n"
        "v_exp_f32 v80, v80\n"
        "v_exp_f32 v81, v81\n"
        "v_exp_f32 v82, v82\n"
        "v_exp_f32 v83, v83\n"
        "v_exp_f32 v84, v84\n"
        "v_exp_f32 v85, v85\n"
        "v_exp_f32 v86, v86\n"
        "v_exp_f32 v87, v87\n"
        "v_exp_f32 v88, v88\n"
        "v_exp_f32 v89, v89\n"
        "v_exp_f32 v90, v90\n"
        "v_exp_f32 v91, v91\n"
        "v_exp_f32 v92, v92\n"
        "v_exp_f32 v93, v93\n"
        "v_exp_f32 v94, v94\n"
        "v_exp_f32 v95, v95\n"
        "v_mul_f32 v128, v128, v218\n"
        "v_mul_f32 v129, v129, v218\n"
        "v_mul_f32 v130, v130, v218\n"
        "v_mul_f32 v131, v131, v218\n"
        "v_mul_f32 v132, v132, v218\n"
        "v_mul_f32 v133, v133, v218\n"
        "v_mul_f32 v134, v134, v218\n"
        "v_mul_f32 v135, v135, v218\n"
        "v_mul_f32 v136, v136, v218\n"
        "v_mul_f32 v137, v137, v218\n"
        "v_mul_f32 v138, v138, v218\n"
        "v_mul_f32 v139, v139, v218\n"
        "v_mul_f32 v140, v140, v218\n"
        "v_mul_f32 v141, v141, v218\n"
        "v_mul_f32 v142, v142, v218\n"
        "v_mul_f32 v143, v143, v218\n"
        "v_cvt_pk_bf16_f32 v112, v80, v81\n"
        "v_cvt_pk_bf16_f32 v113, v82, v83\n"
        "v_cvt_pk_bf16_f32 v114, v84, v85\n"
        "v_cvt_pk_bf16_f32 v115, v86, v87\n"
        "v_cvt_pk_bf16_f32 v116, v88, v89\n"
        "v_cvt_pk_bf16_f32 v117, v90, v91\n"
        "v_cvt_pk_bf16_f32 v118, v92, v93\n"
        "v_cvt_pk_bf16_f32 v119, v94, v95\n"
        "v_mfma_f32_32x32x16_bf16 v[128:143], a[64:67], v[112:115], v[128:143]\n"
        "s_nop 0\n"
        "v_mfma_f32_32x32x16_bf16 v[128:143], a[68:71], v[116:119], v[128:143]\n"
        "v_mfma_f32_32x32x16_bf16 v[80:95], a[4:7], v[32:35], 0\n"
        "s_nop 11\n"
        "v_max3_f32 v217, v80, v81, v216\n"
        "v_max3_f32 v217, v82, v83, v217\n"
        "v_max3_f32 v217, v84, v85, v217\n"
        "v_max3_f32 v217, v86, v87, v217\n"
        "v_max3_f32 v217, v88, v89, v217\n"
        "v_max3_f32 v217, v90, v91, v217\n"
        "v_max3_f32 v217, v92, v93, v217\n"
        "v_max3_f32 v217, v94, v95, v217\n"
        "v_mov_b32 v219, v217\n"
        "s_nop 1\n"
        "v_permlane32_swap_b32 v217, v219\n"
        "v_max_f32 v217, v217, v219\n"
        "v_sub_f32 v218, v216, v217\n"
        "v_exp_f32 v218, v218\n"
        "v_mov_b32 v216, v217\n"
        "v_sub_f32 v80, v80, v217\n"
        "v_sub_f32 v81, v81, v217\n"
        "v_sub_f32 v82, v82, v217\n"
        "v_sub_f32 v83, v83, v217\n"
        "v_sub_f32 v84, v84, v217\n"
        "v_sub_f32 v85, v85, v217\n"
        "v_sub_f32 v86, v86, v217\n"
        "v_sub_f32 v87, v87, v217\n"
        "v_sub_f32 v88, v88, v217\n"
        "v_sub_f32 v89, v89, v217\n"
        "v_sub_f32 v90, v90, v217\n"
        "v_sub_f32 v91, v91, v217\n"
        "v_sub_f32 v92, v92, v217\n"
        "v_sub_f32 v93, v93, v217\n"
        "v_sub_f32 v94, v94, v217\n"
        "v_sub_f32 v95, v95, v217\n"
        "v_exp_f32 v80, v80\n"
        "v_exp_f32 v81, v81\n"
        "v_exp_f32 v82, v82\n"
        "v_exp_f32 v83, v83\n"
        "v_exp_f32 v84, v84\n"
        "v_exp_f32 v85, v85\n"
        "v_exp_f32 v86, v86\n"
        "v_exp_f32 v87, v87\n"
        "v_exp_f32 v88, v88\n"
        "v_exp_f32 v89, v89\n"
        "v_exp_f32 v90, v90\n"
        "v_exp_f32 v91, v91\n"
        "v_exp_f32 v92, v92\n"
        "v_exp_f32 v93, v93\n"
        "v_exp_f32 v94, v94\n"
        "v_exp_f32 v95, v95\n"
        "v_mul_f32 v128, v128, v218\n"
        "v_mul_f32 v129, v129, v218\n"
        "v_mul_f32 v130, v130, v218\n"
        "v_mul_f32 v131, v131, v218\n"
        "v_mul_f32 v132, v132, v218\n"
        "v_mul_f32 v133, v133, v218\n"
        "v_mul_f32 v134, v134, v218\n"
        "v_mul_f32 v135, v135, v218\n"
        "v_mul_f32 v136, v136, v218\n"
        "v_mul_f32 v137, v137, v218\n"
        "v_mul_f32 v138, v138, v218\n"
        "v_mul_f32 v139, v139, v218\n"
        "v_mul_f32 v140, v140, v218\n"
        "v_mul_f32 v141, v141, v218\n"
        "v_mul_f32 v142, v142, v218\n"
        "v_mul_f32 v143, v143, v218\n"
        "v_cvt_pk_bf16_f32 v112, v80, v81\n"
        "v_cvt_pk_bf16_f32 v113, v82, v83\n"
        "v_cvt_pk_bf16_f32 v114, v84, v85\n"
        "v_cvt_pk_bf16_f32 v115, v86, v87\n"
        "v_cvt_pk_bf16_f32 v116, v88, v89\n"
        "v_cvt_pk_bf16_f32 v117, v90, v91\n"
        "v_cvt_pk_bf16_f32 v118, v92, v93\n"
        "v_cvt_pk_bf16_f32 v119, v94, v95\n"
        "v_mfma_f32_32x32x16_bf16 v[128:143], a[72:75], v[112:115], v[128:143]\n"
        "s_nop 0\n"
        "v_mfma_f32_32x32x16_bf16 v[128:143], a[76:79], v[116:119], v[128:143]\n"
        "v_mfma_f32_32x32x16_bf16 v[80:95], a[8:11], v[32:35], 0\n"
        "s_nop 11\n"
        "v_max3_f32 v217, v80, v81, v216\n"
        "v_max3_f32 v217, v82, v83, v217\n"
        "v_max3_f32 v217, v84, v85, v217\n"
        "v_max3_f32 v217, v86, v87, v217\n"
        "v_max3_f32 v217, v88, v89, v217\n"
        "v_max3_f32 v217, v90, v91, v217\n"
        "v_max3_f32 v217, v92, v93, v217\n"
        "v_max3_f32 v217, v94, v95, v217\n"
        "v_mov_b32 v219, v217\n"
        "s_nop 1\n"
        "v_permlane32_swap_b32 v217, v219\n"
        "v_max_f32 v217, v217, v219\n"
        "v_sub_f32 v218, v216, v217\n"
        "v_exp_f32 v218, v218\n"
        "v_mov_b32 v216, v217\n"
        "v_sub_f32 v80, v80, v217\n"
        "v_sub_f32 v81, v81, v217\n"
        "v_sub_f32 v82, v82, v217\n"
        "v_sub_f32 v83, v83, v217\n"
        "v_sub_f32 v84, v84, v217\n"
        "v_sub_f32 v85, v85, v217\n"
        "v_sub_f32 v86, v86, v217\n"
        "v_sub_f32 v87, v87, v217\n"
        "v_sub_f32 v88, v88, v217\n"
        "v_sub_f32 v89, v89, v217\n"
        "v_sub_f32 v90, v90, v217\n"
        "v_sub_f32 v91, v91, v217\n"
        "v_sub_f32 v92, v92, v217\n"
        "v_sub_f32 v93, v93, v217\n"
        "v_sub_f32 v94, v94, v217\n"
        "v_sub_f32 v95, v95, v217\n"
        "v_exp_f32 v80, v80\n"
        "v_exp_f32 v81, v81\n"
        "v_exp_f32 v82, v82\n"
        "v_exp_f32 v83, v83\n"
        "v_exp_f32 v84, v84\n"
        "v_exp_f32 v85, v85\n"
        "v_exp_f32 v86, v86\n"
        "v_exp_f32 v87, v87\n"
        "v_exp_f32 v88, v88\n"
        "v_exp_f32 v89, v89\n"
        "v_exp_f32 v90, v90\n"
        "v_exp_f32 v91, v91\n"
        "v_exp_f32 v92, v92\n"
        "v_exp_f32 v93, v93\n"
        "v_exp_f32 v94, v94\n"
        "v_exp_f32 v95, v95\n"
        "v_mul_f32 v128, v128, v218\n"
        "v_mul_f32 v129, v129, v218\n"
        "v_mul_f32 v130, v130, v218\n"
        "v_mul_f32 v131, v131, v218\n"
        "v_mul_f32 v132, v132, v218\n"
        "v_mul_f32 v133, v133, v218\n"
        "v_mul_f32 v134, v134, v218\n"
        "v_mul_f32 v135, v135, v218\n"
        "v_mul_f32 v136, v136, v218\n"
        "v_mul_f32 v137, v137, v218\n"
        "v_mul_f32 v138, v138, v218\n"
        "v_mul_f32 v139, v139, v218\n"
        "v_mul_f32 v140, v140, v218\n"
        "v_mul_f32 v141, v141, v218\n"
        "v_mul_f32 v142, v142, v218\n"
        "v_mul_f32 v143, v143, v218\n"
        "v_cvt_pk_bf16_f32 v112, v80, v81\n"
        "v_cvt_pk_bf16_f32 v113, v82, v83\n"
        "v_cvt_pk_bf16_f32 v114, v84, v85\n"
        "v_cvt_pk_bf16_f32 v115, v86, v87\n"
        "v_cvt_pk_bf16_f32 v116, v88, v89\n"
        "v_cvt_pk_bf16_f32 v117, v90, v91\n"
        "v_cvt_pk_bf16_f32 v118, v92, v93\n"
        "v_cvt_pk_bf16_f32 v119, v94, v95\n"
        "v_mfma_f32_32x32x16_bf16 v[128:143], a[80:83], v[112:115], v[128:143]\n"
        "s_nop 0\n"
        "v_mfma_f32_32x32x16_bf16 v[128:143], a[84:87], v[116:119], v[128:143]\n"
        "v_mfma_f32_32x32x16_bf16 v[80:95], a[12:15], v[32:35], 0\n"
        "s_nop 11\n"
        "v_max3_f32 v217, v80, v81, v216\n"
        "v_max3_f32 v217, v82, v83, v217\n"
        "v_max3_f32 v217, v84, v85, v217\n"
        "v_max3_f32 v217, v86, v87, v217\n"
        "v_max3_f32 v217, v88, v89, v217\n"
        "v_max3_f32 v217, v90, v91, v217\n"
        "v_max3_f32 v217, v92, v93, v217\n"
        "v_max3_f32 v217, v94, v95, v217\n"
        "v_mov_b32 v219, v217\n"
        "s_nop 1\n"
        "v_permlane32_swap_b32 v217, v219\n"
        "v_max_f32 v217, v217, v219\n"
        "v_sub_f32 v218, v216, v217\n"
        "v_exp_f32 v218, v218\n"
        "v_mov_b32 v216, v217\n"
        "v_sub_f32 v80, v80, v217\n"
        "v_sub_f32 v81, v81, v217\n"
        "v_sub_f32 v82, v82, v217\n"
        "v_sub_f32 v83, v83, v217\n"
        "v_sub_f32 v84, v84, v217\n"
        "v_sub_f32 v85, v85, v217\n"
        "v_sub_f32 v86, v86, v217\n"
        "v_sub_f32 v87, v87, v217\n"
        "v_sub_f32 v88, v88, v217\n"
        "v_sub_f32 v89, v89, v217\n"
        "v_sub_f32 v90, v90, v217\n"
        "v_sub_f32 v91, v91, v217\n"
        "v_sub_f32 v92, v92, v217\n"
        "v_sub_f32 v93, v93, v217\n"
        "v_sub_f32 v94, v94, v217\n"
        "v_sub_f32 v95, v95, v217\n"
        "v_exp_f32 v80, v80\n"
        "v_exp_f32 v81, v81\n"
        "v_exp_f32 v82, v82\n"
        "v_exp_f32 v83, v83\n"
        "v_exp_f32 v84, v84\n"
        "v_exp_f32 v85, v85\n"
        "v_exp_f32 v86, v86\n"
        "v_exp_f32 v87, v87\n"
        "v_exp_f32 v88, v88\n"
        "v_exp_f32 v89, v89\n"
        "v_exp_f32 v90, v90\n"
        "v_exp_f32 v91, v91\n"
        "v_exp_f32 v92, v92\n"
        "v_exp_f32 v93, v93\n"
        "v_exp_f32 v94, v94\n"
        "v_exp_f32 v95, v95\n"
        "v_mul_f32 v128, v128, v218\n"
        "v_mul_f32 v129, v129, v218\n"
        "v_mul_f32 v130, v130, v218\n"
        "v_mul_f32 v131, v131, v218\n"
        "v_mul_f32 v132, v132, v218\n"
        "v_mul_f32 v133, v133, v218\n"
        "v_mul_f32 v134, v134, v218\n"
        "v_mul_f32 v135, v135, v218\n"
        "v_mul_f32 v136, v136, v218\n"
        "v_mul_f32 v137, v137, v218\n"
        "v_mul_f32 v138, v138, v218\n"
        "v_mul_f32 v139, v139, v218\n"
        "v_mul_f32 v140, v140, v218\n"
        "v_mul_f32 v141, v141, v218\n"
        "v_mul_f32 v142, v142, v218\n"
        "v_mul_f32 v143, v143, v218\n"
        "v_cvt_pk_bf16_f32 v112, v80, v81\n"
        "v_cvt_pk_bf16_f32 v113, v82, v83\n"
        "v_cvt_pk_bf16_f32 v114, v84, v85\n"
        "v_cvt_pk_bf16_f32 v115, v86, v87\n"
        "v_cvt_pk_bf16_f32 v116, v88, v89\n"
        "v_cvt_pk_bf16_f32 v117, v90, v91\n"
        "v_cvt_pk_bf16_f32 v118, v92, v93\n"
        "v_cvt_pk_bf16_f32 v119, v94, v95\n"
        "v_mfma_f32_32x32x16_bf16 v[128:143], a[88:91], v[112:115], v[128:143]\n"
        "s_nop 0\n"
        "v_mfma_f32_32x32x16_bf16 v[128:143], a[92:95], v[116:119], v[128:143]\n"
        "v_mfma_f32_32x32x16_bf16 v[80:95], a[16:19], v[32:35], 0\n"
        "s_nop 11\n"
        "v_max3_f32 v217, v80, v81, v216\n"
        "v_max3_f32 v217, v82, v83, v217\n"
        "v_max3_f32 v217, v84, v85, v217\n"
        "v_max3_f32 v217, v86, v87, v217\n"
        "v_max3_f32 v217, v88, v89, v217\n"
        "v_max3_f32 v217, v90, v91, v217\n"
        "v_max3_f32 v217, v92, v93, v217\n"
        "v_max3_f32 v217, v94, v95, v217\n"
        "v_mov_b32 v219, v217\n"
        "s_nop 1\n"
        "v_permlane32_swap_b32 v217, v219\n"
        "v_max_f32 v217, v217, v219\n"
        "v_sub_f32 v218, v216, v217\n"
        "v_exp_f32 v218, v218\n"
        "v_mov_b32 v216, v217\n"
        "v_sub_f32 v80, v80, v217\n"
        "v_sub_f32 v81, v81, v217\n"
        "v_sub_f32 v82, v82, v217\n"
        "v_sub_f32 v83, v83, v217\n"
        "v_sub_f32 v84, v84, v217\n"
        "v_sub_f32 v85, v85, v217\n"
        "v_sub_f32 v86, v86, v217\n"
        "v_sub_f32 v87, v87, v217\n"
        "v_sub_f32 v88, v88, v217\n"
        "v_sub_f32 v89, v89, v217\n"
        "v_sub_f32 v90, v90, v217\n"
        "v_sub_f32 v91, v91, v217\n"
        "v_sub_f32 v92, v92, v217\n"
        "v_sub_f32 v93, v93, v217\n"
        "v_sub_f32 v94, v94, v217\n"
        "v_sub_f32 v95, v95, v217\n"
        "v_exp_f32 v80, v80\n"
        "v_exp_f32 v81, v81\n"
        "v_exp_f32 v82, v82\n"
        "v_exp_f32 v83, v83\n"
        "v_exp_f32 v84, v84\n"
        "v_exp_f32 v85, v85\n"
        "v_exp_f32 v86, v86\n"
        "v_exp_f32 v87, v87\n"
        "v_exp_f32 v88, v88\n"
        "v_exp_f32 v89, v89\n"
        "v_exp_f32 v90, v90\n"
        "v_exp_f32 v91, v91\n"
        "v_exp_f32 v92, v92\n"
        "v_exp_f32 v93, v93\n"
        "v_exp_f32 v94, v94\n"
        "v_exp_f32 v95, v95\n"
        "v_mul_f32 v128, v128, v218\n"
        "v_mul_f32 v129, v129, v218\n"
        "v_mul_f32 v130, v130, v218\n"
        "v_mul_f32 v131, v131, v218\n"
        "v_mul_f32 v132, v132, v218\n"
        "v_mul_f32 v133, v133, v218\n"
        "v_mul_f32 v134, v134, v218\n"
        "v_mul_f32 v135, v135, v218\n"
        "v_mul_f32 v136, v136, v218\n"
        "v_mul_f32 v137, v137, v218\n"
        "v_mul_f32 v138, v138, v218\n"
        "v_mul_f32 v139, v139, v218\n"
        "v_mul_f32 v140, v140, v218\n"
        "v_mul_f32 v141, v141, v218\n"
        "v_mul_f32 v142, v142, v218\n"
        "v_mul_f32 v143, v143, v218\n"
        "v_cvt_pk_bf16_f32 v112, v80, v81\n"
        "v_cvt_pk_bf16_f32 v113, v82, v83\n"
        "v_cvt_pk_bf16_f32 v114, v84, v85\n"
        "v_cvt_pk_bf16_f32 v115, v86, v87\n"
        "v_cvt_pk_bf16_f32 v116, v88, v89\n"
        "v_cvt_pk_bf16_f32 v117, v90, v91\n"
        "v_cvt_pk_bf16_f32 v118, v92, v93\n"
        "v_cvt_pk_bf16_f32 v119, v94, v95\n"
        "v_mfma_f32_32x32x16_bf16 v[128:143], a[96:99], v[112:115], v[128:143]\n"
        "s_nop 0\n"
        "v_mfma_f32_32x32x16_bf16 v[128:143], a[100:103], v[116:119], v[128:143]\n"
        "v_mfma_f32_32x32x16_bf16 v[80:95], a[20:23], v[32:35], 0\n"
        "s_nop 11\n"
        "v_max3_f32 v217, v80, v81, v216\n"
        "v_max3_f32 v217, v82, v83, v217\n"
        "v_max3_f32 v217, v84, v85, v217\n"
        "v_max3_f32 v217, v86, v87, v217\n"
        "v_max3_f32 v217, v88, v89, v217\n"
        "v_max3_f32 v217, v90, v91, v217\n"
        "v_max3_f32 v217, v92, v93, v217\n"
        "v_max3_f32 v217, v94, v95, v217\n"
        "v_mov_b32 v219, v217\n"
        "s_nop 1\n"
        "v_permlane32_swap_b32 v217, v219\n"
        "v_max_f32 v217, v217, v219\n"
        "v_sub_f32 v218, v216, v217\n"
        "v_exp_f32 v218, v218\n"
        "v_mov_b32 v216, v217\n"
        "v_sub_f32 v80, v80, v217\n"
        "v_sub_f32 v81, v81, v217\n"
        "v_sub_f32 v82, v82, v217\n"
        "v_sub_f32 v83, v83, v217\n"
        "v_sub_f32 v84, v84, v217\n"
        "v_sub_f32 v85, v85, v217\n"
        "v_sub_f32 v86, v86, v217\n"
        "v_sub_f32 v87, v87, v217\n"
        "v_sub_f32 v88, v88, v217\n"
        "v_sub_f32 v89, v89, v217\n"
        "v_sub_f32 v90, v90, v217\n"
        "v_sub_f32 v91, v91, v217\n"
        "v_sub_f32 v92, v92, v217\n"
        "v_sub_f32 v93, v93, v217\n"
        "v_sub_f32 v94, v94, v217\n"
        "v_sub_f32 v95, v95, v217\n"
        "v_exp_f32 v80, v80\n"
        "v_exp_f32 v81, v81\n"
        "v_exp_f32 v82, v82\n"
        "v_exp_f32 v83, v83\n"
        "v_exp_f32 v84, v84\n"
        "v_exp_f32 v85, v85\n"
        "v_exp_f32 v86, v86\n"
        "v_exp_f32 v87, v87\n"
        "v_exp_f32 v88, v88\n"
        "v_exp_f32 v89, v89\n"
        "v_exp_f32 v90, v90\n"
        "v_exp_f32 v91, v91\n"
        "v_exp_f32 v92, v92\n"
        "v_exp_f32 v93, v93\n"
        "v_exp_f32 v94, v94\n"
        "v_exp_f32 v95, v95\n"
        "v_mul_f32 v128, v128, v218\n"
        "v_mul_f32 v129, v129, v218\n"
        "v_mul_f32 v130, v130, v218\n"
        "v_mul_f32 v131, v131, v218\n"
        "v_mul_f32 v132, v132, v218\n"
        "v_mul_f32 v133, v133, v218\n"
        "v_mul_f32 v134, v134, v218\n"
        "v_mul_f32 v135, v135, v218\n"
        "v_mul_f32 v136, v136, v218\n"
        "v_mul_f32 v137, v137, v218\n"
        "v_mul_f32 v138, v138, v218\n"
        "v_mul_f32 v139, v139, v218\n"
        "v_mul_f32 v140, v140, v218\n"
        "v_mul_f32 v141, v141, v218\n"
        "v_mul_f32 v142, v142, v218\n"
        "v_mul_f32 v143, v143, v218\n"
        "v_cvt_pk_bf16_f32 v112, v80, v81\n"
        "v_cvt_pk_bf16_f32 v113, v82, v83\n"
        "v_cvt_pk_bf16_f32 v114, v84, v85\n"
        "v_cvt_pk_bf16_f32 v115, v86, v87\n"
        "v_cvt_pk_bf16_f32 v116, v88, v89\n"
        "v_cvt_pk_bf16_f32 v117, v90, v91\n"
        "v_cvt_pk_bf16_f32 v118, v92, v93\n"
        "v_cvt_pk_bf16_f32 v119, v94, v95\n"
        "v_mfma_f32_32x32x16_bf16 v[128:143], a[104:107], v[112:115], v[128:143]\n"
        "s_nop 0\n"
        "v_mfma_f32_32x32x16_bf16 v[128:143], a[108:111], v[116:119], v[128:143]\n"
        "v_mfma_f32_32x32x16_bf16 v[80:95], a[24:27], v[32:35], 0\n"
        "s_nop 11\n"
        "v_max3_f32 v217, v80, v81, v216\n"
        "v_max3_f32 v217, v82, v83, v217\n"
        "v_max3_f32 v217, v84, v85, v217\n"
        "v_max3_f32 v217, v86, v87, v217\n"
        "v_max3_f32 v217, v88, v89, v217\n"
        "v_max3_f32 v217, v90, v91, v217\n"
        "v_max3_f32 v217, v92, v93, v217\n"
        "v_max3_f32 v217, v94, v95, v217\n"
        "v_mov_b32 v219, v217\n"
        "s_nop 1\n"
        "v_permlane32_swap_b32 v217, v219\n"
        "v_max_f32 v217, v217, v219\n"
        "v_sub_f32 v218, v216, v217\n"
        "v_exp_f32 v218, v218\n"
        "v_mov_b32 v216, v217\n"
        "v_sub_f32 v80, v80, v217\n"
        "v_sub_f32 v81, v81, v217\n"
        "v_sub_f32 v82, v82, v217\n"
        "v_sub_f32 v83, v83, v217\n"
        "v_sub_f32 v84, v84, v217\n"
        "v_sub_f32 v85, v85, v217\n"
        "v_sub_f32 v86, v86, v217\n"
        "v_sub_f32 v87, v87, v217\n"
        "v_sub_f32 v88, v88, v217\n"
        "v_sub_f32 v89, v89, v217\n"
        "v_sub_f32 v90, v90, v217\n"
        "v_sub_f32 v91, v91, v217\n"
        "v_sub_f32 v92, v92, v217\n"
        "v_sub_f32 v93, v93, v217\n"
        "v_sub_f32 v94, v94, v217\n"
        "v_sub_f32 v95, v95, v217\n"
        "v_exp_f32 v80, v80\n"
        "v_exp_f32 v81, v81\n"
        "v_exp_f32 v82, v82\n"
        "v_exp_f32 v83, v83\n"
        "v_exp_f32 v84, v84\n"
        "v_exp_f32 v85, v85\n"
        "v_exp_f32 v86, v86\n"
        "v_exp_f32 v87, v87\n"
        "v_exp_f32 v88, v88\n"
        "v_exp_f32 v89, v89\n"
        "v_exp_f32 v90, v90\n"
        "v_exp_f32 v91, v91\n"
        "v_exp_f32 v92, v92\n"
        "v_exp_f32 v93, v93\n"
        "v_exp_f32 v94, v94\n"
        "v_exp_f32 v95, v95\n"
        "v_mul_f32 v128, v128, v218\n"
        "v_mul_f32 v129, v129, v218\n"
        "v_mul_f32 v130, v130, v218\n"
        "v_mul_f32 v131, v131, v218\n"
        "v_mul_f32 v132, v132, v218\n"
        "v_mul_f32 v133, v133, v218\n"
        "v_mul_f32 v134, v134, v218\n"
        "v_mul_f32 v135, v135, v218\n"
        "v_mul_f32 v136, v136, v218\n"
        "v_mul_f32 v137, v137, v218\n"
        "v_mul_f32 v138, v138, v218\n"
        "v_mul_f32 v139, v139, v218\n"
        "v_mul_f32 v140, v140, v218\n"
        "v_mul_f32 v141, v141, v218\n"
        "v_mul_f32 v142, v142, v218\n"
        "v_mul_f32 v143, v143, v218\n"
        "v_cvt_pk_bf16_f32 v112, v80, v81\n"
        "v_cvt_pk_bf16_f32 v113, v82, v83\n"
        "v_cvt_pk_bf16_f32 v114, v84, v85\n"
        "v_cvt_pk_bf16_f32 v115, v86, v87\n"
        "v_cvt_pk_bf16_f32 v116, v88, v89\n"
        "v_cvt_pk_bf16_f32 v117, v90, v91\n"
        "v_cvt_pk_bf16_f32 v118, v92, v93\n"
        "v_cvt_pk_bf16_f32 v119, v94, v95\n"
        "v_mfma_f32_32x32x16_bf16 v[128:143], a[112:115], v[112:115], v[128:143]\n"
        "s_nop 0\n"
        "v_mfma_f32_32x32x16_bf16 v[128:143], a[116:119], v[116:119], v[128:143]\n"
        "v_mfma_f32_32x32x16_bf16 v[80:95], a[28:31], v[32:35], 0\n"
        "s_nop 11\n"
        "v_max3_f32 v217, v80, v81, v216\n"
        "v_max3_f32 v217, v82, v83, v217\n"
        "v_max3_f32 v217, v84, v85, v217\n"
        "v_max3_f32 v217, v86, v87, v217\n"
        "v_max3_f32 v217, v88, v89, v217\n"
        "v_max3_f32 v217, v90, v91, v217\n"
        "v_max3_f32 v217, v92, v93, v217\n"
        "v_max3_f32 v217, v94, v95, v217\n"
        "v_mov_b32 v219, v217\n"
        "s_nop 1\n"
        "v_permlane32_swap_b32 v217, v219\n"
        "v_max_f32 v217, v217, v219\n"
        "v_sub_f32 v218, v216, v217\n"
        "v_exp_f32 v218, v218\n"
        "v_mov_b32 v216, v217\n"
        "v_sub_f32 v80, v80, v217\n"
        "v_sub_f32 v81, v81, v217\n"
        "v_sub_f32 v82, v82, v217\n"
        "v_sub_f32 v83, v83, v217\n"
        "v_sub_f32 v84, v84, v217\n"
        "v_sub_f32 v85, v85, v217\n"
        "v_sub_f32 v86, v86, v217\n"
        "v_sub_f32 v87, v87, v217\n"
        "v_sub_f32 v88, v88, v217\n"
        "v_sub_f32 v89, v89, v217\n"
        "v_sub_f32 v90, v90, v217\n"
        "v_sub_f32 v91, v91, v217\n"
        "v_sub_f32 v92, v92, v217\n"
        "v_sub_f32 v93, v93, v217\n"
        "v_sub_f32 v94, v94, v217\n"
        "v_sub_f32 v95, v95, v217\n"
        "v_exp_f32 v80, v80\n"
        "v_exp_f32 v81, v81\n"
        "v_exp_f32 v82, v82\n"
        "v_exp_f32 v83, v83\n"
        "v_exp_f32 v84, v84\n"
        "v_exp_f32 v85, v85\n"
        "v_exp_f32 v86, v86\n"
        "v_exp_f32 v87, v87\n"
        "v_exp_f32 v88, v88\n"
        "v_exp_f32 v89, v89\n"
        "v_exp_f32 v90, v90\n"
        "v_exp_f32 v91, v91\n"
        "v_exp_f32 v92, v92\n"
        "v_exp_f32 v93, v93\n"
        "v_exp_f32 v94, v94\n"
        "v_exp_f32 v95, v95\n"
        "v_mul_f32 v128, v128, v218\n"
        "v_mul_f32 v129, v129, v218\n"
        "v_mul_f32 v130, v130, v218\n"
        "v_mul_f32 v131, v131, v218\n"
        "v_mul_f32 v132, v132, v218\n"
        "v_mul_f32 v133, v133, v218\n"
        "v_mul_f32 v134, v134, v218\n"
        "v_mul_f32 v135, v135, v218\n"
        "v_mul_f32 v136, v136, v218\n"
        "v_mul_f32 v137, v137, v218\n"
        "v_mul_f32 v138, v138, v218\n"
        "v_mul_f32 v139, v139, v218\n"
        "v_mul_f32 v140, v140, v218\n"
        "v_mul_f32 v141, v141, v218\n"
        "v_mul_f32 v142, v142, v218\n"
        "v_mul_f32 v143, v143, v218\n"
        "v_cvt_pk_bf16_f32 v112, v80, v81\n"
        "v_cvt_pk_bf16_f32 v113, v82, v83\n"
        "v_cvt_pk_bf16_f32 v114, v84, v85\n"
        "v_cvt_pk_bf16_f32 v115, v86, v87\n"
        "v_cvt_pk_bf16_f32 v116, v88, v89\n"
        "v_cvt_pk_bf16_f32 v117, v90, v91\n"
        "v_cvt_pk_bf16_f32 v118, v92, v93\n"
        "v_cvt_pk_bf16_f32 v119, v94, v95\n"
        "v_mfma_f32_32x32x16_bf16 v[128:143], a[120:123], v[112:115], v[128:143]\n"
        "s_nop 0\n"
        "v_mfma_f32_32x32x16_bf16 v[128:143], a[124:127], v[116:119], v[128:143]\n"
        "v_mfma_f32_32x32x16_bf16 v[80:95], a[32:35], v[32:35], 0\n"
        "s_nop 11\n"
        "v_max3_f32 v217, v80, v81, v216\n"
        "v_max3_f32 v217, v82, v83, v217\n"
        "v_max3_f32 v217, v84, v85, v217\n"
        "v_max3_f32 v217, v86, v87, v217\n"
        "v_max3_f32 v217, v88, v89, v217\n"
        "v_max3_f32 v217, v90, v91, v217\n"
        "v_max3_f32 v217, v92, v93, v217\n"
        "v_max3_f32 v217, v94, v95, v217\n"
        "v_mov_b32 v219, v217\n"
        "s_nop 1\n"
        "v_permlane32_swap_b32 v217, v219\n"
        "v_max_f32 v217, v217, v219\n"
        "v_sub_f32 v218, v216, v217\n"
        "v_exp_f32 v218, v218\n"
        "v_mov_b32 v216, v217\n"
        "v_sub_f32 v80, v80, v217\n"
        "v_sub_f32 v81, v81, v217\n"
        "v_sub_f32 v82, v82, v217\n"
        "v_sub_f32 v83, v83, v217\n"
        "v_sub_f32 v84, v84, v217\n"
        "v_sub_f32 v85, v85, v217\n"
        "v_sub_f32 v86, v86, v217\n"
        "v_sub_f32 v87, v87, v217\n"
        "v_sub_f32 v88, v88, v217\n"
        "v_sub_f32 v89, v89, v217\n"
        "v_sub_f32 v90, v90, v217\n"
        "v_sub_f32 v91, v91, v217\n"
        "v_sub_f32 v92, v92, v217\n"
        "v_sub_f32 v93, v93, v217\n"
        "v_sub_f32 v94, v94, v217\n"
        "v_sub_f32 v95, v95, v217\n"
        "v_exp_f32 v80, v80\n"
        "v_exp_f32 v81, v81\n"
        "v_exp_f32 v82, v82\n"
        "v_exp_f32 v83, v83\n"
        "v_exp_f32 v84, v84\n"
        "v_exp_f32 v85, v85\n"
        "v_exp_f32 v86, v86\n"
        "v_exp_f32 v87, v87\n"
        "v_exp_f32 v88, v88\n"
        "v_exp_f32 v89, v89\n"
        "v_exp_f32 v90, v90\n"
        "v_exp_f32 v91, v91\n"
        "v_exp_f32 v92, v92\n"
        "v_exp_f32 v93, v93\n"
        "v_exp_f32 v94, v94\n"
        "v_exp_f32 v95, v95\n"
        "v_mul_f32 v128, v128, v218\n"
        "v_mul_f32 v129, v129, v218\n"
        "v_mul_f32 v130, v130, v218\n"
        "v_mul_f32 v131, v131, v218\n"
        "v_mul_f32 v132, v132, v218\n"
        "v_mul_f32 v133, v133, v218\n"
        "v_mul_f32 v134, v134, v218\n"
        "v_mul_f32 v135, v135, v218\n"
        "v_mul_f32 v136, v136, v218\n"
        "v_mul_f32 v137, v137, v218\n"
        "v_mul_f32 v138, v138, v218\n"
        "v_mul_f32 v139, v139, v218\n"
        "v_mul_f32 v140, v140, v218\n"
        "v_mul_f32 v141, v141, v218\n"
        "v_mul_f32 v142, v142, v218\n"
        "v_mul_f32 v143, v143, v218\n"
        "v_cvt_pk_bf16_f32 v112, v80, v81\n"
        "v_cvt_pk_bf16_f32 v113, v82, v83\n"
        "v_cvt_pk_bf16_f32 v114, v84, v85\n"
        "v_cvt_pk_bf16_f32 v115, v86, v87\n"
        "v_cvt_pk_bf16_f32 v116, v88, v89\n"
        "v_cvt_pk_bf16_f32 v117, v90, v91\n"
        "v_cvt_pk_bf16_f32 v118, v92, v93\n"
        "v_cvt_pk_bf16_f32 v119, v94, v95\n"
        "v_mfma_f32_32x32x16_bf16 v[128:143], a[128:131], v[112:115], v[128:143]\n"
        "s_nop 0\n"
        "v_mfma_f32_32x32x16_bf16 v[128:143], a[132:135], v[116:119], v[128:143]\n"
        "v_mfma_f32_32x32x16_bf16 v[80:95], a[36:39], v[32:35], 0\n"
        "s_nop 11\n"
        "v_max3_f32 v217, v80, v81, v216\n"
        "v_max3_f32 v217, v82, v83, v217\n"
        "v_max3_f32 v217, v84, v85, v217\n"
        "v_max3_f32 v217, v86, v87, v217\n"
        "v_max3_f32 v217, v88, v89, v217\n"
        "v_max3_f32 v217, v90, v91, v217\n"
        "v_max3_f32 v217, v92, v93, v217\n"
        "v_max3_f32 v217, v94, v95, v217\n"
        "v_mov_b32 v219, v217\n"
        "s_nop 1\n"
        "v_permlane32_swap_b32 v217, v219\n"
        "v_max_f32 v217, v217, v219\n"
        "v_sub_f32 v218, v216, v217\n"
        "v_exp_f32 v218, v218\n"
        "v_mov_b32 v216, v217\n"
        "v_sub_f32 v80, v80, v217\n"
        "v_sub_f32 v81, v81, v217\n"
        "v_sub_f32 v82, v82, v217\n"
        "v_sub_f32 v83, v83, v217\n"
        "v_sub_f32 v84, v84, v217\n"
        "v_sub_f32 v85, v85, v217\n"
        "v_sub_f32 v86, v86, v217\n"
        "v_sub_f32 v87, v87, v217\n"
        "v_sub_f32 v88, v88, v217\n"
        "v_sub_f32 v89, v89, v217\n"
        "v_sub_f32 v90, v90, v217\n"
        "v_sub_f32 v91, v91, v217\n"
        "v_sub_f32 v92, v92, v217\n"
        "v_sub_f32 v93, v93, v217\n"
        "v_sub_f32 v94, v94, v217\n"
        "v_sub_f32 v95, v95, v217\n"
        "v_exp_f32 v80, v80\n"
        "v_exp_f32 v81, v81\n"
        "v_exp_f32 v82, v82\n"
        "v_exp_f32 v83, v83\n"
        "v_exp_f32 v84, v84\n"
        "v_exp_f32 v85, v85\n"
        "v_exp_f32 v86, v86\n"
        "v_exp_f32 v87, v87\n"
        "v_exp_f32 v88, v88\n"
        "v_exp_f32 v89, v89\n"
        "v_exp_f32 v90, v90\n"
        "v_exp_f32 v91, v91\n"
        "v_exp_f32 v92, v92\n"
        "v_exp_f32 v93, v93\n"
        "v_exp_f32 v94, v94\n"
        "v_exp_f32 v95, v95\n"
        "v_mul_f32 v128, v128, v218\n"
        "v_mul_f32 v129, v129, v218\n"
        "v_mul_f32 v130, v130, v218\n"
        "v_mul_f32 v131, v131, v218\n"
        "v_mul_f32 v132, v132, v218\n"
        "v_mul_f32 v133, v133, v218\n"
        "v_mul_f32 v134, v134, v218\n"
        "v_mul_f32 v135, v135, v218\n"
        "v_mul_f32 v136, v136, v218\n"
        "v_mul_f32 v137, v137, v218\n"
        "v_mul_f32 v138, v138, v218\n"
        "v_mul_f32 v139, v139, v218\n"
        "v_mul_f32 v140, v140, v218\n"
        "v_mul_f32 v141, v141, v218\n"
        "v_mul_f32 v142, v142, v218\n"
        "v_mul_f32 v143, v143, v218\n"
        "v_cvt_pk_bf16_f32 v112, v80, v81\n"
        "v_cvt_pk_bf16_f32 v113, v82, v83\n"
        "v_cvt_pk_bf16_f32 v114, v84, v85\n"
        "v_cvt_pk_bf16_f32 v115, v86, v87\n"
        "v_cvt_pk_bf16_f32 v116, v88, v89\n"
        "v_cvt_pk_bf16_f32 v117, v90, v91\n"
        "v_cvt_pk_bf16_f32 v118, v92, v93\n"
        "v_cvt_pk_bf16_f32 v119, v94, v95\n"
        "v_mfma_f32_32x32x16_bf16 v[128:143], a[136:139], v[112:115], v[128:143]\n"
        "s_nop 0\n"
        "v_mfma_f32_32x32x16_bf16 v[128:143], a[140:143], v[116:119], v[128:143]\n"
        "v_mfma_f32_32x32x16_bf16 v[80:95], a[40:43], v[32:35], 0\n"
        "s_nop 11\n"
        "v_max3_f32 v217, v80, v81, v216\n"
        "v_max3_f32 v217, v82, v83, v217\n"
        "v_max3_f32 v217, v84, v85, v217\n"
        "v_max3_f32 v217, v86, v87, v217\n"
        "v_max3_f32 v217, v88, v89, v217\n"
        "v_max3_f32 v217, v90, v91, v217\n"
        "v_max3_f32 v217, v92, v93, v217\n"
        "v_max3_f32 v217, v94, v95, v217\n"
        "v_mov_b32 v219, v217\n"
        "s_nop 1\n"
        "v_permlane32_swap_b32 v217, v219\n"
        "v_max_f32 v217, v217, v219\n"
        "v_sub_f32 v218, v216, v217\n"
        "v_exp_f32 v218, v218\n"
        "v_mov_b32 v216, v217\n"
        "v_sub_f32 v80, v80, v217\n"
        "v_sub_f32 v81, v81, v217\n"
        "v_sub_f32 v82, v82, v217\n"
        "v_sub_f32 v83, v83, v217\n"
        "v_sub_f32 v84, v84, v217\n"
        "v_sub_f32 v85, v85, v217\n"
        "v_sub_f32 v86, v86, v217\n"
        "v_sub_f32 v87, v87, v217\n"
        "v_sub_f32 v88, v88, v217\n"
        "v_sub_f32 v89, v89, v217\n"
        "v_sub_f32 v90, v90, v217\n"
        "v_sub_f32 v91, v91, v217\n"
        "v_sub_f32 v92, v92, v217\n"
        "v_sub_f32 v93, v93, v217\n"
        "v_sub_f32 v94, v94, v217\n"
        "v_sub_f32 v95, v95, v217\n"
        "v_exp_f32 v80, v80\n"
        "v_exp_f32 v81, v81\n"
        "v_exp_f32 v82, v82\n"
        "v_exp_f32 v83, v83\n"
        "v_exp_f32 v84, v84\n"
        "v_exp_f32 v85, v85\n"
        "v_exp_f32 v86, v86\n"
        "v_exp_f32 v87, v87\n"
        "v_exp_f32 v88, v88\n"
        "v_exp_f32 v89, v89\n"
        "v_exp_f32 v90, v90\n"
        "v_exp_f32 v91, v91\n"
        "v_exp_f32 v92, v92\n"
        "v_exp_f32 v93, v93\n"
        "v_exp_f32 v94, v94\n"
        "v_exp_f32 v95, v95\n"
        "v_mul_f32 v128, v128, v218\n"
        "v_mul_f32 v129, v129, v218\n"
        "v_mul_f32 v130, v130, v218\n"
        "v_mul_f32 v131, v131, v218\n"
        "v_mul_f32 v132, v132, v218\n"
        "v_mul_f32 v133, v133, v218\n"
        "v_mul_f32 v134, v134, v218\n"
        "v_mul_f32 v135, v135, v218\n"
        "v_mul_f32 v136, v136, v218\n"
        "v_mul_f32 v137, v137, v218\n"
        "v_mul_f32 v138, v138, v218\n"
        "v_mul_f32 v139, v139, v218\n"
        "v_mul_f32 v140, v140, v218\n"
        "v_mul_f32 v141, v141, v218\n"
        "v_mul_f32 v142, v142, v218\n"
        "v_mul_f32 v143, v143, v218\n"
        "v_cvt_pk_bf16_f32 v112, v80, v81\n"
        "v_cvt_pk_bf16_f32 v113, v82, v83\n"
        "v_cvt_pk_bf16_f32 v114, v84, v85\n"
        "v_cvt_pk_bf16_f32 v115, v86, v87\n"
        "v_cvt_pk_bf16_f32 v116, v88, v89\n"
        "v_cvt_pk_bf16_f32 v117, v90, v91\n"
        "v_cvt_pk_bf16_f32 v118, v92, v93\n"
        "v_cvt_pk_bf16_f32 v119, v94, v95\n"
        "v_mfma_f32_32x32x16_bf16 v[128:143], a[144:147], v[112:115], v[128:143]\n"
        "s_nop 0\n"
        "v_mfma_f32_32x32x16_bf16 v[128:143], a[148:151], v[116:119], v[128:143]\n"
        "v_mfma_f32_32x32x16_bf16 v[80:95], a[44:47], v[32:35], 0\n"
        "s_nop 11\n"
        "v_max3_f32 v217, v80, v81, v216\n"
        "v_max3_f32 v217, v82, v83, v217\n"
        "v_max3_f32 v217, v84, v85, v217\n"
        "v_max3_f32 v217, v86, v87, v217\n"
        "v_max3_f32 v217, v88, v89, v217\n"
        "v_max3_f32 v217, v90, v91, v217\n"
        "v_max3_f32 v217, v92, v93, v217\n"
        "v_max3_f32 v217, v94, v95, v217\n"
        "v_mov_b32 v219, v217\n"
        "s_nop 1\n"
        "v_permlane32_swap_b32 v217, v219\n"
        "v_max_f32 v217, v217, v219\n"
        "v_sub_f32 v218, v216, v217\n"
        "v_exp_f32 v218, v218\n"
        "v_mov_b32 v216, v217\n"
        "v_sub_f32 v80, v80, v217\n"
        "v_sub_f32 v81, v81, v217\n"
        "v_sub_f32 v82, v82, v217\n"
        "v_sub_f32 v83, v83, v217\n"
        "v_sub_f32 v84, v84, v217\n"
        "v_sub_f32 v85, v85, v217\n"
        "v_sub_f32 v86, v86, v217\n"
        "v_sub_f32 v87, v87, v217\n"
        "v_sub_f32 v88, v88, v217\n"
        "v_sub_f32 v89, v89, v217\n"
        "v_sub_f32 v90, v90, v217\n"
        "v_sub_f32 v91, v91, v217\n"
        "v_sub_f32 v92, v92, v217\n"
        "v_sub_f32 v93, v93, v217\n"
        "v_sub_f32 v94, v94, v217\n"
        "v_sub_f32 v95, v95, v217\n"
        "v_exp_f32 v80, v80\n"
        "v_exp_f32 v81, v81\n"
        "v_exp_f32 v82, v82\n"
        "v_exp_f32 v83, v83\n"
        "v_exp_f32 v84, v84\n"
        "v_exp_f32 v85, v85\n"
        "v_exp_f32 v86, v86\n"
        "v_exp_f32 v87, v87\n"
        "v_exp_f32 v88, v88\n"
        "v_exp_f32 v89, v89\n"
        "v_exp_f32 v90, v90\n"
        "v_exp_f32 v91, v91\n"
        "v_exp_f32 v92, v92\n"
        "v_exp_f32 v93, v93\n"
        "v_exp_f32 v94, v94\n"
        "v_exp_f32 v95, v95\n"
        "v_mul_f32 v128, v128, v218\n"
        "v_mul_f32 v129, v129, v218\n"
        "v_mul_f32 v130, v130, v218\n"
        "v_mul_f32 v131, v131, v218\n"
        "v_mul_f32 v132, v132, v218\n"
        "v_mul_f32 v133, v133, v218\n"
        "v_mul_f32 v134, v134, v218\n"
        "v_mul_f32 v135, v135, v218\n"
        "v_mul_f32 v136, v136, v218\n"
        "v_mul_f32 v137, v137, v218\n"
        "v_mul_f32 v138, v138, v218\n"
        "v_mul_f32 v139, v139, v218\n"
        "v_mul_f32 v140, v140, v218\n"
        "v_mul_f32 v141, v141, v218\n"
        "v_mul_f32 v142, v142, v218\n"
        "v_mul_f32 v143, v143, v218\n"
        "v_cvt_pk_bf16_f32 v112, v80, v81\n"
        "v_cvt_pk_bf16_f32 v113, v82, v83\n"
        "v_cvt_pk_bf16_f32 v114, v84, v85\n"
        "v_cvt_pk_bf16_f32 v115, v86, v87\n"
        "v_cvt_pk_bf16_f32 v116, v88, v89\n"
        "v_cvt_pk_bf16_f32 v117, v90, v91\n"
        "v_cvt_pk_bf16_f32 v118, v92, v93\n"
        "v_cvt_pk_bf16_f32 v119, v94, v95\n"
        "v_mfma_f32_32x32x16_bf16 v[128:143], a[152:155], v[112:115], v[128:143]\n"
        "s_nop 0\n"
        "v_mfma_f32_32x32x16_bf16 v[128:143], a[156:159], v[116:119], v[128:143]\n"
        "v_mfma_f32_32x32x16_bf16 v[80:95], a[48:51], v[32:35], 0\n"
        "s_nop 11\n"
        "v_max3_f32 v217, v80, v81, v216\n"
        "v_max3_f32 v217, v82, v83, v217\n"
        "v_max3_f32 v217, v84, v85, v217\n"
        "v_max3_f32 v217, v86, v87, v217\n"
        "v_max3_f32 v217, v88, v89, v217\n"
        "v_max3_f32 v217, v90, v91, v217\n"
        "v_max3_f32 v217, v92, v93, v217\n"
        "v_max3_f32 v217, v94, v95, v217\n"
        "v_mov_b32 v219, v217\n"
        "s_nop 1\n"
        "v_permlane32_swap_b32 v217, v219\n"
        "v_max_f32 v217, v217, v219\n"
        "v_sub_f32 v218, v216, v217\n"
        "v_exp_f32 v218, v218\n"
        "v_mov_b32 v216, v217\n"
        "v_sub_f32 v80, v80, v217\n"
        "v_sub_f32 v81, v81, v217\n"
        "v_sub_f32 v82, v82, v217\n"
        "v_sub_f32 v83, v83, v217\n"
        "v_sub_f32 v84, v84, v217\n"
        "v_sub_f32 v85, v85, v217\n"
        "v_sub_f32 v86, v86, v217\n"
        "v_sub_f32 v87, v87, v217\n"
        "v_sub_f32 v88, v88, v217\n"
        "v_sub_f32 v89, v89, v217\n"
        "v_sub_f32 v90, v90, v217\n"
        "v_sub_f32 v91, v91, v217\n"
        "v_sub_f32 v92, v92, v217\n"
        "v_sub_f32 v93, v93, v217\n"
        "v_sub_f32 v94, v94, v217\n"
        "v_sub_f32 v95, v95, v217\n"
        "v_exp_f32 v80, v80\n"
        "v_exp_f32 v81, v81\n"
        "v_exp_f32 v82, v82\n"
        "v_exp_f32 v83, v83\n"
        "v_exp_f32 v84, v84\n"
        "v_exp_f32 v85, v85\n"
        "v_exp_f32 v86, v86\n"
        "v_exp_f32 v87, v87\n"
        "v_exp_f32 v88, v88\n"
        "v_exp_f32 v89, v89\n"
        "v_exp_f32 v90, v90\n"
        "v_exp_f32 v91, v91\n"
        "v_exp_f32 v92, v92\n"
        "v_exp_f32 v93, v93\n"
        "v_exp_f32 v94, v94\n"
        "v_exp_f32 v95, v95\n"
        "v_mul_f32 v128, v128, v218\n"
        "v_mul_f32 v129, v129, v218\n"
        "v_mul_f32 v130, v130, v218\n"
        "v_mul_f32 v131, v131, v218\n"
        "v_mul_f32 v132, v132, v218\n"
        "v_mul_f32 v133, v133, v218\n"
        "v_mul_f32 v134, v134, v218\n"
        "v_mul_f32 v135, v135, v218\n"
        "v_mul_f32 v136, v136, v218\n"
        "v_mul_f32 v137, v137, v218\n"
        "v_mul_f32 v138, v138, v218\n"
        "v_mul_f32 v139, v139, v218\n"
        "v_mul_f32 v140, v140, v218\n"
        "v_mul_f32 v141, v141, v218\n"
        "v_mul_f32 v142, v142, v218\n"
        "v_mul_f32 v143, v143, v218\n"
        "v_cvt_pk_bf16_f32 v112, v80, v81\n"
        "v_cvt_pk_bf16_f32 v113, v82, v83\n"
        "v_cvt_pk_bf16_f32 v114, v84, v85\n"
        "v_cvt_pk_bf16_f32 v115, v86, v87\n"
        "v_cvt_pk_bf16_f32 v116, v88, v89\n"
        "v_cvt_pk_bf16_f32 v117, v90, v91\n"
        "v_cvt_pk_bf16_f32 v118, v92, v93\n"
        "v_cvt_pk_bf16_f32 v119, v94, v95\n"
        "v_mfma_f32_32x32x16_bf16 v[128:143], a[160:163], v[112:115], v[128:143]\n"
        "s_nop 0\n"
        "v_mfma_f32_32x32x16_bf16 v[128:143], a[164:167], v[116:119], v[128:143]\n"
        "v_mfma_f32_32x32x16_bf16 v[80:95], a[52:55], v[32:35], 0\n"
        "s_nop 11\n"
        "v_max3_f32 v217, v80, v81, v216\n"
        "v_max3_f32 v217, v82, v83, v217\n"
        "v_max3_f32 v217, v84, v85, v217\n"
        "v_max3_f32 v217, v86, v87, v217\n"
        "v_max3_f32 v217, v88, v89, v217\n"
        "v_max3_f32 v217, v90, v91, v217\n"
        "v_max3_f32 v217, v92, v93, v217\n"
        "v_max3_f32 v217, v94, v95, v217\n"
        "v_mov_b32 v219, v217\n"
        "s_nop 1\n"
        "v_permlane32_swap_b32 v217, v219\n"
        "v_max_f32 v217, v217, v219\n"
        "v_sub_f32 v218, v216, v217\n"
        "v_exp_f32 v218, v218\n"
        "v_mov_b32 v216, v217\n"
        "v_sub_f32 v80, v80, v217\n"
        "v_sub_f32 v81, v81, v217\n"
        "v_sub_f32 v82, v82, v217\n"
        "v_sub_f32 v83, v83, v217\n"
        "v_sub_f32 v84, v84, v217\n"
        "v_sub_f32 v85, v85, v217\n"
        "v_sub_f32 v86, v86, v217\n"
        "v_sub_f32 v87, v87, v217\n"
        "v_sub_f32 v88, v88, v217\n"
        "v_sub_f32 v89, v89, v217\n"
        "v_sub_f32 v90, v90, v217\n"
        "v_sub_f32 v91, v91, v217\n"
        "v_sub_f32 v92, v92, v217\n"
        "v_sub_f32 v93, v93, v217\n"
        "v_sub_f32 v94, v94, v217\n"
        "v_sub_f32 v95, v95, v217\n"
        "v_exp_f32 v80, v80\n"
        "v_exp_f32 v81, v81\n"
        "v_exp_f32 v82, v82\n"
        "v_exp_f32 v83, v83\n"
        "v_exp_f32 v84, v84\n"
        "v_exp_f32 v85, v85\n"
        "v_exp_f32 v86, v86\n"
        "v_exp_f32 v87, v87\n"
        "v_exp_f32 v88, v88\n"
        "v_exp_f32 v89, v89\n"
        "v_exp_f32 v90, v90\n"
        "v_exp_f32 v91, v91\n"
        "v_exp_f32 v92, v92\n"
        "v_exp_f32 v93, v93\n"
        "v_exp_f32 v94, v94\n"
        "v_exp_f32 v95, v95\n"
        "v_mul_f32 v128, v128, v218\n"
        "v_mul_f32 v129, v129, v218\n"
        "v_mul_f32 v130, v130, v218\n"
        "v_mul_f32 v131, v131, v218\n"
        "v_mul_f32 v132, v132, v218\n"
        "v_mul_f32 v133, v133, v218\n"
        "v_mul_f32 v134, v134, v218\n"
        "v_mul_f32 v135, v135, v218\n"
        "v_mul_f32 v136, v136, v218\n"
        "v_mul_f32 v137, v137, v218\n"
        "v_mul_f32 v138, v138, v218\n"
        "v_mul_f32 v139, v139, v218\n"
        "v_mul_f32 v140, v140, v218\n"
        "v_mul_f32 v141, v141, v218\n"
        "v_mul_f32 v142, v142, v218\n"
        "v_mul_f32 v143, v143, v218\n"
        "v_cvt_pk_bf16_f32 v112, v80, v81\n"
        "v_cvt_pk_bf16_f32 v113, v82, v83\n"
        "v_cvt_pk_bf16_f32 v114, v84, v85\n"
        "v_cvt_pk_bf16_f32 v115, v86, v87\n"
        "v_cvt_pk_bf16_f32 v116, v88, v89\n"
        "v_cvt_pk_bf16_f32 v117, v90, v91\n"
        "v_cvt_pk_bf16_f32 v118, v92, v93\n"
        "v_cvt_pk_bf16_f32 v119, v94, v95\n"
        "v_mfma_f32_32x32x16_bf16 v[128:143], a[168:171], v[112:115], v[128:143]\n"
        "s_nop 0\n"
        "v_mfma_f32_32x32x16_bf16 v[128:143], a[172:175], v[116:119], v[128:143]\n"
        "v_mfma_f32_32x32x16_bf16 v[80:95], a[56:59], v[32:35], 0\n"
        "s_nop 11\n"
        "v_max3_f32 v217, v80, v81, v216\n"
        "v_max3_f32 v217, v82, v83, v217\n"
        "v_max3_f32 v217, v84, v85, v217\n"
        "v_max3_f32 v217, v86, v87, v217\n"
        "v_max3_f32 v217, v88, v89, v217\n"
        "v_max3_f32 v217, v90, v91, v217\n"
        "v_max3_f32 v217, v92, v93, v217\n"
        "v_max3_f32 v217, v94, v95, v217\n"
        "v_mov_b32 v219, v217\n"
        "s_nop 1\n"
        "v_permlane32_swap_b32 v217, v219\n"
        "v_max_f32 v217, v217, v219\n"
        "v_sub_f32 v218, v216, v217\n"
        "v_exp_f32 v218, v218\n"
        "v_mov_b32 v216, v217\n"
        "v_sub_f32 v80, v80, v217\n"
        "v_sub_f32 v81, v81, v217\n"
        "v_sub_f32 v82, v82, v217\n"
        "v_sub_f32 v83, v83, v217\n"
        "v_sub_f32 v84, v84, v217\n"
        "v_sub_f32 v85, v85, v217\n"
        "v_sub_f32 v86, v86, v217\n"
        "v_sub_f32 v87, v87, v217\n"
        "v_sub_f32 v88, v88, v217\n"
        "v_sub_f32 v89, v89, v217\n"
        "v_sub_f32 v90, v90, v217\n"
        "v_sub_f32 v91, v91, v217\n"
        "v_sub_f32 v92, v92, v217\n"
        "v_sub_f32 v93, v93, v217\n"
        "v_sub_f32 v94, v94, v217\n"
        "v_sub_f32 v95, v95, v217\n"
        "v_exp_f32 v80, v80\n"
        "v_exp_f32 v81, v81\n"
        "v_exp_f32 v82, v82\n"
        "v_exp_f32 v83, v83\n"
        "v_exp_f32 v84, v84\n"
        "v_exp_f32 v85, v85\n"
        "v_exp_f32 v86, v86\n"
        "v_exp_f32 v87, v87\n"
        "v_exp_f32 v88, v88\n"
        "v_exp_f32 v89, v89\n"
        "v_exp_f32 v90, v90\n"
        "v_exp_f32 v91, v91\n"
        "v_exp_f32 v92, v92\n"
        "v_exp_f32 v93, v93\n"
        "v_exp_f32 v94, v94\n"
        "v_exp_f32 v95, v95\n"
        "v_mul_f32 v128, v128, v218\n"
        "v_mul_f32 v129, v129, v218\n"
        "v_mul_f32 v130, v130, v218\n"
        "v_mul_f32 v131, v131, v218\n"
        "v_mul_f32 v132, v132, v218\n"
        "v_mul_f32 v133, v133, v218\n"
        "v_mul_f32 v134, v134, v218\n"
        "v_mul_f32 v135, v135, v218\n"
        "v_mul_f32 v136, v136, v218\n"
        "v_mul_f32 v137, v137, v218\n"
        "v_mul_f32 v138, v138, v218\n"
        "v_mul_f32 v139, v139, v218\n"
        "v_mul_f32 v140, v140, v218\n"
        "v_mul_f32 v141, v141, v218\n"
        "v_mul_f32 v142, v142, v218\n"
        "v_mul_f32 v143, v143, v218\n"
        "v_cvt_pk_bf16_f32 v112, v80, v81\n"
        "v_cvt_pk_bf16_f32 v113, v82, v83\n"
        "v_cvt_pk_bf16_f32 v114, v84, v85\n"
        "v_cvt_pk_bf16_f32 v115, v86, v87\n"
        "v_cvt_pk_bf16_f32 v116, v88, v89\n"
        "v_cvt_pk_bf16_f32 v117, v90, v91\n"
        "v_cvt_pk_bf16_f32 v118, v92, v93\n"
        "v_cvt_pk_bf16_f32 v119, v94, v95\n"
        "v_mfma_f32_32x32x16_bf16 v[128:143], a[176:179], v[112:115], v[128:143]\n"
        "s_nop 0\n"
        "v_mfma_f32_32x32x16_bf16 v[128:143], a[180:183], v[116:119], v[128:143]\n"
        "v_mfma_f32_32x32x16_bf16 v[80:95], a[60:63], v[32:35], 0\n"
        "s_nop 11\n"
        "v_max3_f32 v217, v80, v81, v216\n"
        "v_max3_f32 v217, v82, v83, v217\n"
        "v_max3_f32 v217, v84, v85, v217\n"
        "v_max3_f32 v217, v86, v87, v217\n"
        "v_max3_f32 v217, v88, v89, v217\n"
        "v_max3_f32 v217, v90, v91, v217\n"
        "v_max3_f32 v217, v92, v93, v217\n"
        "v_max3_f32 v217, v94, v95, v217\n"
        "v_mov_b32 v219, v217\n"
        "s_nop 1\n"
        "v_permlane32_swap_b32 v217, v219\n"
        "v_max_f32 v217, v217, v219\n"
        "v_sub_f32 v218, v216, v217\n"
        "v_exp_f32 v218, v218\n"
        "v_mov_b32 v216, v217\n"
        "v_sub_f32 v80, v80, v217\n"
        "v_sub_f32 v81, v81, v217\n"
        "v_sub_f32 v82, v82, v217\n"
        "v_sub_f32 v83, v83, v217\n"
        "v_sub_f32 v84, v84, v217\n"
        "v_sub_f32 v85, v85, v217\n"
        "v_sub_f32 v86, v86, v217\n"
        "v_sub_f32 v87, v87, v217\n"
        "v_sub_f32 v88, v88, v217\n"
        "v_sub_f32 v89, v89, v217\n"
        "v_sub_f32 v90, v90, v217\n"
        "v_sub_f32 v91, v91, v217\n"
        "v_sub_f32 v92, v92, v217\n"
        "v_sub_f32 v93, v93, v217\n"
        "v_sub_f32 v94, v94, v217\n"
        "v_sub_f32 v95, v95, v217\n"
        "v_exp_f32 v80, v80\n"
        "v_exp_f32 v81, v81\n"
        "v_exp_f32 v82, v82\n"
        "v_exp_f32 v83, v83\n"
        "v_exp_f32 v84, v84\n"
        "v_exp_f32 v85, v85\n"
        "v_exp_f32 v86, v86\n"
        "v_exp_f32 v87, v87\n"
        "v_exp_f32 v88, v88\n"
        "v_exp_f32 v89, v89\n"
        "v_exp_f32 v90, v90\n"
        "v_exp_f32 v91, v91\n"
        "v_exp_f32 v92, v92\n"
        "v_exp_f32 v93, v93\n"
        "v_exp_f32 v94, v94\n"
        "v_exp_f32 v95, v95\n"
        "v_mul_f32 v128, v128, v218\n"
        "v_mul_f32 v129, v129, v218\n"
        "v_mul_f32 v130, v130, v218\n"
        "v_mul_f32 v131, v131, v218\n"
        "v_mul_f32 v132, v132, v218\n"
        "v_mul_f32 v133, v133, v218\n"
        "v_mul_f32 v134, v134, v218\n"
        "v_mul_f32 v135, v135, v218\n"
        "v_mul_f32 v136, v136, v218\n"
        "v_mul_f32 v137, v137, v218\n"
        "v_mul_f32 v138, v138, v218\n"
        "v_mul_f32 v139, v139, v218\n"
        "v_mul_f32 v140, v140, v218\n"
        "v_mul_f32 v141, v141, v218\n"
        "v_mul_f32 v142, v142, v218\n"
        "v_mul_f32 v143, v143, v218\n"
        "v_cvt_pk_bf16_f32 v112, v80, v81\n"
        "v_cvt_pk_bf16_f32 v113, v82, v83\n"
        "v_cvt_pk_bf16_f32 v114, v84, v85\n"
        "v_cvt_pk_bf16_f32 v115, v86, v87\n"
        "v_cvt_pk_bf16_f32 v116, v88, v89\n"
        "v_cvt_pk_bf16_f32 v117, v90, v91\n"
        "v_cvt_pk_bf16_f32 v118, v92, v93\n"
        "v_cvt_pk_bf16_f32 v119, v94, v95\n"
        "v_mfma_f32_32x32x16_bf16 v[128:143], a[184:187], v[112:115], v[128:143]\n"
        "s_nop 0\n"
        "v_mfma_f32_32x32x16_bf16 v[128:143], a[188:191], v[116:119], v[128:143]\n"
        "v_lshlrev_b32 v219, 2, v0\n"
        "v_add_u32 v219, 0x27500, v219\n"
        "s_lshl_b32 s36, s28, 8\n"
        "v_add_u32 v217, s36, v219\n"
        "ds_write_b32 v217, v216\n"
        "s_waitcnt lgkmcnt(0)\n"
        "s_barrier\n"
        "ds_read_b32 v192, v219 offset:0\n"
        "ds_read_b32 v193, v219 offset:256\n"
        "ds_read_b32 v194, v219 offset:512\n"
        "ds_read_b32 v195, v219 offset:768\n"
        "s_waitcnt lgkmcnt(0)\n"
        "v_max3_f32 v217, v192, v193, v194\n"
        "v_max_f32 v217, v217, v195\n"
        "v_sub_f32 v218, v216, v217\n"
        "v_exp_f32 v218, v218\n"
        "s_nop 0\n"
        "v_mul_f32 v128, v128, v218\n"
        "v_mul_f32 v129, v129, v218\n"
        "v_mul_f32 v130, v130, v218\n"
        "v_mul_f32 v131, v131, v218\n"
        "v_mul_f32 v132, v132, v218\n"
        "v_mul_f32 v133, v133, v218\n"
        "v_mul_f32 v134, v134, v218\n"
        "v_mul_f32 v135, v135, v218\n"
        "v_mul_f32 v136, v136, v218\n"
        "v_mul_f32 v137, v137, v218\n"
        "v_mul_f32 v138, v138, v218\n"
        "v_mul_f32 v139, v139, v218\n"
        "v_mul_f32 v140, v140, v218\n"
        "v_mul_f32 v141, v141, v218\n"
        "v_mul_f32 v142, v142, v218\n"
        "v_mul_f32 v143, v143, v218\n"
        "ds_write_b128 v5, v[132:135]\n"
        "ds_write_b128 v6, v[136:139]\n"
        "ds_write_b128 v7, v[140:143]\n"
        "s_waitcnt lgkmcnt(0)\n"
        "s_barrier\n"
        "ds_read_b128 v[192:195], v8 offset:0\n"
        "ds_read_b128 v[196:199], v8 offset:1024\n"
        "ds_read_b128 v[200:203], v8 offset:2048\n"
        "s_waitcnt lgkmcnt(0)\n"
        "v_add_f32 v212, v128, v192\n"
        "v_add_f32 v213, v129, v193\n"
        "v_add_f32 v214, v130, v194\n"
        "v_add_f32 v196, v196, v200\n"
        "v_add_f32 v197, v197, v201\n"
        "v_add_f32 v198, v198, v202\n"
        "v_add_f32 v214, v214, v198\n"
        "v_add_f32 v212, v212, v196\n"
        "v_add_f32 v213, v213, v197\n"
        "v_rcp_f32 v214, v214\n"
        "s_nop 0\n"
        "v_mul_f32 v212, v212, v214\n"
        "v_mul_f32 v213, v213, v214\n"
        "v_cvt_pk_bf16_f32 v212, v212, v213\n"
        "ds_write_b32 v9, v212\n"
        "s_waitcnt lgkmcnt(0)\n"
        "s_barrier\n"
        "ds_read_b128 v[204:207], v10\n"
        "ds_read_b128 v[208:211], v10 offset:512\n"
        "s_waitcnt vmcnt(0) lgkmcnt(0)\n"
        "v_mfma_f32_16x16x32_bf16 v[160:163], a[192:195], v[204:207], v[160:163]\n"
        "v_mfma_f32_16x16x32_bf16 v[164:167], a[196:199], v[204:207], v[164:167]\n"
        "v_mfma_f32_16x16x32_bf16 v[168:171], a[200:203], v[204:207], v[168:171]\n"
        "v_mfma_f32_16x16x32_bf16 v[172:175], a[204:207], v[204:207], v[172:175]\n"
        "v_mfma_f32_16x16x32_bf16 v[176:179], a[208:211], v[204:207], v[176:179]\n"
        "v_mfma_f32_16x16x32_bf16 v[180:183], a[212:215], v[204:207], v[180:183]\n"
        "v_mfma_f32_16x16x32_bf16 v[184:187], a[216:219], v[204:207], v[184:187]\n"
        "v_mfma_f32_16x16x32_bf16 v[188:191], a[220:223], v[204:207], v[188:191]\n"
        "v_mfma_f32_16x16x32_bf16 v[160:163], a[224:227], v[208:211], v[160:163]\n"
        "v_mfma_f32_16x16x32_bf16 v[164:167], a[228:231], v[208:211], v[164:167]\n"
        "v_mfma_f32_16x16x32_bf16 v[168:171], a[232:235], v[208:211], v[168:171]\n"
        "v_mfma_f32_16x16x32_bf16 v[172:175], a[236:239], v[208:211], v[172:175]\n"
        "v_mfma_f32_16x16x32_bf16 v[176:179], a[240:243], v[208:211], v[176:179]\n"
        "v_mfma_f32_16x16x32_bf16 v[180:183], a[244:247], v[208:211], v[180:183]\n"
        "v_mfma_f32_16x16x32_bf16 v[184:187], a[248:251], v[208:211], v[184:187]\n"
        "v_mfma_f32_16x16x32_bf16 v[188:191], a[252:255], v[208:211], v[188:191]\n"
        "s_nop 7\n"
        "s_nop 7\n"
        "s_add_u32 s57, s57, 1\n"
        "s_cmp_lt_u32 s57, 8\n"
        "s_cbranch_scc1 L_slow_q\n"
        "buffer_load_dwordx4 v[40:43], v1, s[16:19], s60 offen offset:0\n"
        "buffer_load_dwordx4 v[44:47], v1, s[16:19], s61 offen offset:0\n"
        "buffer_load_dwordx4 v[48:51], v1, s[16:19], s60 offen offset:1024\n"
        "buffer_load_dwordx4 v[52:55], v1, s[16:19], s61 offen offset:1024\n"
        "buffer_load_dwordx4 v[56:59], v1, s[16:19], s60 offen offset:2048\n"
        "buffer_load_dwordx4 v[60:63], v1, s[16:19], s61 offen offset:2048\n"
        "buffer_load_dwordx4 v[120:123], v1, s[16:19], s60 offen offset:3072\n"
        "buffer_load_dwordx4 v[124:127], v1, s[16:19], s61 offen offset:3072\n"
        "buffer_load_dwordx4 v[228:231], v24, s[16:19], s60 offen offset:0\n"
        "buffer_load_dwordx4 v[232:235], v24, s[16:19], s61 offen offset:0\n"
        "buffer_load_dwordx4 v[236:239], v24, s[16:19], s60 offen offset:1024\n"
        "buffer_load_dwordx4 v[240:243], v24, s[16:19], s61 offen offset:1024\n"
        "buffer_load_dwordx4 v[244:247], v24, s[16:19], s60 offen offset:2048\n"
        "buffer_load_dwordx4 v[248:251], v24, s[16:19], s61 offen offset:2048\n"
        "buffer_load_dwordx4 a[24:27], v1, s[16:19], s62 offen offset:0\n"
        "buffer_load_dwordx4 a[28:31], v1, s[16:19], s63 offen offset:0\n"
        "buffer_load_dwordx4 a[32:35], v1, s[16:19], s62 offen offset:1024\n"
        "buffer_load_dwordx4 a[36:39], v1, s[16:19], s63 offen offset:1024\n"
        "buffer_load_dwordx4 a[40:43], v1, s[16:19], s62 offen offset:2048\n"
        "buffer_load_dwordx4 a[44:47], v1, s[16:19], s63 offen offset:2048\n"
        "buffer_load_dwordx4 a[48:51], v1, s[16:19], s64 offen offset:0\n"
        "buffer_load_dwordx4 a[52:55], v1, s[16:19], s65 offen offset:0\n"
        "buffer_load_dwordx4 a[56:59], v1, s[16:19], s64 offen offset:1024\n"
        "buffer_load_dwordx4 a[60:63], v1, s[16:19], s65 offen offset:1024\n"
        "s_branch L_A_epi\n"
        "L_end:\n"
        :
        : "s"(ws), "s"(xin), "s"(out), "s"(blockIdx.x), "s"(w), "s"(layer)
        : "memory", "vcc", "scc", "v0", "v1", "v2", "v3", "v4", "v5", "v6", "v7", "v8", "v9", "v10", "v11", "v12", "v13", "v14", "v15", "v16", "v17", "v18", "v19", "v20", "v21", "v22", "v23", "v24", "v25", "v26", "v27", "v28", "v29", "v30", "v31", "v32", "v33", "v34", "v35", "v36", "v37", "v38", "v39", "v40", "v41", "v42", "v43", "v44", "v45", "v46", "v47", "v48", "v49", "v50", "v51", "v52", "v53", "v54", "v55", "v56", "v57", "v58", "v59", "v60", "v61", "v62", "v63", "v64", "v65", "v66", "v67", "v68", "v69", "v70", "v71", "v72", "v73", "v74", "v75", "v76", "v77", "v78", "v79", "v80", "v81", "v82", "v83", "v84", "v85", "v86", "v87", "v88", "v89", "v90", "v91", "v92", "v93", "v94", "v95", "v96", "v97", "v98", "v99", "v100", "v101", "v102", "v103", "v104", "v105", "v106", "v107", "v108", "v109", "v110", "v111", "v112", "v113", "v114", "v115", "v116", "v117", "v118", "v119", "v120", "v121", "v122", "v123", "v124", "v125", "v126", "v127", "v128", "v129", "v130", "v131", "v132", "v133", "v134", "v135", "v136", "v137", "v138", "v139", "v140", "v141", "v142", "v143", "v144", "v145", "v146", "v147", "v148", "v149", "v150", "v151", "v152", "v153", "v154", "v155", "v156", "v157", "v158", "v159", "v160", "v161", "v162", "v163", "v164", "v165", "v166", "v167", "v168", "v169", "v170", "v171", "v172", "v173", "v174", "v175", "v176", "v177", "v178", "v179", "v180", "v181", "v182", "v183", "v184", "v185", "v186", "v187", "v188", "v189", "v190", "v191", "v192", "v193", "v194", "v195", "v196", "v197", "v198", "v199", "v200", "v201", "v202", "v203", "v204", "v205", "v206", "v207", "v208", "v209", "v210", "v211", "v212", "v213", "v214", "v215", "v216", "v217", "v218", "v219", "v220", "v221", "v222", "v223", "v224", "v225", "v226", "v227", "v228", "v229", "v230", "v231", "v232", "v233", "v234", "v235", "v236", "v237", "v238", "v239", "v240", "v241", "v242", "v243", "v244", "v245", "v246", "v247", "v248", "v249", "v250", "v251", "v252", "v253", "v254", "v255", "a0", "a1", "a2", "a3", "a4", "a5", "a6", "a7", "a8", "a9", "a10", "a11", "a12", "a13", "a14", "a15", "a16", "a17", "a18", "a19", "a20", "a21", "a22", "a23", "a24", "a25", "a26", "a27", "a28", "a29", "a30", "a31", "a32", "a33", "a34", "a35", "a36", "a37", "a38", "a39", "a40", "a41", "a42", "a43", "a44", "a45", "a46", "a47", "a48", "a49", "a50", "a51", "a52", "a53", "a54", "a55", "a56", "a57", "a58", "a59", "a60", "a61", "a62", "a63", "a64", "a65", "a66", "a67", "a68", "a69", "a70", "a71", "a72", "a73", "a74", "a75", "a76", "a77", "a78", "a79", "a80", "a81", "a82", "a83", "a84", "a85", "a86", "a87", "a88", "a89", "a90", "a91", "a92", "a93", "a94", "a95", "a96", "a97", "a98", "a99", "a100", "a101", "a102", "a103", "a104", "a105", "a106", "a107", "a108", "a109", "a110", "a111", "a112", "a113", "a114", "a115", "a116", "a117", "a118", "a119", "a120", "a121", "a122", "a123", "a124", "a125", "a126", "a127", "a128", "a129", "a130", "a131", "a132", "a133", "a134", "a135", "a136", "a137", "a138", "a139", "a140", "a141", "a142", "a143", "a144", "a145", "a146", "a147", "a148", "a149", "a150", "a151", "a152", "a153", "a154", "a155", "a156", "a157", "a158", "a159", "a160", "a161", "a162", "a163", "a164", "a165", "a166", "a167", "a168", "a169", "a170", "a171", "a172", "a173", "a174", "a175", "a176", "a177", "a178", "a179", "a180", "a181", "a182", "a183", "a184", "a185", "a186", "a187", "a188", "a189", "a190", "a191", "a192", "a193", "a194", "a195", "a196", "a197", "a198", "a199", "a200", "a201", "a202", "a203", "a204", "a205", "a206", "a207", "a208", "a209", "a210", "a211", "a212", "a213", "a214", "a215", "a216", "a217", "a218", "a219", "a220", "a221", "a222", "a223", "a224", "a225", "a226", "a227", "a228", "a229", "a230", "a231", "a232", "a233", "a234", "a235", "a236", "a237", "a238", "a239", "a240", "a241", "a242", "a243", "a244", "a245", "a246", "a247", "a248", "a249", "a250", "a251", "a252", "a253", "a254", "a255", "s16", "s17", "s18", "s19", "s20", "s21", "s22", "s23", "s24", "s25", "s26", "s27", "s28", "s29", "s30", "s31", "s33", "s34", "s35", "s36", "s37", "s38", "s39", "s40", "s41", "s42", "s43", "s44", "s45", "s46", "s47", "s48", "s49", "s50", "s51", "s52", "s53", "s54", "s55", "s56", "s57", "s58", "s59", "s60", "s61", "s62", "s63", "s64", "s65", "s66", "s67", "s68", "s69", "s70", "s71", "s72", "s73", "s74", "s75", "s76", "s77", "s78", "s79", "s80", "s81", "s82", "s83", "s84", "s85", "s86", "s87", "s88", "s89", "s90", "s91", "s92", "s93", "s94", "s95", "s96", "s97", "s98", "s99"
    );
}

extern "C" void kernel_launch(void* const* d_in, const int* in_sizes, int n_in, void* d_out, int out_size, void* d_ws, size_t ws_size,
                              hipStream_t stream) {
    const u16* x = (const u16*)d_in[0];
    const u16* wq = (const u16*)d_in[1]; const u16* wk = (const u16*)d_in[2]; const u16* wv = (const u16*)d_in[3]; const u16* wo = (const u16*)d_in[4];
    const u16* w1 = (const u16*)d_in[6]; const u16* w3 = (const u16*)d_in[7]; const u16* w2 = (const u16*)d_in[8];
    char* ws = (char*)d_ws; u16* out = (u16*)d_out;
    (void)hipFuncSetAttribute((const void*)k_first, hipFuncAttributeMaxDynamicSharedMemorySize, 157696);
    (void)hipFuncSetAttribute((const void*)k_layer, hipFuncAttributeMaxDynamicSharedMemorySize, 163840);
    k_first<<<256, 256, 157696, stream>>>(ws, x, wq, wk, wv, wo, w1, w3, w2);
    k_layer<<<256, 256, 163840, stream>>>(ws, x, out, 0, 0);
    k_layer<<<256, 256, 163840, stream>>>(ws, x, out, 1, 0);
}
```
